```python
import torch

def _softmax(x, axis=-1):
    return torch.softmax(x, dim=axis)
def _gelu(x):
    return torch.nn.functional.gelu(x, approximate="tanh")
def _silu(x):
    return torch.nn.functional.silu(x)
def _sigmoid(x):
    return torch.sigmoid(x)
def _layernorm(x, axis=-1, eps=1e-5):
    m = x.mean(dim=axis, keepdim=True)
    v = x.var(dim=axis, keepdim=True, unbiased=False)
    return (x - m) / torch.sqrt(v + eps)
def _scatter_add(tgt, idx, src, axis=0):
    return tgt.clone().index_add_(axis, idx, src)
def _segment_sum(src, seg, n):
    out = torch.zeros((n,) + src.shape[1:], dtype=src.dtype)
    return out.index_add_(0, seg, src)
def _pad(x, pw):
    flat = tuple(v for pair in reversed(pw) for v in pair)
    return torch.nn.functional.pad(x, flat)

kernel_name = 'gated_mix__sliding_block__mha_block__prompt'

def setup_inputs():

    _w = (lambda a: (lambda t: t.to(torch.bfloat16) if t.is_floating_point() else t)(torch.as_tensor(a)))
    def _A_setup():
        B, S, D = 1, 2048, 1024
        return {
            "x": _w((lambda *s: torch.randn(*s, dtype=torch.bfloat16) * 0.05)(B, S, D)),
            "wqkv": _w((lambda *s: torch.randn(*s, dtype=torch.bfloat16) * 0.8)(D, 3 * D)),
            "wo": _w((lambda *s: torch.randn(*s, dtype=torch.bfloat16) * 0.05)(D, D)),
        }
    _A_in = _A_setup()

    def _B_setup():
        B, S, D, H = 1, 2048, 1024, 32
        return {
            "x": _w((lambda *s: torch.randn(*s, dtype=torch.bfloat16) * 0.05)(B, S, D)),
            "wqkv": _w((lambda *s: torch.randn(*s, dtype=torch.bfloat16) * 0.8)(D, 3 * D)),
            "wo": _w((lambda *s: torch.randn(*s, dtype=torch.bfloat16) * 0.1)(D, D)),
        }
    _B_in = _B_setup()

    return {
        "A_x": _A_in["x"],
        "A_wqkv": _A_in["wqkv"],
        "A_wo": _A_in["wo"],
        "B_x": _B_in["x"],
        "B_wqkv": _B_in["wqkv"],
        "B_wo": _B_in["wo"],
        "gate_w": _w((lambda *s: torch.randn(*s, dtype=torch.bfloat16) * 0.05)(1024) * 16.0),
    }

def reference(A_x, A_wqkv, A_wo, B_x, B_wqkv, B_wo, gate_w):
    def _A_ref(x, wqkv, wo):
        B, S, D = x.shape
        H = 32; Dh = D // H; W = 256
        qkv = (lambda x, *d: x.permute(*d))((x @ wqkv).reshape(B, S, 3, H, Dh), 2, 0, 3, 1, 4)
        q, k, v = qkv[0], qkv[1], qkv[2]
        scores = torch.einsum("bhqd,bhkd->bhqk", q, k) * (Dh ** -0.5)
        idx = torch.arange(S)
        mask = (idx[:, None] - idx[None, :] >= 0) & (idx[:, None] - idx[None, :] < W)
        scores = torch.where(mask[None, None], scores, -1e9)
        p = _softmax(scores, axis=-1)
        o = torch.einsum("bhqk,bhkd->bhqd", p, v)
        return (lambda x, *d: x.permute(*d))(o, 0, 2, 1, 3).reshape(B, S, D) @ wo

    def _B_ref(x, wqkv, wo):
        B, S, D = x.shape
        H = 32
        Dh = D // H
        qkv = (lambda x, *d: x.permute(*d))((x @ wqkv).reshape(B, S, 3, H, Dh), 2, 0, 3, 1, 4)
        q, k, v = qkv[0], qkv[1], qkv[2]
        scores = torch.einsum("bhqd,bhkd->bhqk", q, k) * (Dh ** -0.5)
        p = _softmax(scores, axis=-1)
        o = torch.einsum("bhqk,bhkd->bhqd", p, v)
        return (lambda x, *d: x.permute(*d))(o, 0, 2, 1, 3).reshape(B, S, D) @ wo

    x = A_x
    g = _sigmoid((x * gate_w).sum(-1, keepdims=True))
    a = _A_ref(x, A_wqkv, A_wo)
    b = _B_ref(x, B_wqkv, B_wo)
    return g * a + (1 - g) * b
```

```cpp
#include <hip/hip_runtime.h>
#include <hip/hip_bf16.h>
#include <stdint.h>
#pragma clang diagnostic ignored "-Winline-asm"

typedef unsigned short u16;
typedef short bf16x8 __attribute__((ext_vector_type(8)));
typedef short s16x4 __attribute__((ext_vector_type(4)));
typedef float f32x16 __attribute__((ext_vector_type(16)));
typedef float f32x4 __attribute__((ext_vector_type(4)));
typedef float f32x2 __attribute__((ext_vector_type(2)));
typedef __bf16 bf16x2_t __attribute__((ext_vector_type(2)));
typedef unsigned u32x4 __attribute__((ext_vector_type(4)));
typedef unsigned u32x2 __attribute__((ext_vector_type(2)));
#define LAS __attribute__((address_space(3)))
#define DI __device__ __forceinline__
#define MFMA32(a, b, c) __builtin_amdgcn_mfma_f32_32x32x16_bf16((a), (b), (c), 0, 0, 0)
#define MFMA16(a, b, c) __builtin_amdgcn_mfma_f32_16x16x32_bf16((a), (b), (c), 0, 0, 0)
#define SBAR() __builtin_amdgcn_sched_barrier(0)
#define PIN(x) asm volatile("" : "+v"(x))
#define WAIT_BAR(N) asm volatile("s_waitcnt vmcnt(" #N ") lgkmcnt(0)\n\ts_barrier" ::: "memory")
#define WAITV(N) asm volatile("s_waitcnt vmcnt(" #N ")" ::: "memory")

constexpr int SEQ = 2048, DM = 1024, NH = 32;
constexpr float QSCALE = 0.17677669529663687f * 1.4426950408889634f;
constexpr size_t WS_Q = 0, WS_K = 8u << 20, WS_V = 16u << 20, WS_O = 24u << 20, WS_G = 32u << 20;

DI unsigned cvtpk(float lo, float hi) { f32x2 v = {lo, hi}; bf16x2_t b = __builtin_convertvector(v, bf16x2_t); return __builtin_bit_cast(unsigned, b); }
DI bf16x8 pack8(const f32x16& x, int s, float sc) {
    u32x4 w = {cvtpk(x[8 * s] * sc, x[8 * s + 1] * sc), cvtpk(x[8 * s + 2] * sc, x[8 * s + 3] * sc), cvtpk(x[8 * s + 4] * sc, x[8 * s + 5] * sc), cvtpk(x[8 * s + 6] * sc, x[8 * s + 7] * sc)};
    return __builtin_bit_cast(bf16x8, w);
}
DI void glds16(unsigned voff, const void* sbase, unsigned lds_dst) {
    asm volatile("s_mov_b32 m0, %2\n\ts_nop 0\n\tglobal_load_lds_dwordx4 %0, %1" ::"v"(voff), "s"(sbase), "s"(lds_dst) : "memory", "m0");
}
typedef short v4i16_t __attribute__((ext_vector_type(4)));
DI s16x4 ldtr(unsigned a) { return __builtin_bit_cast(s16x4, __builtin_amdgcn_ds_read_tr16_b64_v4i16((LAS v4i16_t*)(uintptr_t)a)); }
DI bf16x8 ld128(unsigned a) { return *(const LAS bf16x8*)(uintptr_t)a; }
DI bf16x8 cat8(s16x4 lo, s16x4 hi) { return __builtin_shufflevector(lo, hi, 0, 1, 2, 3, 4, 5, 6, 7); }
DI float bf2f(u16 b) { return __uint_as_float((unsigned)b << 16); }

constexpr int K1_XSLOT = 32768, K1_WBASE = 3 * K1_XSLOT, K1_WSLOT = 12288, K1_LDS = K1_WBASE + 5 * K1_WSLOT;

__global__ __launch_bounds__(512, 2) void k_qkv(const u16* __restrict__ xA, const u16* __restrict__ wA, const u16* __restrict__ xB, const u16* __restrict__ wB,
                                                const u16* __restrict__ gw, char* __restrict__ ws) {
    extern __shared__ __attribute__((aligned(16))) char lds[];
    const unsigned lds0 = (unsigned)(uintptr_t)(LAS char*)lds;
    const int tid = threadIdx.x, lane = tid & 63;
    const int w = __builtin_amdgcn_readfirstlane(tid >> 6);
    const int bid = blockIdx.x, xcd = bid & 7, idx = bid >> 3;
    const int br = xcd >> 2, xq = xcd & 3;
    const int rt = (xq & 1) * 4 + (idx & 3), hp = (xq >> 1) * 8 + (idx >> 2);
    const char* xg = (const char*)(br ? xB : xA) + (size_t)rt * 256 * 2048;
    const char* wg = (const char*)(br ? wB : wA) + hp * 128;

    u32x4 gx0, gx1, gw0, gw1;
    {
        const char* px = (const char*)xA + (size_t)(bid * 8 + w) * 2048;
        const unsigned go = lane * 32;
        asm volatile("global_load_dwordx4 %0, %4, %5\n\tglobal_load_dwordx4 %1, %4, %5 offset:16\n\tglobal_load_dwordx4 %2, %4, %6\n\tglobal_load_dwordx4 %3, %4, %6 offset:16"
                     : "=&v"(gx0), "=&v"(gx1), "=&v"(gw0), "=&v"(gw1) : "v"(go), "s"(px), "s"(gw) : "memory");
    }
    unsigned vx[4], vw[3];
    {
        const int r8 = lane >> 3, c8 = lane & 7;
        const int gsw = (4 * w + (lane >> 4)) & 7;
#pragma unroll
        for (int i = 0; i < 4; ++i) vx[i] = (unsigned)((8 * (w + 8 * i) + r8) * 2048 + ((c8 ^ gsw) << 4));
        const int csw = c8 ^ (((lane >> 4) & 1) << 2);
#pragma unroll
        for (int s = 0; s < 3; ++s) vw[s] = (unsigned)((8 * w + r8) * 6144 + s * 2048 + (csw << 4));
    }
    const unsigned xdst = lds0 + w * 1024, wdst = lds0 + K1_WBASE + (w & 3) * 1024;
    const int whalf = w >> 2;
#define K1_ISSUE_X(T, slot) do { const char* b_ = xg + (T) * 128; const unsigned d_ = xdst + (slot) * K1_XSLOT; \
        glds16(vx[0], b_, d_); glds16(vx[1], b_, d_ + 8192); glds16(vx[2], b_, d_ + 16384); glds16(vx[3], b_, d_ + 24576); } while (0)
#define K1_ISSUE_W(T, hs0, hs1) do { const char* b_ = wg + (size_t)(T) * (64 * 6144); const unsigned d_ = wdst + (whalf ? (hs1) : (hs0)) * K1_WSLOT; \
        glds16(vw[0], b_, d_); glds16(vw[1], b_, d_ + 4096); glds16(vw[2], b_, d_ + 8192); } while (0)
    K1_ISSUE_X(0, 0); K1_ISSUE_W(0, 0, 1); K1_ISSUE_X(1, 1); K1_ISSUE_W(1, 2, 3);

    float gate;
    {
        asm volatile("s_waitcnt vmcnt(14)" : "+v"(gx0), "+v"(gx1), "+v"(gw0), "+v"(gw1)::"memory");
        float s = 0.f;
#pragma unroll
        for (int i = 0; i < 4; ++i) {
            s += __uint_as_float(gx0[i] << 16) * __uint_as_float(gw0[i] << 16) + __uint_as_float(gx0[i] & 0xffff0000u) * __uint_as_float(gw0[i] & 0xffff0000u);
            s += __uint_as_float(gx1[i] << 16) * __uint_as_float(gw1[i] << 16) + __uint_as_float(gx1[i] & 0xffff0000u) * __uint_as_float(gw1[i] & 0xffff0000u);
        }
#pragma unroll
        for (int o = 32; o >= 1; o >>= 1) s += __shfl_xor(s, o);
        gate = 1.0f / (1.0f + __expf(-s));
    }

    const int wm = w >> 1, wn = w & 1, r31 = lane & 31, h = lane >> 5;
    unsigned xa[4];
    {
        const int row = wm * 64 + r31, gx = (r31 >> 1) & 7;
#pragma unroll
        for (int s = 0; s < 4; ++s) xa[s] = lds0 + row * 128 + (((2 * s + h) ^ gx) << 4);
    }
    const int q4 = (lane & 15) >> 2, p4 = lane & 3, blk = (lane >> 4) & 1;
    const unsigned wa = lds0 + K1_WBASE + (8 * h + q4) * 128 + ((wn ^ (q4 >> 1)) * 64) + blk * 32 + p4 * 8;

    f32x16 acc[2][3];
#pragma unroll
    for (int i = 0; i < 2; ++i)
#pragma unroll
        for (int j = 0; j < 3; ++j) acc[i][j] = f32x16{};
    bf16x8 xf[2][2], wf[2][3];
#define K1_RD(buf, xo, s, wo, sp) do { \
        const unsigned xa_ = xa[s] + (xo); const unsigned wa_ = wa + (wo) + (sp) * 2048; \
        xf[buf][0] = ld128(xa_); xf[buf][1] = ld128(xa_ + 4096); \
        _Pragma("unroll") for (int t_ = 0; t_ < 3; ++t_) wf[buf][t_] = cat8(ldtr(wa_ + t_ * 4096), ldtr(wa_ + t_ * 4096 + 512)); } while (0)
#define K1_MMA(buf) do { \
        _Pragma("unroll") for (int rb_ = 0; rb_ < 2; ++rb_) { \
            acc[rb_][0] = MFMA32(wf[buf][0], xf[buf][rb_], acc[rb_][0]); \
            acc[rb_][1] = MFMA32(wf[buf][1], xf[buf][rb_], acc[rb_][1]); \
            acc[rb_][2] = MFMA32(xf[buf][rb_], wf[buf][2], acc[rb_][2]); } } while (0)

    WAIT_BAR(7);
    K1_RD(0, 0, 0, 0, 0);
    int xs = 0;
    int ws0 = 0;
    for (int T = 0; T < 16; ++T) {
        const int xs1 = xs == 2 ? 0 : xs + 1, xs2 = xs1 == 2 ? 0 : xs1 + 1;
        const int ws1 = ws0 == 4 ? 0 : ws0 + 1, ws2 = ws1 == 4 ? 0 : ws1 + 1;
        const int ws4 = ws0 == 0 ? 4 : ws0 - 1;
        const unsigned xo = xs * K1_XSLOT, xo1 = xs1 * K1_XSLOT;
        if (T > 0) { if (T < 15) WAIT_BAR(7); else WAIT_BAR(0); }
        if (T < 14) K1_ISSUE_X(T + 2, xs2);
        K1_RD(1, xo, 1, ws0 * K1_WSLOT, 1); K1_MMA(0);
        K1_RD(0, xo, 2, ws1 * K1_WSLOT, 0); K1_MMA(1);
        if (T < 14) WAIT_BAR(4); else WAIT_BAR(0);
        if (T < 14) K1_ISSUE_W(T + 2, ws4, ws0);
        K1_RD(1, xo, 3, ws1 * K1_WSLOT, 1); K1_MMA(0);
        if (T < 15) K1_RD(0, xo1, 0, ws2 * K1_WSLOT, 0);
        K1_MMA(1);
        xs = xs1; ws0 = ws2;
    }
    const int head = 2 * hp + wn;
#pragma unroll
    for (int rb = 0; rb < 2; ++rb) {
        const int RB = rt * 8 + wm * 2 + rb;
        const size_t off = ((size_t)((br * 32 + head) * 64 + RB) * 2) * 1024 + lane * 16;
#pragma unroll
        for (int s = 0; s < 2; ++s) {
            *(bf16x8*)(ws + WS_Q + off + s * 1024) = pack8(acc[rb][0], s, QSCALE);
            *(bf16x8*)(ws + WS_K + off + s * 1024) = pack8(acc[rb][1], s, 1.0f);
            *(bf16x8*)(ws + WS_V + off + s * 1024) = pack8(acc[rb][2], s, 1.0f);
        }
    }
    if (lane == 0) ((float*)(ws + WS_G))[bid * 8 + w] = gate;
#undef K1_ISSUE_X
#undef K1_ISSUE_W
#undef K1_RD
#undef K1_MMA
}

constexpr int K2_ABASE = 0, K2_BBASE = 65536, K2_SLOT = 32768, K2_LDS = K2_BBASE + 3 * K2_SLOT;

DI float xhalf_max(float m) { auto rr = __builtin_amdgcn_permlane32_swap(__float_as_uint(m), __float_as_uint(m), false, false); return fmaxf(__uint_as_float(rr[0]), __uint_as_float(rr[1])); }
DI float xhalf_sum(float m) { auto rr = __builtin_amdgcn_permlane32_swap(__float_as_uint(m), __float_as_uint(m), false, false); return __uint_as_float(rr[0]) + __uint_as_float(rr[1]); }
DI int crow(int r, int h) { return (r & 3) + 8 * (r >> 2) + 4 * h; }
DI float rowmax16(const f32x16& s) {
    float a = fmaxf(fmaxf(s[0], s[1]), s[2]), b = fmaxf(fmaxf(s[3], s[4]), s[5]);
#pragma unroll
    for (int r = 6; r < 16; r += 2) a = fmaxf(fmaxf(a, s[r]), s[r + 1]);
    return xhalf_max(fmaxf(a, b));
}
DI bf16x8 packp(const f32x16& p, int s) {
    u32x4 w = {cvtpk(p[8 * s], p[8 * s + 1]), cvtpk(p[8 * s + 2], p[8 * s + 3]), cvtpk(p[8 * s + 4], p[8 * s + 5]), cvtpk(p[8 * s + 6], p[8 * s + 7])};
    return __builtin_bit_cast(bf16x8, w);
}
template <bool WIN>
DI void attn_safe(const char* Kg, const char* Vg, bf16x8 q0, bf16x8 q1, int rb, int lane, f32x16& Oout, float& lout) {
    const int ql = lane & 31, h = lane >> 5;
    float m = -INFINITY, l = 0.f; f32x16 O = {};
    const int nb = WIN ? (rb < 8 ? rb + 1 : 9) : 64;
    for (int j = 0; j < nb; ++j) {
        const int kb = WIN ? rb - j : j;
        const bf16x8 k0 = *(const bf16x8*)(Kg + kb * 2048 + lane * 16), k1 = *(const bf16x8*)(Kg + kb * 2048 + 1024 + lane * 16);
        const bf16x8 v0 = *(const bf16x8*)(Vg + kb * 2048 + lane * 16), v1 = *(const bf16x8*)(Vg + kb * 2048 + 1024 + lane * 16);
        f32x16 s = {}; s = MFMA32(k0, q0, s); s = MFMA32(k1, q1, s);
        if (WIN) {
#pragma unroll
            for (int r = 0; r < 16; ++r) { const int kl = crow(r, h); if ((j == 0 && kl > ql) || (j == 8 && kl <= ql)) s[r] = -INFINITY; }
        }
        const float mn = fmaxf(m, rowmax16(s));
        const float al = __builtin_amdgcn_exp2f(m - mn); m = mn;
        float ps = 0.f;
#pragma unroll
        for (int r = 0; r < 16; ++r) { s[r] = __builtin_amdgcn_exp2f(s[r] - mn); }
        const bf16x8 p0 = packp(s, 0), p1 = packp(s, 1);
#pragma unroll
        for (int r = 0; r < 8; ++r) { ps += __uint_as_float((unsigned)(u16)p0[r] << 16) + __uint_as_float((unsigned)(u16)p1[r] << 16); }
        l = l * al + ps;
#pragma unroll
        for (int r = 0; r < 16; ++r) O[r] *= al;
        O = MFMA32(v0, p0, O); O = MFMA32(v1, p1, O);
    }
    Oout = O; lout = xhalf_sum(l);
}
DI bool unit_ok(const f32x16& O, float l) {
    bool ok = l <= 1.0995116e12f;
    float a = 0.f;
#pragma unroll
    for (int r = 0; r < 16; ++r) a = fmaxf(a, __builtin_fabsf(O[r]));
    bool okO = a <= 3.0e38f;
#pragma unroll
    for (int r = 0; r < 16; ++r) okO = okO && (O[r] == O[r]);
    return __all(ok && okO);
}

__global__ __launch_bounds__(512, 2) void k_attn(char* __restrict__ ws) {
    extern __shared__ __attribute__((aligned(16))) char lds[];
    const unsigned lds0 = (unsigned)(uintptr_t)(LAS char*)lds;
    const int tid = threadIdx.x, lane = tid & 63;
    const int w = __builtin_amdgcn_readfirstlane(tid >> 6);
    const int bid = blockIdx.x, xcd = bid & 7, idx = bid >> 3;
    const int head = xcd * 4 + (idx >> 3), qb = idx & 7;
    const int rb = qb * 8 + w;
    const char* KA = ws + WS_K + (size_t)head * 131072; const char* VA = ws + WS_V + (size_t)head * 131072;
    const char* KB = KA + (size_t)32 * 131072; const char* VB = VA + (size_t)32 * 131072;
    const char* QA = ws + WS_Q + (size_t)head * 131072 + rb * 2048; const char* QB = QA + (size_t)32 * 131072;
    const unsigned l16 = lane * 16;

    bf16x8 qa0, qa1, qb0, qb1; float gate;
    {
        const float* gp = (const float*)(ws + WS_G) + rb * 32;
        const unsigned go = (lane & 31) * 4;
        asm volatile("global_load_dwordx4 %0, %5, %6\n\tglobal_load_dwordx4 %1, %5, %6 offset:1024\n\tglobal_load_dwordx4 %2, %5, %7\n\tglobal_load_dwordx4 %3, %5, %7 offset:1024\n\tglobal_load_dword %4, %8, %9"
                     : "=&v"(qa0), "=&v"(qa1), "=&v"(qb0), "=&v"(qb1), "=&v"(gate) : "v"(l16), "s"(QA), "s"(QB), "v"(go), "s"(gp) : "memory");
    }
    const bool a_load = (qb > 0) || (w >= 4);
    if (a_load) {
        const char* ks = KA + (ptrdiff_t)(qb * 8 - 8 + 2 * w) * 2048; const char* vs = VA + (ptrdiff_t)(qb * 8 - 8 + 2 * w) * 2048;
        const unsigned d = lds0 + K2_ABASE + w * 4096;
#pragma unroll
        for (int i = 0; i < 4; ++i) glds16(l16, ks + i * 1024, d + i * 1024);
#pragma unroll
        for (int i = 0; i < 4; ++i) glds16(l16, vs + i * 1024, d + 32768 + i * 1024);
    }
#define K2_ISSUE(c, slot) do { const char* ks_ = KB + ((c) * 8 + w) * 2048; const char* vs_ = VB + ((c) * 8 + w) * 2048; const unsigned d_ = lds0 + K2_BBASE + (slot) * K2_SLOT + w * 2048; \
        glds16(l16, ks_, d_); glds16(l16, ks_ + 1024, d_ + 1024); glds16(l16, vs_, d_ + 16384); glds16(l16, vs_ + 1024, d_ + 16384 + 1024); } while (0)
    K2_ISSUE(0, 0); K2_ISSUE(1, 1);

    bf16x8 sel;
    { const short o = (((lane >> 4) & 1) == ((lane >> 2) & 1)) ? (short)0x3F80 : (short)0;
#pragma unroll
      for (int i = 0; i < 8; ++i) sel[i] = o; }
    const int ql = lane & 31, h = lane >> 5;

    asm volatile("s_waitcnt vmcnt(8) lgkmcnt(0)\n\ts_barrier" : "+v"(qa0), "+v"(qa1), "+v"(qb0), "+v"(qb1), "+v"(gate)::"memory");
    {
        f32x16 O = {}, negm; f32x4 L = {};
        const int nb = rb < 8 ? rb + 1 : 9;
        const unsigned ab = lds0 + K2_ABASE + (w + 8) * 2048 + l16;
        for (int j = 0; j < nb; ++j) {
            const unsigned a = ab - j * 2048;
            const bf16x8 k0 = ld128(a), k1 = ld128(a + 1024), v0 = ld128(a + 32768), v1 = ld128(a + 32768 + 1024);
            f32x16 s;
            if (j == 0) {
                s = f32x16{}; s = MFMA32(k0, qa0, s); s = MFMA32(k1, qa1, s);
#pragma unroll
                for (int r = 0; r < 16; ++r) if (crow(r, h) > ql) s[r] = -INFINITY;
                const float m = rowmax16(s);
#pragma unroll
                for (int r = 0; r < 16; ++r) { negm[r] = -m; s[r] -= m; }
            } else {
                s = MFMA32(k0, qa0, negm); s = MFMA32(k1, qa1, s);
                if (j == 8) {
#pragma unroll
                    for (int r = 0; r < 16; ++r) if (crow(r, h) <= ql) s[r] = -INFINITY;
                }
            }
#pragma unroll
            for (int r = 0; r < 16; ++r) s[r] = __builtin_amdgcn_exp2f(s[r]);
            const bf16x8 p0 = packp(s, 0), p1 = packp(s, 1);
            O = MFMA32(v0, p0, O); L = MFMA16(sel, p0, L);
            O = MFMA32(v1, p1, O); L = MFMA16(sel, p1, L);
        }
        float l = L[0];
        if (!unit_ok(O, l)) attn_safe<true>(KA, VA, qa0, qa1, rb, lane, O, l);
        const float f = gate / l;
        char* op = ws + WS_O + ((size_t)rb * 128 + head * 2) * 1024 + l16;
        *(bf16x8*)op = pack8(O, 0, f); *(bf16x8*)(op + 1024) = pack8(O, 1, f);
    }
    {
        f32x16 O = {}, negm, SA, SB; f32x4 L = {};
        bf16x8 k0, k1, v0, v1; u32x4 plo, phi;
        WAIT_BAR(2);
        unsigned cur = lds0 + K2_BBASE + l16, nxt = cur + K2_SLOT;
        {
            k0 = ld128(cur); k1 = ld128(cur + 1024);
            SA = f32x16{}; SA = MFMA32(k0, qb0, SA); SA = MFMA32(k1, qb1, SA);
            const float m = rowmax16(SA);
#pragma unroll
            for (int r = 0; r < 16; ++r) { negm[r] = -m; SA[r] -= m; }
            k0 = ld128(cur + 2048); k1 = ld128(cur + 2048 + 1024);
            v0 = ld128(cur + 16384);
            v1 = bf16x8{}; phi = u32x4{}; plo = u32x4{};
        }
#define EX(x) __builtin_amdgcn_exp2f(x)
#define P8(x) __builtin_bit_cast(bf16x8, x)
#define KADDR(j, s) (((j) < 8 ? cur + (j) * 2048 : nxt + ((j) - 8) * 2048) + (s) * 1024)
#define VADDR(j, s) (KADDR(j, s) + 16384)
#define STEP(SC, SN, j) do { \
        SN = MFMA32(k0, qb0, negm); k0 = ld128(KADDR((j) + 2, 0)); \
        SC[0] = EX(SC[0]); SC[1] = EX(SC[1]); SC[2] = EX(SC[2]); SC[3] = EX(SC[3]); plo[0] = cvtpk(SC[0], SC[1]); plo[1] = cvtpk(SC[2], SC[3]); PIN(plo); SBAR(); \
        O = MFMA32(v1, P8(phi), O); L = MFMA16(sel, P8(phi), L); v1 = ld128(VADDR(j, 1)); \
        SC[4] = EX(SC[4]); SC[5] = EX(SC[5]); SC[6] = EX(SC[6]); SC[7] = EX(SC[7]); plo[2] = cvtpk(SC[4], SC[5]); plo[3] = cvtpk(SC[6], SC[7]); PIN(plo); SBAR(); \
        SN = MFMA32(k1, qb1, SN); k1 = ld128(KADDR((j) + 2, 1)); \
        SC[8] = EX(SC[8]); SC[9] = EX(SC[9]); SC[10] = EX(SC[10]); SC[11] = EX(SC[11]); phi[0] = cvtpk(SC[8], SC[9]); phi[1] = cvtpk(SC[10], SC[11]); PIN(phi); SBAR(); \
        O = MFMA32(v0, P8(plo), O); L = MFMA16(sel, P8(plo), L); v0 = ld128(VADDR((j) + 1, 0)); \
        SC[12] = EX(SC[12]); SC[13] = EX(SC[13]); SC[14] = EX(SC[14]); SC[15] = EX(SC[15]); phi[2] = cvtpk(SC[12], SC[13]); phi[3] = cvtpk(SC[14], SC[15]); PIN(phi); SBAR(); \
    } while (0)
        int slot = 0;
        for (int c = 0; c < 8; ++c) {
            const int s2 = slot == 0 ? 2 : slot - 1;
            if (c < 6) K2_ISSUE(c + 2, s2);
            STEP(SA, SB, 0); STEP(SB, SA, 1); STEP(SA, SB, 2); STEP(SB, SA, 3);
            STEP(SA, SB, 4); STEP(SB, SA, 5); STEP(SA, SB, 6); STEP(SB, SA, 7);
            WAIT_BAR(0);
            slot = slot == 2 ? 0 : slot + 1;
            cur = nxt; nxt = lds0 + K2_BBASE + (slot == 2 ? 0 : slot + 1) * K2_SLOT + l16;
        }
        O = MFMA32(v1, P8(phi), O); L = MFMA16(sel, P8(phi), L);
        float l = L[0];
        if (!unit_ok(O, l)) attn_safe<false>(KB, VB, qb0, qb1, rb, lane, O, l);
        const float f = (1.0f - gate) / l;
        char* op = ws + WS_O + ((size_t)rb * 128 + 64 + head * 2) * 1024 + l16;
        *(bf16x8*)op = pack8(O, 0, f); *(bf16x8*)(op + 1024) = pack8(O, 1, f);
#undef STEP
#undef KADDR
#undef VADDR
#undef EX
#undef P8
    }
#undef K2_ISSUE
}

constexpr int K3_SLOT = 16384, K3_NS = 8, K3_LDS = K3_SLOT * K3_NS;

__global__ __launch_bounds__(512, 2) void k_out(const u16* __restrict__ woA, const u16* __restrict__ woB, const char* __restrict__ ws, u16* __restrict__ out) {
    extern __shared__ __attribute__((aligned(16))) char lds[];
    const unsigned lds0 = (unsigned)(uintptr_t)(LAS char*)lds;
    const int tid = threadIdx.x, lane = tid & 63;
    const int w = __builtin_amdgcn_readfirstlane(tid >> 6);
    const int bid = blockIdx.x, xcd = bid & 7, idx = bid >> 3;
    const int rt = (xcd & 3) * 4 + (idx & 3), ct = (xcd >> 2) * 8 + (idx >> 2);
    const int p = w & 3, half = w >> 2;
    const char* wsrc = (const char*)(half ? woB : woA) + ct * 128;
    const unsigned vw0 = (unsigned)((16 * p + (lane >> 3)) * 2048 + (((lane & 7) ^ (((lane >> 4) & 1) << 2)) << 4)), vw1 = vw0 + 8 * 2048;
    const unsigned wdst = lds0 + half * 8192 + p * 2048;
#define K3_ISSUE_W(st, slot) do { const char* b_ = wsrc + (size_t)(st) * (64 * 2048); glds16(vw0, b_, wdst + (slot) * K3_SLOT); glds16(vw1, b_, wdst + (slot) * K3_SLOT + 1024); } while (0)
    const char* osrc = ws + WS_O + ((size_t)(rt * 4 + p) * 128 + half * 64) * 1024;
    const unsigned l16 = lane * 16;
    bf16x8 of[4][4];
#define K3_ISSUE_O(st, b) do { const char* b_ = osrc + (st) * 4096; \
        asm volatile("global_load_dwordx4 %0, %4, %5\n\tglobal_load_dwordx4 %1, %4, %5 offset:1024\n\tglobal_load_dwordx4 %2, %4, %5 offset:2048\n\tglobal_load_dwordx4 %3, %4, %5 offset:3072" \
                     : "=&v"(of[b][0]), "=&v"(of[b][1]), "=&v"(of[b][2]), "=&v"(of[b][3]) : "v"(l16), "s"(b_) : "memory"); } while (0)
    K3_ISSUE_W(0, 0); K3_ISSUE_W(1, 1); K3_ISSUE_W(2, 2);
    K3_ISSUE_W(3, 3); K3_ISSUE_O(0, 0);
    K3_ISSUE_W(4, 4); K3_ISSUE_O(1, 1);
    K3_ISSUE_W(5, 5); K3_ISSUE_O(2, 2);
    const int h = lane >> 5, q4 = (lane & 15) >> 2, p4 = lane & 3, blk = (lane >> 4) & 1;
    unsigned wa[2];
#pragma unroll
    for (int c = 0; c < 2; ++c) wa[c] = lds0 + half * 8192 + (4 * h + q4) * 128 + ((c ^ (q4 >> 1)) * 64) + blk * 32 + p4 * 8;
    f32x16 acc[2] = {f32x16{}, f32x16{}};
#define K3_STAGE(st, b, NW) do { \
        asm volatile("s_waitcnt vmcnt(" #NW ") lgkmcnt(0)\n\ts_barrier" : "+v"(of[b][0]), "+v"(of[b][1]), "+v"(of[b][2]), "+v"(of[b][3])::"memory"); \
        if ((st) + 6 < 16) K3_ISSUE_W((st) + 6, ((st) + 6) & 7); \
        const unsigned so_ = ((st) & 7) * K3_SLOT; \
        _Pragma("unroll") for (int s_ = 0; s_ < 4; ++s_) { \
            _Pragma("unroll") for (int c_ = 0; c_ < 2; ++c_) { \
                const bf16x8 wf_ = cat8(ldtr(wa[c_] + so_ + s_ * 2048), ldtr(wa[c_] + so_ + s_ * 2048 + 1024)); \
                acc[c_] = MFMA32(wf_, of[b][s_], acc[c_]); } } \
        SBAR(); \
        if ((st) + 3 < 16) K3_ISSUE_O((st) + 3, ((b) + 3) & 3); \
    } while (0)
    K3_STAGE(0, 0, 12); K3_STAGE(1, 1, 12); K3_STAGE(2, 2, 12); K3_STAGE(3, 3, 12);
    K3_STAGE(4, 0, 12); K3_STAGE(5, 1, 12); K3_STAGE(6, 2, 12); K3_STAGE(7, 3, 12);
    K3_STAGE(8, 0, 12); K3_STAGE(9, 1, 12); K3_STAGE(10, 2, 12); K3_STAGE(11, 3, 10);
    K3_STAGE(12, 0, 8); K3_STAGE(13, 1, 8); K3_STAGE(14, 2, 4); K3_STAGE(15, 3, 0);
    asm volatile("s_waitcnt lgkmcnt(0)\n\ts_barrier" ::: "memory");
    {
        LAS f32x4* mine = (LAS f32x4*)(uintptr_t)(lds0 + w * 4096 + lane * 16);
        LAS f32x4* theirs = (LAS f32x4*)(uintptr_t)(lds0 + (w ^ 4) * 4096 + lane * 16);
        const f32x16 snd = half ? acc[0] : acc[1];
#pragma unroll
        for (int i = 0; i < 4; ++i) mine[i * 64] = f32x4{snd[4 * i], snd[4 * i + 1], snd[4 * i + 2], snd[4 * i + 3]};
        asm volatile("s_waitcnt lgkmcnt(0)\n\ts_barrier" ::: "memory");
        f32x16 r = half ? acc[1] : acc[0];
#pragma unroll
        for (int i = 0; i < 4; ++i) { const f32x4 t = theirs[i * 64]; r[4 * i] += t[0]; r[4 * i + 1] += t[1]; r[4 * i + 2] += t[2]; r[4 * i + 3] += t[3]; }
        unsigned pk[8];
#pragma unroll
        for (int i = 0; i < 8; ++i) pk[i] = cvtpk(r[2 * i], r[2 * i + 1]);
        char* orow = (char*)out + (size_t)(rt * 128 + p * 32 + (lane & 31)) * 2048 + (ct * 64 + half * 32) * 2 + h * 16;
#pragma unroll
        for (int g = 0; g < 4; g += 2) {
            auto r0 = __builtin_amdgcn_permlane32_swap(pk[2 * g], pk[2 * g + 2], false, false);
            auto r1 = __builtin_amdgcn_permlane32_swap(pk[2 * g + 1], pk[2 * g + 3], false, false);
            *(u32x4*)(orow + g * 16) = u32x4{r0[0], r1[0], r0[1], r1[1]};
        }
    }
#undef K3_ISSUE_W
#undef K3_ISSUE_O
#undef K3_STAGE
}

extern "C" void kernel_launch(void* const* d_in, const int* in_sizes, int n_in, void* d_out, int out_size, void* d_ws, size_t ws_size, hipStream_t stream) {
    (void)in_sizes; (void)n_in; (void)out_size; (void)ws_size;
    const u16* xA = (const u16*)d_in[0]; const u16* wqkvA = (const u16*)d_in[1]; const u16* woA = (const u16*)d_in[2];
    const u16* xB = xA;    const u16* wqkvB = (const u16*)d_in[4]; const u16* woB = (const u16*)d_in[5];
    const u16* gw = (const u16*)d_in[6];
    (void)hipFuncSetAttribute((const void*)k_qkv, hipFuncAttributeMaxDynamicSharedMemorySize, K1_LDS);
    (void)hipFuncSetAttribute((const void*)k_attn, hipFuncAttributeMaxDynamicSharedMemorySize, K2_LDS);
    (void)hipFuncSetAttribute((const void*)k_out, hipFuncAttributeMaxDynamicSharedMemorySize, K3_LDS);
    k_qkv<<<256, 512, K1_LDS, stream>>>(xA, wqkvA, xB, wqkvB, gw, (char*)d_ws);
    k_attn<<<256, 512, K2_LDS, stream>>>((char*)d_ws);
    k_out<<<256, 512, K3_LDS, stream>>>(woA, woB, (const char*)d_ws, (u16*)d_out);
}
```

```cpp
#include <hip/hip_runtime.h>
#include <hip/hip_bf16.h>
#include <stdint.h>
#pragma clang diagnostic ignored "-Winline-asm"

typedef unsigned short u16;
typedef short bf16x8 __attribute__((ext_vector_type(8)));
typedef short s16x4 __attribute__((ext_vector_type(4)));
typedef float f32x16 __attribute__((ext_vector_type(16)));
typedef float f32x4 __attribute__((ext_vector_type(4)));
typedef float f32x2 __attribute__((ext_vector_type(2)));
typedef __bf16 bf16x2_t __attribute__((ext_vector_type(2)));
typedef unsigned u32x4 __attribute__((ext_vector_type(4)));
typedef unsigned u32x2 __attribute__((ext_vector_type(2)));
#define LAS __attribute__((address_space(3)))
#define GAS __attribute__((address_space(1)))
#define DI __device__ __forceinline__
#define MFMA32(a, b, c) __builtin_amdgcn_mfma_f32_32x32x16_bf16((a), (b), (c), 0, 0, 0)
#define MFMA16(a, b, c) __builtin_amdgcn_mfma_f32_16x16x32_bf16((a), (b), (c), 0, 0, 0)
#define SBAR() __builtin_amdgcn_sched_barrier(0)
#define PIN(x) asm volatile("" : "+v"(x))
#define WAIT_BAR(N) asm volatile("s_waitcnt vmcnt(" #N ") lgkmcnt(0)\n\ts_barrier" ::: "memory")
#define LBAR() asm volatile("s_waitcnt lgkmcnt(0)\n\ts_barrier" ::: "memory")
#define NOP_ do {} while (0)

constexpr float QSCALE = 0.17677669529663687f * 1.4426950408889634f;
constexpr size_t WS_KV = 0;
constexpr size_t WS_O = 16u << 20;
constexpr size_t WS_G = 24u << 20;
constexpr size_t WS_F = (24u << 20) + 8192;
constexpr int LDS_BYTES = 163840;

DI unsigned cvtpk(float lo, float hi) { f32x2 v = {lo, hi}; bf16x2_t b = __builtin_convertvector(v, bf16x2_t); return __builtin_bit_cast(unsigned, b); }
DI bf16x8 pack8(const f32x16& x, int s, float sc) {
    u32x4 w = {cvtpk(x[8 * s] * sc, x[8 * s + 1] * sc), cvtpk(x[8 * s + 2] * sc, x[8 * s + 3] * sc), cvtpk(x[8 * s + 4] * sc, x[8 * s + 5] * sc), cvtpk(x[8 * s + 6] * sc, x[8 * s + 7] * sc)};
    return __builtin_bit_cast(bf16x8, w);
}
DI u32x4 pack44(const f32x4& a, const f32x4& b, float sc) { return u32x4{cvtpk(a[0] * sc, a[1] * sc), cvtpk(a[2] * sc, a[3] * sc), cvtpk(b[0] * sc, b[1] * sc), cvtpk(b[2] * sc, b[3] * sc)}; }
DI void glds16(unsigned voff, const void* sbase, unsigned lds_dst) {
    asm volatile("s_mov_b32 m0, %2\n\ts_nop 0\n\tglobal_load_lds_dwordx4 %0, %1" ::"v"(voff), "s"(sbase), "s"(lds_dst) : "memory", "m0");
}
DI void st16_wt(unsigned voff, void* sbase, u32x4 v) { asm volatile("global_store_dwordx4 %0, %1, %2 sc1\n\ts_nop 1" ::"v"(voff), "v"(v), "s"(sbase) : "memory"); }
typedef short v4i16_t __attribute__((ext_vector_type(4)));
DI s16x4 ldtr(unsigned a) { return __builtin_bit_cast(s16x4, __builtin_amdgcn_ds_read_tr16_b64_v4i16((LAS v4i16_t*)(uintptr_t)a)); }
DI bf16x8 ld128(unsigned a) { return *(const LAS bf16x8*)(uintptr_t)a; }
DI void st128(unsigned a, u32x4 v) { *(LAS u32x4*)(uintptr_t)a = v; }
DI bf16x8 cat8(s16x4 lo, s16x4 hi) { return __builtin_shufflevector(lo, hi, 0, 1, 2, 3, 4, 5, 6, 7); }

#define STAMP_DECL do {} while (0)
#define STAMP_AT(i) do {} while (0)
#define STAMP_FLUSH(wsp, n) do {} while (0)

DI void flag_set(unsigned* f, unsigned v) { __hip_atomic_store((GAS unsigned*)f, v, __ATOMIC_RELAXED, __HIP_MEMORY_SCOPE_AGENT); }
DI void poll_acquire(const unsigned* f, int n, unsigned want, int lane) {
    const unsigned long long t0 = __builtin_amdgcn_s_memrealtime();
    for (;;) {
        unsigned v = want;
        if (lane < n) v = __hip_atomic_load((GAS unsigned*)(f + lane), __ATOMIC_RELAXED, __HIP_MEMORY_SCOPE_AGENT);
        if (__all(v == want)) break;
        if (__builtin_amdgcn_s_memrealtime() - t0 > 400000ull) break;
        __builtin_amdgcn_s_sleep(1);
    }
    __builtin_amdgcn_fence(__ATOMIC_ACQUIRE, "agent");
    asm volatile("s_waitcnt vmcnt(0)" ::: "memory");
}

DI float xhalf_max(float m) { auto rr = __builtin_amdgcn_permlane32_swap(__float_as_uint(m), __float_as_uint(m), false, false); return fmaxf(__uint_as_float(rr[0]), __uint_as_float(rr[1])); }
DI float xhalf_sum(float m) { auto rr = __builtin_amdgcn_permlane32_swap(__float_as_uint(m), __float_as_uint(m), false, false); return __uint_as_float(rr[0]) + __uint_as_float(rr[1]); }
DI int keyof(int r, int h) { return 16 * ((r >> 2) & 1) + 8 * (r >> 3) + 4 * h + (r & 3); }
DI float rowmax16(const f32x16& s) {
    float a = fmaxf(fmaxf(s[0], s[1]), s[2]), b = fmaxf(fmaxf(s[3], s[4]), s[5]);
#pragma unroll
    for (int r = 6; r < 16; r += 2) a = fmaxf(fmaxf(a, s[r]), s[r + 1]);
    return xhalf_max(fmaxf(a, b));
}
DI bf16x8 packp(const f32x16& p, int s) {
    u32x4 w = {cvtpk(p[8 * s], p[8 * s + 1]), cvtpk(p[8 * s + 2], p[8 * s + 3]), cvtpk(p[8 * s + 4], p[8 * s + 5]), cvtpk(p[8 * s + 6], p[8 * s + 7])};
    return __builtin_bit_cast(bf16x8, w);
}
template <bool WIN>
DI void attn_safe(const char* kv, bf16x8 q0, bf16x8 q1, int rb, int lane, f32x16& Oout, float& lout) {
    const int ql = lane & 31, h = lane >> 5;
    float m = -INFINITY, l = 0.f; f32x16 O = {};
    const int nb = WIN ? (rb < 8 ? rb + 1 : 9) : 64;
    for (int j = 0; j < nb; ++j) {
        const int kb = WIN ? rb - j : j;
        const char* kp = kv + (kb >> 3) * 32768 + (kb & 7) * 2048 + lane * 16;
        const bf16x8 k0 = *(const bf16x8*)kp, k1 = *(const bf16x8*)(kp + 1024), v0 = *(const bf16x8*)(kp + 16384), v1 = *(const bf16x8*)(kp + 16384 + 1024);
        f32x16 s = {}; s = MFMA32(k0, q0, s); s = MFMA32(k1, q1, s);
        if (WIN) {
#pragma unroll
            for (int r = 0; r < 16; ++r) { const int kl = keyof(r, h); if ((j == 0 && kl > ql) || (j == 8 && kl <= ql)) s[r] = -INFINITY; }
        }
        const float mn = fmaxf(m, rowmax16(s));
        const float al = __builtin_amdgcn_exp2f(m - mn); m = mn;
        float ps = 0.f;
#pragma unroll
        for (int r = 0; r < 16; ++r) { s[r] = __builtin_amdgcn_exp2f(s[r] - mn); }
        const bf16x8 p0 = packp(s, 0), p1 = packp(s, 1);
#pragma unroll
        for (int r = 0; r < 8; ++r) { ps += __uint_as_float((unsigned)(u16)p0[r] << 16) + __uint_as_float((unsigned)(u16)p1[r] << 16); }
        l = l * al + ps;
#pragma unroll
        for (int r = 0; r < 16; ++r) O[r] *= al;
        O = MFMA32(v0, p0, O); O = MFMA32(v1, p1, O);
    }
    Oout = O; lout = xhalf_sum(l);
}
DI bool unit_ok(const f32x16& O, float l) {
    bool ok = l <= 1.0995116e12f;
    float a = 0.f;
#pragma unroll
    for (int r = 0; r < 16; ++r) a = fmaxf(a, __builtin_fabsf(O[r]));
    bool okO = a <= 3.0e38f;
#pragma unroll
    for (int r = 0; r < 16; ++r) okO = okO && (O[r] == O[r]);
    return __all(ok && okO);
}

constexpr int P1_XSLOT = 32768, P1_WBASE = 3 * P1_XSLOT, P1_WSLOT = 12288;
constexpr int P2_AK = 0, P2_AV = 32768, P2_B = 65536, P2_SLOT = 32768;
constexpr int P2_Q = P2_B + 2 * P2_SLOT;
constexpr int P3_SLOT = 16384;

__global__ __launch_bounds__(512, 2) void k_fused(const u16* __restrict__ x, const u16* __restrict__ wA, const u16* __restrict__ wB, const u16* __restrict__ woA,
                                                  const u16* __restrict__ woB, const u16* __restrict__ gw, char* __restrict__ ws, u16* __restrict__ out) {
    extern __shared__ __attribute__((aligned(16))) char lds[];
    const unsigned lds0 = (unsigned)(uintptr_t)(LAS char*)lds;
    STAMP_DECL; STAMP_AT(0);
    const int tid = threadIdx.x, lane = tid & 63;
    const int w = __builtin_amdgcn_readfirstlane(tid >> 6);
    const int bid = blockIdx.x, xcd = bid & 7, idx = bid >> 3;
    const int head = xcd * 4 + (idx >> 3), rt = idx & 7;
    unsigned* flags = (unsigned*)(ws + WS_F);
    const unsigned l16 = lane * 16;
    float gate;
    unsigned token;

    f32x4 acc[4][6];
    {
        const char* xg = (const char*)x + (size_t)rt * 256 * 2048;
        const char* wg = (const char*)(((w & 3) >> 1) ? wB : wA) + head * 64;
        unsigned vx[4], vw[3];
        {
            const int r8 = lane >> 3, c8 = lane & 7, gsw = (4 * w + (lane >> 4)) & 7;
#pragma unroll
            for (int i = 0; i < 4; ++i) vx[i] = (unsigned)((8 * (w + 8 * i) + r8) * 2048 + ((c8 ^ gsw) << 4));
            const int sc = (lane & 3) ^ (((lane >> 5) & 1) << 1);
#pragma unroll
            for (int j = 0; j < 3; ++j) { const int i = 3 * (w & 3) + j, seg = i >> 1, rg = i & 1, type = seg % 3;
                vw[j] = (unsigned)(((w >> 2) * 32 + rg * 16 + (lane >> 2)) * 6144 + type * 2048 + (sc << 4)); }
        }
        const unsigned xdst = lds0 + w * 1024, wdst = lds0 + P1_WBASE + (w & 3) * 3072;
        const int whalf = w >> 2;
#define P1_GX(T, slot, i) glds16(vx[i], xg + (T) * 128, xdst + (slot) * P1_XSLOT + (i) * 8192)
#define P1_GW(T, hs, i) glds16(vw[i], wg + (size_t)(T) * (64 * 6144), wdst + (hs) * P1_WSLOT + (i) * 1024)
#pragma unroll
        for (int i = 0; i < 4; ++i) P1_GX(0, 0, i);
#pragma unroll
        for (int i = 0; i < 3; ++i) P1_GW(0, whalf, i);
#pragma unroll
        for (int i = 0; i < 4; ++i) P1_GX(1, 1, i);
#pragma unroll
        for (int i = 0; i < 3; ++i) P1_GW(1, 2 + whalf, i);
        u32x4 gx0, gx1, gw0, gw1;
        {
            const char* px = (const char*)x + (size_t)(rt * 256 + head * 8 + w) * 2048;
            const unsigned go = lane * 32, z = 0;
            asm volatile("global_load_dwordx4 %0, %5, %6\n\tglobal_load_dwordx4 %1, %5, %6 offset:16\n\tglobal_load_dwordx4 %2, %5, %7\n\tglobal_load_dwordx4 %3, %5, %7 offset:16\n\t"
                         "global_load_dword %4, %8, %9 sc1"
                         : "=&v"(gx0), "=&v"(gx1), "=&v"(gw0), "=&v"(gw1), "=&v"(token) : "v"(go), "s"(px), "s"(gw), "v"(z), "s"(flags) : "memory");
        }
        const int wm = w >> 1, wn = w & 1, g4 = lane >> 4;
        unsigned xa[2], wa[2];
#pragma unroll
        for (int u = 0; u < 2; ++u) xa[u] = lds0 + (wm * 64 + (lane & 15)) * 128 + (((4 * u + g4) ^ ((lane >> 1) & 7)) << 4);
#pragma unroll
        for (int nt = 0; nt < 2; ++nt) wa[nt] = lds0 + P1_WBASE + wn * 6144 + (8 * g4 + ((lane & 15) >> 2)) * 64 + ((nt ^ (g4 & 1)) * 32) + (lane & 3) * 8;
#pragma unroll
        for (int i = 0; i < 4; ++i)
#pragma unroll
            for (int j = 0; j < 6; ++j) acc[i][j] = f32x4{};
        bf16x8 xf[2][4]; s16x4 wl[2][6], wh[2][6];
#define P1_WF(b, n) cat8(wl[b][n], wh[b][n])
#define P1_MM(b, m, n) acc[m][n] = ((n) < 4) ? MFMA16(P1_WF(b, n), xf[b][m], acc[m][n]) : MFMA16(xf[b][m], P1_WF(b, n), acc[m][n])
#define P1_RW(b, wa_, n) do { wl[b][n] = ldtr(wa_[(n) & 1] + ((n) >> 1) * 2048); wh[b][n] = ldtr(wa_[(n) & 1] + ((n) >> 1) * 2048 + 256); } while (0)
#define P1_STEP(c, n, xo, u, wo, RD, G0, G1, G2, G3) do { \
            const unsigned xa_ = xa[u] + (xo); const unsigned wa_[2] = {wa[0] + (wo), wa[1] + (wo)}; \
            P1_MM(c, 0, 0); P1_MM(c, 0, 1); if (RD) { xf[n][0] = ld128(xa_); P1_RW(n, wa_, 0); } SBAR(); \
            P1_MM(c, 0, 2); P1_MM(c, 0, 3); if (RD) { P1_RW(n, wa_, 1); } SBAR(); \
            P1_MM(c, 0, 4); P1_MM(c, 0, 5); if (RD) { P1_RW(n, wa_, 2); } G0; SBAR(); \
            P1_MM(c, 1, 0); P1_MM(c, 1, 1); if (RD) { P1_RW(n, wa_, 3); } SBAR(); \
            P1_MM(c, 1, 2); P1_MM(c, 1, 3); if (RD) { P1_RW(n, wa_, 4); } SBAR(); \
            P1_MM(c, 1, 4); P1_MM(c, 1, 5); if (RD) { P1_RW(n, wa_, 5); } G1; SBAR(); \
            P1_MM(c, 2, 0); P1_MM(c, 2, 1); if (RD) { xf[n][1] = ld128(xa_ + 2048); } SBAR(); \
            P1_MM(c, 2, 2); P1_MM(c, 2, 3); if (RD) { xf[n][2] = ld128(xa_ + 4096); } SBAR(); \
            P1_MM(c, 2, 4); P1_MM(c, 2, 5); if (RD) { xf[n][3] = ld128(xa_ + 6144); } G2; SBAR(); \
            P1_MM(c, 3, 0); P1_MM(c, 3, 1); SBAR(); \
            P1_MM(c, 3, 2); P1_MM(c, 3, 3); G3; SBAR(); \
            P1_MM(c, 3, 4); P1_MM(c, 3, 5); SBAR(); } while (0)
        STAMP_AT(1);
        asm volatile("s_waitcnt vmcnt(12) lgkmcnt(0)\n\ts_barrier" ::: "memory");
        STAMP_AT(2);
        { xf[0][0] = ld128(xa[0]); xf[0][1] = ld128(xa[0] + 2048); xf[0][2] = ld128(xa[0] + 4096); xf[0][3] = ld128(xa[0] + 6144);
#pragma unroll
          for (int n = 0; n < 6; ++n) P1_RW(0, wa, n); }
        int xs = 0, ws0 = 0;
#define P1_TILE(T, ISS, WA, WB, RDLAST) do { \
            const int xs1 = xs == 2 ? 0 : xs + 1, xs2 = xs1 == 2 ? 0 : xs1 + 1; \
            const int ws1 = ws0 == 4 ? 0 : ws0 + 1, ws2 = ws1 == 4 ? 0 : ws1 + 1; \
            const int ws4 = ws0 == 0 ? 4 : ws0 - 1; const int wsd = whalf ? ws0 : ws4; \
            P1_STEP(0, 1, xs * P1_XSLOT, 1, ws1 * P1_WSLOT, 1, if (ISS) P1_GX((T) + 2, xs2, 0), if (ISS) P1_GX((T) + 2, xs2, 1), if (ISS) P1_GX((T) + 2, xs2, 2), if (ISS) P1_GX((T) + 2, xs2, 3)); \
            WA; \
            P1_STEP(1, 0, xs1 * P1_XSLOT, 0, ws2 * P1_WSLOT, RDLAST, if (ISS) P1_GW((T) + 2, wsd, 0), if (ISS) P1_GW((T) + 2, wsd, 1), if (ISS) P1_GW((T) + 2, wsd, 2), NOP_); \
            WB; \
            xs = xs1; ws0 = ws2; } while (0)
        P1_TILE(0, 1, asm volatile("s_waitcnt vmcnt(4) lgkmcnt(0)\n\ts_barrier" : "+v"(gx0), "+v"(gx1), "+v"(gw0), "+v"(gw1), "+v"(token)::"memory"), WAIT_BAR(7), 1);
        {
            float s = 0.f;
#pragma unroll
            for (int i = 0; i < 4; ++i) {
                s += __uint_as_float(gx0[i] << 16) * __uint_as_float(gw0[i] << 16) + __uint_as_float(gx0[i] & 0xffff0000u) * __uint_as_float(gw0[i] & 0xffff0000u);
                s += __uint_as_float(gx1[i] << 16) * __uint_as_float(gw1[i] << 16) + __uint_as_float(gx1[i] & 0xffff0000u) * __uint_as_float(gw1[i] & 0xffff0000u);
            }
#pragma unroll
            for (int o = 32; o >= 1; o >>= 1) s += __shfl_xor(s, o);
            gate = 1.0f / (1.0f + __expf(-s));
            token = __builtin_amdgcn_readfirstlane(token) + 1u;
        }
        for (int T = 1; T < 14; ++T) P1_TILE(T, 1, WAIT_BAR(4), WAIT_BAR(7), 1);
        P1_TILE(14, 0, WAIT_BAR(0), NOP_, 1);
        P1_TILE(15, 0, NOP_, NOP_, 0);
#undef P1_TILE
#undef P1_STEP
#undef P1_RW
#undef P1_MM
#undef P1_WF
#undef P1_GX
#undef P1_GW
    }
    STAMP_AT(3);
    LBAR();
    {
        const int wm = w >> 1, wn = w & 1, g4 = lane >> 4, m = lane & 15;
        char* kvg = ws + WS_KV + (size_t)((wn * 32 + head) * 8 + rt) * 32768;
        const unsigned kl = lds0 + (wn ? P2_B : P2_AK + 16384), vl = lds0 + (wn ? P2_B + 16384 : P2_AV + 16384), ql_ = lds0 + P2_Q + wn * 16384;
        const unsigned so = (g4 >> 1) * 1024 + (g4 & 1) * 512;
#pragma unroll
        for (int mt = 0; mt < 4; ++mt) {
            const int bl = wm * 2 + (mt >> 1), a = mt & 1;
            const unsigned oq = bl * 2048 + so + (a * 16 + m) * 16;
            const unsigned ok = bl * 2048 + so + (16 * (m >> 3) + 8 * a + (m & 7)) * 16;
            st128(ql_ + oq, pack44(acc[mt][0], acc[mt][1], QSCALE));
            const u32x4 kk = pack44(acc[mt][2], acc[mt][3], 1.0f);
            st128(kl + ok, kk); st16_wt(ok, kvg, kk);
        }
#pragma unroll
        for (int b2 = 0; b2 < 2; ++b2)
#pragma unroll
            for (int nt = 0; nt < 2; ++nt) {
                const unsigned ov = (wm * 2 + b2) * 2048 + so + (nt * 16 + m) * 16;
                const u32x4 vv = pack44(acc[2 * b2][4 + nt], acc[2 * b2 + 1][4 + nt], 1.0f);
                st128(vl + ov, vv); st16_wt(16384 + ov, kvg, vv);
            }
    }
    LBAR();
    STAMP_AT(4);

    const int rb = rt * 8 + w;
    const int ql = lane & 31, hh = lane >> 5;
    const char* kvA = ws + WS_KV + (size_t)(head * 8) * 32768;
    const char* kvB = ws + WS_KV + (size_t)((32 + head) * 8) * 32768;
    bf16x8 sel;
    { const short o = (((lane >> 4) & 1) == ((lane >> 2) & 1)) ? (short)0x3F80 : (short)0;
#pragma unroll
      for (int i = 0; i < 8; ++i) sel[i] = o; }
    const bf16x8 qa0 = ld128(lds0 + P2_Q + w * 2048 + l16), qa1 = ld128(lds0 + P2_Q + w * 2048 + 1024 + l16);
    const bf16x8 qb0 = ld128(lds0 + P2_Q + 16384 + w * 2048 + l16), qb1 = ld128(lds0 + P2_Q + 16384 + w * 2048 + 1024 + l16);
    char* og = ws + WS_O + ((size_t)rb * 64 + head) * 2048;
    const unsigned oo = (ql >> 4) * 1024 + (hh * 16 + (ql & 15)) * 16;

#define EX(x) __builtin_amdgcn_exp2f(x)
#define P8(x) __builtin_bit_cast(bf16x8, x)
#define STEP(SC, SN, Q0, Q1, AK0, AK1, AV1, AV0) do { \
        phi[2] = cvtpk(t0, t1); phi[3] = cvtpk(t2, t3); PIN(phi); \
        SN = MFMA32(k0, Q0, negm); k0 = ld128(AK0); \
        t0 = EX(SC[0]); t1 = EX(SC[1]); t2 = EX(SC[2]); t3 = EX(SC[3]); PIN(t0); PIN(t1); PIN(t2); PIN(t3); SBAR(); \
        SN = MFMA32(k1, Q1, SN); k1 = ld128(AK1); \
        plo[0] = cvtpk(t0, t1); plo[1] = cvtpk(t2, t3); \
        t0 = EX(SC[4]); t1 = EX(SC[5]); t2 = EX(SC[6]); t3 = EX(SC[7]); PIN(plo); PIN(t0); PIN(t1); PIN(t2); PIN(t3); SBAR(); \
        O = MFMA32(v1, P8(phi), O); L = MFMA16(sel, P8(phi), L); v1 = ld128(AV1); \
        plo[2] = cvtpk(t0, t1); plo[3] = cvtpk(t2, t3); \
        t0 = EX(SC[8]); t1 = EX(SC[9]); t2 = EX(SC[10]); t3 = EX(SC[11]); PIN(plo); PIN(t0); PIN(t1); PIN(t2); PIN(t3); SBAR(); \
        O = MFMA32(v0, P8(plo), O); L = MFMA16(sel, P8(plo), L); v0 = ld128(AV0); \
        phi[0] = cvtpk(t0, t1); phi[1] = cvtpk(t2, t3); \
        t0 = EX(SC[12]); t1 = EX(SC[13]); t2 = EX(SC[14]); t3 = EX(SC[15]); PIN(phi); PIN(t0); PIN(t1); PIN(t2); PIN(t3); SBAR(); \
    } while (0)
#define CHUNK_DMA(src, dstK, dstV) do { const char* s_ = (src) + w * 2048; \
        glds16(l16, s_, (dstK) + w * 2048); glds16(l16, s_ + 1024, (dstK) + w * 2048 + 1024); glds16(l16, s_ + 16384, (dstV) + w * 2048); glds16(l16, s_ + 16384 + 1024, (dstV) + w * 2048 + 1024); } while (0)
    {
        f32x16 O = {}, negm, SA, SB; f32x4 L = {};
        bf16x8 k0, k1, v0, v1; u32x4 plo, phi; float t0 = 0.f, t1 = 0.f, t2 = 0.f, t3 = 0.f;
        unsigned cur = lds0 + P2_B + l16, nxt = cur + P2_SLOT;
        {
            k0 = ld128(cur); k1 = ld128(cur + 1024);
            SA = f32x16{}; SA = MFMA32(k0, qb0, SA); SA = MFMA32(k1, qb1, SA);
            const float m = rowmax16(SA);
#pragma unroll
            for (int r = 0; r < 16; ++r) { negm[r] = -m; SA[r] -= m; }
            k0 = ld128(cur + 2048); k1 = ld128(cur + 2048 + 1024);
            v0 = ld128(cur + 16384);
            v1 = bf16x8{}; phi = u32x4{}; plo = u32x4{};
        }
#define KAD(j, s) (((j) < 8 ? cur + (j) * 2048 : nxt + ((j) - 8) * 2048) + (s) * 1024)
#define BSTEP(SC, SN, j) STEP(SC, SN, qb0, qb1, KAD((j) + 2, 0), KAD((j) + 2, 1), KAD(j, 1) + 16384, KAD((j) + 1, 0) + 16384)
        BSTEP(SA, SB, 0);
        WAIT_BAR(0);
        if (tid == 0) flag_set(flags + 64 + head * 8 + rt, token);
        BSTEP(SB, SA, 1); BSTEP(SA, SB, 2);
        if (w == 0) poll_acquire(flags + 64 + head * 8, 8, token, lane);
        LBAR();
        CHUNK_DMA(kvB + ((rt + 1) & 7) * 32768, lds0 + P2_B + P2_SLOT, lds0 + P2_B + P2_SLOT + 16384);
        CHUNK_DMA(kvB + ((rt + 2) & 7) * 32768, lds0 + P2_B + 2 * P2_SLOT, lds0 + P2_B + 2 * P2_SLOT + 16384);
        CHUNK_DMA(kvA + (rt > 0 ? rt - 1 : 0) * 32768, lds0 + P2_AK, lds0 + P2_AV);
        BSTEP(SB, SA, 3); BSTEP(SA, SB, 4); BSTEP(SB, SA, 5);
        WAIT_BAR(8);
        BSTEP(SA, SB, 6); BSTEP(SB, SA, 7);
        STAMP_AT(5);
        int slot = 1;
        for (int i = 1; i < 8; ++i) {
            cur = nxt; nxt = lds0 + P2_B + (slot == 2 ? 0 : slot + 1) * P2_SLOT + l16;
            BSTEP(SA, SB, 0); BSTEP(SB, SA, 1); BSTEP(SA, SB, 2); BSTEP(SB, SA, 3); BSTEP(SA, SB, 4); BSTEP(SB, SA, 5);
            if (i == 1) WAIT_BAR(4); else WAIT_BAR(0);
            if (i < 6) { const int s2 = slot == 0 ? 2 : slot - 1; CHUNK_DMA(kvB + ((rt + i + 2) & 7) * 32768, lds0 + P2_B + s2 * P2_SLOT, lds0 + P2_B + s2 * P2_SLOT + 16384); }
            BSTEP(SA, SB, 6); BSTEP(SB, SA, 7);
            slot = slot == 2 ? 0 : slot + 1;
        }
        phi[2] = cvtpk(t0, t1); phi[3] = cvtpk(t2, t3);
        O = MFMA32(v1, P8(phi), O); L = MFMA16(sel, P8(phi), L);
        float l = L[0];
        if (!unit_ok(O, l)) attn_safe<false>(kvB, qb0, qb1, rb, lane, O, l);
        const float f = 1.0f / l;
        st16_wt(oo, og + 32 * 2048, __builtin_bit_cast(u32x4, pack8(O, 0, f))); st16_wt(oo + 512, og + 32 * 2048, __builtin_bit_cast(u32x4, pack8(O, 1, f)));
#undef BSTEP
#undef KAD
    }
    STAMP_AT(6);
    const int rt3 = (xcd & 3) * 4 + (idx & 3), ct = (xcd >> 2) * 8 + (idx >> 2);
    const int p3 = w & 3, half = w >> 2;
    const char* wsrc = (const char*)(half ? woB : woA) + ct * 128;
    const unsigned vw0 = (unsigned)((16 * p3 + (lane >> 3)) * 2048 + (((lane & 7) ^ (((lane >> 4) & 3) << 1)) << 4)), vw1 = vw0 + 8 * 2048;
    const unsigned wdst3 = lds0 + half * 8192 + p3 * 2048;
#define P3_SOFF(s) ((s) < 6 ? 65536 + (s) * P3_SLOT : ((s) - 6) * P3_SLOT)
#define P3_GW(st, i) glds16((i) ? vw1 : vw0, wsrc + (size_t)(st) * (64 * 2048), wdst3 + P3_SOFF((st) & 7) + (i) * 1024)
    LBAR();
#pragma unroll
    for (int st = 0; st < 6; ++st) { P3_GW(st, 0); P3_GW(st, 1); }
    {
        f32x16 O = {}, negm, SA, SB; f32x4 L = {};
        bf16x8 k0, k1, v0, v1; u32x4 plo, phi; float t0 = 0.f, t1 = 0.f, t2 = 0.f, t3 = 0.f;
        const unsigned ab = lds0 + P2_AK + w * 2048 + l16;
        const int jmax = rt > 0 ? 8 : w;
#define AAD(j, s) (ab + (8 - ((j) > 8 ? 8 : (j))) * 2048 + (s) * 1024)
        {
            k0 = ld128(AAD(0, 0)); k1 = ld128(AAD(0, 1));
            SA = f32x16{}; SA = MFMA32(k0, qa0, SA); SA = MFMA32(k1, qa1, SA);
#pragma unroll
            for (int r = 0; r < 16; ++r) if (keyof(r, hh) > ql) SA[r] = -INFINITY;
            const float m = rowmax16(SA);
#pragma unroll
            for (int r = 0; r < 16; ++r) { negm[r] = -m; SA[r] -= m; }
            k0 = ld128(AAD(1, 0)); k1 = ld128(AAD(1, 1));
            v0 = ld128(AAD(0, 0) + 32768);
            v1 = bf16x8{}; phi = u32x4{}; plo = u32x4{};
        }
#define ASTEP(SC, SN, j) do { if ((j) + 1 > jmax) { _Pragma("unroll") for (int r_ = 0; r_ < 16; ++r_) negm[r_] = -INFINITY; } \
        STEP(SC, SN, qa0, qa1, AAD((j) + 2, 0), AAD((j) + 2, 1), AAD(j, 1) + 32768, AAD((j) + 1, 0) + 32768); } while (0)
        ASTEP(SA, SB, 0); ASTEP(SB, SA, 1); ASTEP(SA, SB, 2); ASTEP(SB, SA, 3);
        ASTEP(SA, SB, 4); ASTEP(SB, SA, 5); ASTEP(SA, SB, 6); ASTEP(SB, SA, 7);
#pragma unroll
        for (int r = 0; r < 16; ++r) if (keyof(r, hh) <= ql) SA[r] = -INFINITY;
        ASTEP(SA, SB, 8);
        phi[2] = cvtpk(t0, t1); phi[3] = cvtpk(t2, t3);
        O = MFMA32(v1, P8(phi), O); L = MFMA16(sel, P8(phi), L);
        float l = L[0];
        if (!unit_ok(O, l)) attn_safe<true>(kvA, qa0, qa1, rb, lane, O, l);
        const float f = 1.0f / l;
        st16_wt(oo, og, __builtin_bit_cast(u32x4, pack8(O, 0, f))); st16_wt(oo + 512, og, __builtin_bit_cast(u32x4, pack8(O, 1, f)));
        if (lane == 0) __hip_atomic_store((GAS float*)(ws + WS_G) + rt * 256 + head * 8 + w, gate, __ATOMIC_RELAXED, __HIP_MEMORY_SCOPE_AGENT);
#undef ASTEP
#undef AAD
    }
#undef STEP
#undef EX
#undef P8
#undef CHUNK_DMA
    STAMP_AT(7);
    WAIT_BAR(0);
    if (tid == 0) flag_set(flags + 64 + 256 + rt * 32 + head, token);

    {
        if (w == 0) poll_acquire(flags + 64 + 256 + (rt3 >> 1) * 32, 32, token, lane);
        LBAR();
        STAMP_AT(8);
        const char* osrc = ws + WS_O + ((size_t)(rt3 * 4 + p3) * 64 + half * 32) * 2048;
        bf16x8 of[4][4];
#define P3_GO(st, b) do { const char* b_ = osrc + (st) * 4096; \
            asm volatile("global_load_dwordx4 %0, %4, %5\n\tglobal_load_dwordx4 %1, %4, %5 offset:1024\n\tglobal_load_dwordx4 %2, %4, %5 offset:2048\n\tglobal_load_dwordx4 %3, %4, %5 offset:3072" \
                         : "=&v"(of[b][0]), "=&v"(of[b][1]), "=&v"(of[b][2]), "=&v"(of[b][3]) : "v"(l16), "s"(b_) : "memory"); } while (0)
        P3_GO(0, 0); P3_GO(1, 1); P3_GO(2, 2);
        float gq[2];
        { const float* gp = (const float*)(ws + WS_G) + rt3 * 128 + p3 * 32; const unsigned go = (lane & 15) * 4;
          asm volatile("global_load_dword %0, %2, %3\n\tglobal_load_dword %1, %2, %3 offset:64" : "=&v"(gq[0]), "=&v"(gq[1]) : "v"(go), "s"(gp) : "memory"); }
        const int g4 = lane >> 4, q4 = (lane & 15) >> 2;
        unsigned wa[4];
        { const unsigned a0 = (16 * (g4 >> 1) + 4 * (g4 & 1) + q4) * 128 + ((2 * (g4 & 1) + (q4 >> 1)) * 32) + (lane & 3) * 8;
#pragma unroll
          for (int n = 0; n < 4; ++n) wa[n] = lds0 + half * 8192 + (a0 ^ (n << 5)); }
        f32x4 ac[2][4];
#pragma unroll
        for (int i = 0; i < 2; ++i)
#pragma unroll
            for (int j = 0; j < 4; ++j) ac[i][j] = f32x4{};
        s16x4 wl[2][2][4], wh[2][2][4];
#define P3_RW(buf, so_, k_, n_) do { wl[buf][k_][n_] = ldtr(wa[n_] + (so_) + (k_) * 4096); wh[buf][k_][n_] = ldtr(wa[n_] + (so_) + (k_) * 4096 + 1024); } while (0)
#define P3_STAGE(st, b, NW) do { \
            asm volatile("s_waitcnt vmcnt(" #NW ") lgkmcnt(0)\n\ts_barrier" : "+v"(of[b][0]), "+v"(of[b][1]), "+v"(of[b][2]), "+v"(of[b][3])::"memory"); \
            const unsigned sn_ = P3_SOFF(((st) + 1) & 7); \
            _Pragma("unroll") for (int k_ = 0; k_ < 2; ++k_) { \
                _Pragma("unroll") for (int n_ = 0; n_ < 4; ++n_) { \
                    const bf16x8 wf_ = cat8(wl[(st) & 1][k_][n_], wh[(st) & 1][k_][n_]); \
                    ac[0][n_] = MFMA16(wf_, of[b][2 * k_], ac[0][n_]); ac[1][n_] = MFMA16(wf_, of[b][2 * k_ + 1], ac[1][n_]); \
                    if ((st) + 1 < 16) P3_RW(((st) + 1) & 1, sn_, k_, n_); \
                    if (k_ == 0 && n_ == 0 && (st) + 6 < 16) P3_GW((st) + 6, 0); \
                    if (k_ == 0 && n_ == 2 && (st) + 6 < 16) P3_GW((st) + 6, 1); \
                    SBAR(); } } \
            if ((st) + 3 < 16) P3_GO((st) + 3, ((b) + 3) & 3); \
        } while (0)
#pragma unroll
        for (int k_ = 0; k_ < 2; ++k_)
#pragma unroll
            for (int n_ = 0; n_ < 4; ++n_) P3_RW(0, P3_SOFF(0), k_, n_);
        P3_STAGE(0, 0, 10); P3_STAGE(1, 1, 12); P3_STAGE(2, 2, 14); P3_STAGE(3, 3, 12);
        P3_STAGE(4, 0, 12); P3_STAGE(5, 1, 12); P3_STAGE(6, 2, 12); P3_STAGE(7, 3, 12);
        P3_STAGE(8, 0, 12); P3_STAGE(9, 1, 12); P3_STAGE(10, 2, 12); P3_STAGE(11, 3, 10);
        P3_STAGE(12, 0, 8); P3_STAGE(13, 1, 8); P3_STAGE(14, 2, 4); P3_STAGE(15, 3, 0);
        STAMP_AT(9);
        asm volatile("" : "+v"(gq[0]), "+v"(gq[1]));
        const float f0 = half ? 1.0f - gq[0] : gq[0], f1 = half ? 1.0f - gq[1] : gq[1];
#pragma unroll
        for (int n = 0; n < 4; ++n) { ac[0][n] *= f0; ac[1][n] *= f1; }
        LBAR();
        LAS f32x4* mine = (LAS f32x4*)(uintptr_t)(lds0 + w * 4096 + l16);
        LAS f32x4* theirs = (LAS f32x4*)(uintptr_t)(lds0 + (w ^ 4) * 4096 + l16);
#pragma unroll
        for (int q = 0; q < 2; ++q) { mine[(q * 2) * 64] = half ? ac[q][0] : ac[q][2]; mine[(q * 2 + 1) * 64] = half ? ac[q][1] : ac[q][3]; }
        LBAR();
#pragma unroll
        for (int q = 0; q < 2; ++q)
#pragma unroll
            for (int e = 0; e < 2; ++e) {
                const f32x4 r = (half ? ac[q][2 + e] : ac[q][e]) + theirs[(q * 2 + e) * 64];
                u16* op = out + (size_t)(rt3 * 128 + p3 * 32 + q * 16 + (lane & 15)) * 1024 + ct * 64 + half * 32 + e * 16 + g4 * 4;
                *(u32x2*)op = u32x2{cvtpk(r[0], r[1]), cvtpk(r[2], r[3])};
            }
#undef P3_GO
#undef P3_RW
#undef P3_STAGE
    }
#undef P3_GW
#undef P3_SOFF
    if (bid == 0 && w == 0) {
        const unsigned long long t0 = __builtin_amdgcn_s_memrealtime();
        for (;;) {
            bool ok = true;
#pragma unroll
            for (int i = 0; i < 4; ++i) ok = ok && (__hip_atomic_load((GAS unsigned*)(flags + 64 + 256 + i * 64 + lane), __ATOMIC_RELAXED, __HIP_MEMORY_SCOPE_AGENT) == token);
            if (__all(ok)) break;
            if (__builtin_amdgcn_s_memrealtime() - t0 > 400000ull) break;
            __builtin_amdgcn_s_sleep(1);
        }
        if (lane == 0) flag_set(flags, token);
    }
    STAMP_AT(10);
    STAMP_FLUSH(ws, 11);
}

extern "C" void kernel_launch(void* const* d_in, const int* in_sizes, int n_in, void* d_out, int out_size, void* d_ws, size_t ws_size, hipStream_t stream) {
    (void)in_sizes; (void)n_in; (void)out_size; (void)ws_size;
    const u16* x = (const u16*)d_in[0]; const u16* wqkvA = (const u16*)d_in[1]; const u16* woA = (const u16*)d_in[2];
    const u16* wqkvB = (const u16*)d_in[4]; const u16* woB = (const u16*)d_in[5]; const u16* gw = (const u16*)d_in[6];
    (void)hipFuncSetAttribute((const void*)k_fused, hipFuncAttributeMaxDynamicSharedMemorySize, LDS_BYTES);
    k_fused<<<256, 512, LDS_BYTES, stream>>>(x, wqkvA, wqkvB, woA, woB, gw, (char*)d_ws, (u16*)d_out);
}
```

```cpp
#include <hip/hip_runtime.h>
#include <hip/hip_bf16.h>
#include <stdint.h>
#pragma clang diagnostic ignored "-Winline-asm"

typedef unsigned short u16;
typedef short bf16x8 __attribute__((ext_vector_type(8)));
typedef short s16x4 __attribute__((ext_vector_type(4)));
typedef float f32x16 __attribute__((ext_vector_type(16)));
typedef float f32x4 __attribute__((ext_vector_type(4)));
typedef float f32x2 __attribute__((ext_vector_type(2)));
typedef __bf16 bf16x2_t __attribute__((ext_vector_type(2)));
typedef unsigned u32x4 __attribute__((ext_vector_type(4)));
typedef unsigned u32x2 __attribute__((ext_vector_type(2)));
#define LAS __attribute__((address_space(3)))
#define GAS __attribute__((address_space(1)))
#define DI __device__ __forceinline__
#define MFMA32(a, b, c) __builtin_amdgcn_mfma_f32_32x32x16_bf16((a), (b), (c), 0, 0, 0)
#define MFMA16(a, b, c) __builtin_amdgcn_mfma_f32_16x16x32_bf16((a), (b), (c), 0, 0, 0)
#define SBAR() __builtin_amdgcn_sched_barrier(0)
#define PIN(x) asm volatile("" : "+v"(x))
#define WAITCNT_VM_LGKM0(N) __builtin_amdgcn_s_waitcnt(((N) & 15) | (((N) >> 4) << 14) | 0x70)
#define WAIT_BAR(N) do { asm volatile("" ::: "memory"); WAITCNT_VM_LGKM0(N); __builtin_amdgcn_s_barrier(); asm volatile("" ::: "memory"); } while (0)
#define LBAR() WAIT_BAR(63)
#define NOP_ do {} while (0)

constexpr float QSCALE = 0.17677669529663687f * 1.4426950408889634f;
constexpr size_t WS_KV = 0;
constexpr size_t WS_O = 16u << 20;
constexpr size_t WS_G = 24u << 20;
constexpr size_t WS_F = (24u << 20) + 8192;
constexpr int LDS_BYTES = 163840;

DI unsigned cvtpk(float lo, float hi) { f32x2 v = {lo, hi}; bf16x2_t b = __builtin_convertvector(v, bf16x2_t); return __builtin_bit_cast(unsigned, b); }
DI bf16x8 pack8(const f32x16& x, int s, float sc) {
    u32x4 w = {cvtpk(x[8 * s] * sc, x[8 * s + 1] * sc), cvtpk(x[8 * s + 2] * sc, x[8 * s + 3] * sc), cvtpk(x[8 * s + 4] * sc, x[8 * s + 5] * sc), cvtpk(x[8 * s + 6] * sc, x[8 * s + 7] * sc)};
    return __builtin_bit_cast(bf16x8, w);
}
DI u32x4 pack44(const f32x4& a, const f32x4& b, float sc) { return u32x4{cvtpk(a[0] * sc, a[1] * sc), cvtpk(a[2] * sc, a[3] * sc), cvtpk(b[0] * sc, b[1] * sc), cvtpk(b[2] * sc, b[3] * sc)}; }
DI void glds16(unsigned voff, const void* sbase, unsigned lds_dst) {
    asm volatile("s_mov_b32 m0, %2\n\ts_nop 0\n\tglobal_load_lds_dwordx4 %0, %1" ::"v"(voff), "s"(sbase), "s"(lds_dst) : "memory", "m0");
}
DI void glds16c(unsigned voff, const void* sbase, unsigned lds_dst) {
    asm volatile("s_mov_b32 m0, %2\n\ts_nop 0\n\tglobal_load_lds_dwordx4 %0, %1 sc1" ::"v"(voff), "s"(sbase), "s"(lds_dst) : "memory", "m0");
}
DI void glds16m(unsigned voff, const void* sbase, unsigned lds_dst, unsigned mask) {
    asm volatile("s_mov_b32 m0, %2\n\ts_mov_b32 exec_lo, %3\n\ts_mov_b32 exec_hi, %3\n\tglobal_load_lds_dwordx4 %0, %1\n\ts_mov_b64 exec, -1" ::"v"(voff), "s"(sbase), "s"(lds_dst), "s"(mask) : "memory", "m0");
}
DI void st16_wt(unsigned voff, void* sbase, u32x4 v) { asm volatile("global_store_dwordx4 %0, %1, %2 sc1\n\ts_nop 1" ::"v"(voff), "v"(v), "s"(sbase) : "memory"); }
DI void st16_wtm(unsigned voff, void* sbase, u32x4 v, unsigned mask) {
    asm volatile("s_mov_b32 exec_lo, %3\n\ts_mov_b32 exec_hi, %3\n\tglobal_store_dwordx4 %0, %1, %2 sc1\n\ts_mov_b64 exec, -1\n\ts_nop 0" ::"v"(voff), "v"(v), "s"(sbase), "s"(mask) : "memory");
}
typedef short v4i16_t __attribute__((ext_vector_type(4)));
DI s16x4 ldtr(unsigned a) { return __builtin_bit_cast(s16x4, __builtin_amdgcn_ds_read_tr16_b64_v4i16((LAS v4i16_t*)(uintptr_t)a)); }
DI bf16x8 ld128(unsigned a) { return *(const LAS bf16x8*)(uintptr_t)a; }
DI void st128(unsigned a, u32x4 v) { *(LAS u32x4*)(uintptr_t)a = v; }
DI bf16x8 cat8(s16x4 lo, s16x4 hi) { return __builtin_shufflevector(lo, hi, 0, 1, 2, 3, 4, 5, 6, 7); }

#define STAMP_DECL do {} while (0)
#define STAMP_AT(i) do {} while (0)
#define STAMP_FLUSH(wsp, n) do {} while (0)

DI void flag_set(unsigned* f, unsigned v) { __hip_atomic_store((GAS unsigned*)f, v, __ATOMIC_RELAXED, __HIP_MEMORY_SCOPE_AGENT); }
DI void poll_spin(const unsigned* f, int n, unsigned want, int lane) {
    const unsigned long long t0 = __builtin_amdgcn_s_memrealtime();
    for (;;) {
        unsigned v = want;
        if (lane < n) v = __hip_atomic_load((GAS unsigned*)(f + lane), __ATOMIC_RELAXED, __HIP_MEMORY_SCOPE_AGENT);
        if (__all(v == want)) break;
        if (__builtin_amdgcn_s_memrealtime() - t0 > 400000ull) break;
        __builtin_amdgcn_s_sleep(1);
    }
    __builtin_amdgcn_fence(__ATOMIC_ACQUIRE, "wavefront");
}
#define POLL_ISSUE(pv, f, n) do { const unsigned* p_ = (f) + (lane < (n) ? lane : 0); asm volatile("global_load_dword %0, %1, off sc1" : "=v"(pv) : "v"(p_) : "memory"); } while (0)
#define POLL_CHECK(pv, f, n) do { asm volatile("s_waitcnt vmcnt(0)" : "+v"(pv)::"memory"); if (!__all((pv) == token)) poll_spin(f, n, token, lane); } while (0)

DI float xhalf_max(float m) { auto rr = __builtin_amdgcn_permlane32_swap(__float_as_uint(m), __float_as_uint(m), false, false); return fmaxf(__uint_as_float(rr[0]), __uint_as_float(rr[1])); }
DI float xhalf_sum(float m) { auto rr = __builtin_amdgcn_permlane32_swap(__float_as_uint(m), __float_as_uint(m), false, false); return __uint_as_float(rr[0]) + __uint_as_float(rr[1]); }
DI int keyof(int r, int h) { return (r & 3) + 8 * (r >> 2) + 4 * h; }
DI float rowmax16(const f32x16& s) {
    float a = fmaxf(fmaxf(s[0], s[1]), s[2]), b = fmaxf(fmaxf(s[3], s[4]), s[5]);
#pragma unroll
    for (int r = 6; r < 16; r += 2) a = fmaxf(fmaxf(a, s[r]), s[r + 1]);
    return xhalf_max(fmaxf(a, b));
}
DI bf16x8 packp(const f32x16& p, int s) {
    u32x4 w = {cvtpk(p[8 * s], p[8 * s + 1]), cvtpk(p[8 * s + 2], p[8 * s + 3]), cvtpk(p[8 * s + 4], p[8 * s + 5]), cvtpk(p[8 * s + 6], p[8 * s + 7])};
    return __builtin_bit_cast(bf16x8, w);
}
template <bool WIN>
DI void attn_safe(const char* kv, bf16x8 q0, bf16x8 q1, int rb, int lane, f32x16& Oout, float& lout) {
    const int ql = lane & 31, h = lane >> 5;
    __builtin_amdgcn_fence(__ATOMIC_ACQUIRE, "agent");
    asm volatile("s_waitcnt vmcnt(0)" ::: "memory");
    float m = -INFINITY, l = 0.f; f32x16 O = {};
    const int nb = WIN ? (rb < 8 ? rb + 1 : 9) : 64;
    for (int j = 0; j < nb; ++j) {
        const int kb = WIN ? rb - j : j;
        const char* kp = kv + (kb >> 3) * 32768 + (kb & 7) * 2048 + lane * 16;
        const bf16x8 k0 = *(const bf16x8*)kp, k1 = *(const bf16x8*)(kp + 1024), v0 = *(const bf16x8*)(kp + 16384), v1 = *(const bf16x8*)(kp + 16384 + 1024);
        f32x16 s = {}; s = MFMA32(k0, q0, s); s = MFMA32(k1, q1, s);
        if (WIN) {
#pragma unroll
            for (int r = 0; r < 16; ++r) { const int kl = keyof(r, h); if ((j == 0 && kl > ql) || (j == 8 && kl <= ql)) s[r] = -INFINITY; }
        }
        const float mn = fmaxf(m, rowmax16(s));
        const float al = __builtin_amdgcn_exp2f(m - mn); m = mn;
        float ps = 0.f;
#pragma unroll
        for (int r = 0; r < 16; ++r) { s[r] = __builtin_amdgcn_exp2f(s[r] - mn); }
        const bf16x8 p0 = packp(s, 0), p1 = packp(s, 1);
#pragma unroll
        for (int r = 0; r < 8; ++r) { ps += __uint_as_float((unsigned)(u16)p0[r] << 16) + __uint_as_float((unsigned)(u16)p1[r] << 16); }
        l = l * al + ps;
#pragma unroll
        for (int r = 0; r < 16; ++r) O[r] *= al;
        O = MFMA32(v0, p0, O); O = MFMA32(v1, p1, O);
    }
    Oout = O; lout = xhalf_sum(l);
}
DI bool unit_ok(const f32x16& O, float l) {
    bool ok = l <= 1.0995116e12f;
    float a = 0.f;
#pragma unroll
    for (int r = 0; r < 16; ++r) a = fmaxf(a, __builtin_fabsf(O[r]));
    bool okO = a <= 3.0e38f;
#pragma unroll
    for (int r = 0; r < 16; ++r) okO = okO && (O[r] == O[r]);
    return __all(ok && okO);
}

constexpr int P1_XSLOT = 32768, P1_WBASE = 3 * P1_XSLOT, P1_WSLOT = 12288;
constexpr int P2_AK = 0, P2_AV = 32768, P2_B = 65536, P2_SLOT = 32768;
constexpr int P2_Q = P2_B + 2 * P2_SLOT;
constexpr int P3_SLOT = 16384;

__global__ __launch_bounds__(512, 2) void k_fused(const u16* __restrict__ x, const u16* __restrict__ wA, const u16* __restrict__ wB, const u16* __restrict__ woA,
                                                  const u16* __restrict__ woB, const u16* __restrict__ gw, char* __restrict__ ws, u16* __restrict__ out) {
    extern __shared__ __attribute__((aligned(16))) char lds[];
    const unsigned lds0 = (unsigned)(uintptr_t)(LAS char*)lds;
    STAMP_DECL; STAMP_AT(0);
    const int tid = threadIdx.x, lane = tid & 63;
    const int w = __builtin_amdgcn_readfirstlane(tid >> 6);
    const int bid = blockIdx.x, xcd = bid & 7, idx = bid >> 3;
    const int head = xcd * 4 + (idx >> 3), rt = idx & 7;
    unsigned* flags = (unsigned*)(ws + WS_F);
    const unsigned l16 = lane * 16;
    float gate;
    unsigned token;

    f32x16 acc[2][3];
    {
        const char* xg = (const char*)x + (size_t)rt * 256 * 2048;
        const bool w0 = w == 0, w7 = w == 7;
        const int whalf = w >= 4;
        unsigned vo[4], ve[4];
        const char* ebA; const char* ebB; unsigned estride, edst;
        {
            const int r8 = lane >> 3, c8 = lane & 7;
            const int sc = (lane & 3) ^ (((lane >> 5) & 1) << 1);
#pragma unroll
            for (int j = 0; j < 4; ++j) {
                const int p = 4 * (w7 ? 0 : w) + j;
                vo[j] = (unsigned)((8 * p + r8) * 2048 + ((c8 ^ ((4 * p + (lane >> 4)) & 7)) << 4));
                const int pe = 28 + j;
                const unsigned vxe = (unsigned)((8 * pe + r8) * 2048 + ((c8 ^ ((4 * pe + (lane >> 4)) & 7)) << 4));
                const int i = 4 * ((w7 || w0 ? 1 : w) - 1) + j, ii = i >= 12 ? i - 12 : i, seg = ii >> 1, rg = ii & 1, type = seg >= 3 ? seg - 3 : seg;
                const unsigned vwe = (unsigned)((whalf * 32 + rg * 16 + (lane >> 2)) * 6144 + type * 2048 + (sc << 4));
                ve[j] = w0 ? vxe : vwe;
            }
            const int i0 = 4 * ((w7 || w0 ? 1 : w) - 1), ii0 = i0 >= 12 ? i0 - 12 : i0;
            ebA = w0 ? xg : (const char*)(ii0 >= 6 ? wB : wA) + head * 64;
            ebB = w0 ? xg : (const char*)(ii0 + 2 >= 6 ? wB : wA) + head * 64;
            estride = w0 ? 128u : 64u * 6144u;
            edst = w0 ? 28 * 1024 : P1_WBASE + ii0 * 1024;
        }
        const unsigned odst = lds0 + (w7 ? 0 : w) * 4096;
        const unsigned mstage = __builtin_amdgcn_readfirstlane(w7 ? 0u : ~0u), mwarm = ~mstage;
        unsigned pf = 0;
        const char* pfw = (const char*)(bid < 128 ? wA : wB);
        unsigned pfo;
        { const int L = (bid & 127) * 24 + (lane < 24 ? lane : 0); pfo = (unsigned)((L / 48) * 6144 + (L % 48) * 128); }
#define P1_PF(base, off) asm volatile("s_mov_b32 exec_lo, %3\n\ts_mov_b32 exec_hi, %3\n\tglobal_load_dword %0, %1, %2\n\ts_mov_b64 exec, -1" : "+v"(pf) : "v"(off), "s"(base), "s"(mwarm) : "memory")
#define P1_GO(og_, od_, j) glds16m(vo[j], og_, (od_) + (j) * 1024, mstage)
#define P1_GE(eA_, eB_, ed_, j) glds16m(ve[j], (j) < 2 ? (eA_) : (eB_), (ed_) + (j) * 1024, mstage)
#define P1_GO2 P1_GO
#define P1_GE2 P1_GE
#define P1_EDST(xo_, h0, h1) (lds0 + edst + (w0 ? (xo_) : (whalf ? (h1) : (h0))))
        u32x4 gx0, gx1, gw0, gw1;
        {
            const char* px = (const char*)x + (size_t)(rt * 256 + head * 8 + w) * 2048;
            const unsigned go = lane * 32, z = 0;
            asm volatile("global_load_dwordx4 %0, %5, %6\n\tglobal_load_dwordx4 %1, %5, %6 offset:16\n\tglobal_load_dwordx4 %2, %5, %7\n\tglobal_load_dwordx4 %3, %5, %7 offset:16\n\t"
                         "global_load_dword %4, %8, %9 sc1"
                         : "=&v"(gx0), "=&v"(gx1), "=&v"(gw0), "=&v"(gw1), "=&v"(token) : "v"(go), "s"(px), "s"(gw), "v"(z), "s"(flags) : "memory");
        }
#pragma unroll
        for (int j = 0; j < 4; ++j) P1_GO(xg, odst, j);
        { const unsigned ed = P1_EDST(0, 0, P1_WSLOT);
#pragma unroll
          for (int j = 0; j < 4; ++j) P1_GE(ebA, ebB, ed, j); }
#pragma unroll
        for (int j = 0; j < 4; ++j) P1_GO(xg + 128, odst + P1_XSLOT, j);
        { const unsigned ed = P1_EDST(P1_XSLOT, 2 * P1_WSLOT, 3 * P1_WSLOT);
#pragma unroll
          for (int j = 0; j < 4; ++j) P1_GE(ebA + estride, ebB + estride, ed, j); }
        ebA += 2 * (size_t)estride; ebB += 2 * (size_t)estride;
        {
            P1_PF(pfw + 2 * (64 * 6144), pfo); P1_PF(pfw + 3 * (64 * 6144), pfo);
            const char* xl = (const char*)x + (size_t)bid * 16384; const unsigned lo = lane * 128;
            P1_PF(xl, lo); P1_PF(xl + 8192, lo);
            P1_PF(pfw + 4 * (64 * 6144), pfo); P1_PF(pfw + 5 * (64 * 6144), pfo);
            if (w7) asm volatile("s_waitcnt vmcnt(6)" ::: "memory");
        }
        const int wm = w >> 1, wn = w & 1, hh1 = lane >> 5;
        unsigned xa[4], wa;
#pragma unroll
        for (int u = 0; u < 4; ++u) xa[u] = lds0 + (wm * 64 + (lane & 31)) * 128 + (((2 * u + hh1) ^ ((lane >> 1) & 7)) << 4);
        wa = lds0 + P1_WBASE + wn * 6144 + (8 * hh1 + ((lane & 15) >> 2)) * 64 + ((((lane >> 4) & 1) ^ hh1) * 32) + (lane & 3) * 8;
#pragma unroll
        for (int i = 0; i < 2; ++i)
#pragma unroll
            for (int j = 0; j < 3; ++j) acc[i][j] = f32x16{};
        bf16x8 xf[2][2][2]; s16x4 wl[2][2][3], wh[2][2][3];
#define P1_WF(b, k, t) cat8(wl[b][k][t], wh[b][k][t])
#define P1_MM(b, k, r, t) acc[r][t] = ((t) < 2) ? MFMA32(P1_WF(b, k, t), xf[b][k][r], acc[r][t]) : MFMA32(xf[b][k][r], P1_WF(b, k, t), acc[r][t])
#define P1_RW(b, wa_, k, t) do { wl[b][k][t] = ldtr((wa_) + (t) * 2048 + (k) * 1024); wh[b][k][t] = ldtr((wa_) + (t) * 2048 + (k) * 1024 + 256); } while (0)
#define P1_STEP(HF, c, n, xo, u, wo, RD, G0, G1, G2, G3) do { \
            const unsigned xa0_ = xa[2 * (u)] + (xo), xa1_ = xa[2 * (u) + 1] + (xo), wa_ = wa + (wo); \
            P1_MM(c, 0, 0, 0); if (RD) { xf[n][0][0] = ld128(xa0_); P1_RW(n, wa_, 0, 0); } if (HF == 0) { G0; } SBAR(); \
            P1_MM(c, 0, 0, 1); if (RD) { P1_RW(n, wa_, 0, 1); } SBAR(); \
            P1_MM(c, 0, 0, 2); if (RD) { P1_RW(n, wa_, 0, 2); } if (HF == 1) { G0; } SBAR(); \
            P1_MM(c, 0, 1, 0); if (RD) { xf[n][0][1] = ld128(xa0_ + 4096); } if (HF == 0) { G1; } SBAR(); \
            P1_MM(c, 0, 1, 1); if (RD) { xf[n][1][0] = ld128(xa1_); } SBAR(); \
            P1_MM(c, 0, 1, 2); if (RD) { P1_RW(n, wa_, 1, 0); } if (HF == 1) { G1; } SBAR(); \
            P1_MM(c, 1, 0, 0); if (RD) { P1_RW(n, wa_, 1, 1); } if (HF == 0) { G2; } SBAR(); \
            P1_MM(c, 1, 0, 1); if (RD) { P1_RW(n, wa_, 1, 2); } SBAR(); \
            P1_MM(c, 1, 0, 2); if (RD) { xf[n][1][1] = ld128(xa1_ + 4096); } if (HF == 1) { G2; } SBAR(); \
            P1_MM(c, 1, 1, 0); if (HF == 0) { G3; } SBAR(); \
            P1_MM(c, 1, 1, 1); if (HF == 1) { G3; } SBAR(); \
            P1_MM(c, 1, 1, 2); SBAR(); } while (0)
        STAMP_AT(1);
        asm volatile("s_waitcnt vmcnt(8) lgkmcnt(0)\n\ts_barrier" : "+v"(gx0), "+v"(gx1), "+v"(gw0), "+v"(gw1), "+v"(token)::"memory");
        STAMP_AT(2);
#pragma unroll
        for (int j = 0; j < 4; ++j) P1_GO(xg + 256, odst + 2 * P1_XSLOT, j);
        {
            float s = 0.f;
#pragma unroll
            for (int i = 0; i < 4; ++i) {
                s += __uint_as_float(gx0[i] << 16) * __uint_as_float(gw0[i] << 16) + __uint_as_float(gx0[i] & 0xffff0000u) * __uint_as_float(gw0[i] & 0xffff0000u);
                s += __uint_as_float(gx1[i] << 16) * __uint_as_float(gw1[i] << 16) + __uint_as_float(gx1[i] & 0xffff0000u) * __uint_as_float(gw1[i] & 0xffff0000u);
            }
            gate = s; PIN(gate);
            token = __builtin_amdgcn_readfirstlane(token) + 1u;
        }
        { xf[0][0][0] = ld128(xa[0]); xf[0][0][1] = ld128(xa[0] + 4096); xf[0][1][0] = ld128(xa[1]); xf[0][1][1] = ld128(xa[1] + 4096);
#pragma unroll
          for (int n = 0; n < 6; ++n) P1_RW(0, wa, n / 3, n % 3); }
        LBAR();
        unsigned xo0 = 0, xo1 = P1_XSLOT, xo2 = 2 * P1_XSLOT;
        unsigned wo0 = 0, wo1 = P1_WSLOT, wo2 = 2 * P1_WSLOT, wo3 = 3 * P1_WSLOT, wo4 = 4 * P1_WSLOT;
#define P1_TILE(HF, T, IE, IO, WA, WB, RDLAST, PFX) do { \
            const unsigned ed_ = P1_EDST(xo2, wo4, wo0), od_ = odst + xo0; const char* og_ = xg + ((T) + 3) * 128; \
            P1_STEP(HF, 0, 1, xo0, 1, wo1, P1_RD0, if (IE) P1_GE2(ebA, ebB, ed_, 0), if (IE) P1_GE2(ebA, ebB, ed_, 1), if (IE) P1_GE2(ebA, ebB, ed_, 2), if (IE) P1_GE2(ebA, ebB, ed_, 3)); \
            WA; \
            PFX; \
            P1_STEP(HF, 1, 0, xo1, 0, wo2, RDLAST, if (IO) P1_GO2(og_, od_, 0), if (IO) P1_GO2(og_, od_, 1), if (IO) P1_GO2(og_, od_, 2), if (IO) P1_GO2(og_, od_, 3)); \
            WB; \
            ebA += estride; ebB += estride; \
            { const unsigned t_ = xo0; xo0 = xo1; xo1 = xo2; xo2 = t_; const unsigned a_ = wo0, b_ = wo1; wo0 = wo2; wo1 = wo3; wo2 = wo4; wo3 = a_; wo4 = b_; } } while (0)
#define P1_WB(N) WAIT_BAR(N)
#define P1_RDL 1
#define P1_RD0 1
#define P1_LOOP(HF) do { \
            for (int T = 0; T < 13; ++T) { \
                  \
                const char* pb = T < 10 ? pfw + (size_t)(T + 6) * (64 * 6144) : (const char*)(bid < 128 ? woA : woB) + (bid & 127) * 16384 + (T & 1) * 8192; \
                const unsigned po = T < 10 ? pfo : lane * 128; \
                P1_TILE(HF, T, 1, 1, P1_WB(8), P1_WB(12), P1_RDL, if (HF == 1) P1_PF(pb, po)); \
            } \
            P1_TILE(HF, 13, 1, 0, WAIT_BAR(8), WAIT_BAR(8), 1, NOP_); \
            P1_TILE(HF, 14, 0, 0, WAIT_BAR(0), NOP_, 1, NOP_); \
            P1_TILE(HF, 15, 0, 0, NOP_, NOP_, 0, NOP_); } while (0)
        if (w < 4) P1_LOOP(0); else P1_LOOP(1);
        asm volatile("s_waitcnt vmcnt(0)" : "+v"(pf)::"memory");
#undef P1_LOOP
#undef P1_TILE
#undef P1_STEP
#undef P1_RW
#undef P1_MM
#undef P1_WF
#undef P1_GO
#undef P1_GE
#undef P1_EDST
#undef P1_PF
    }
    STAMP_AT(3);
#pragma unroll
    for (int o = 32; o >= 1; o >>= 1) gate += __shfl_xor(gate, o);
    gate = 1.0f / (1.0f + __expf(-gate));
    LBAR();
    u32x4 keep[8];
    const unsigned mB = __builtin_amdgcn_readfirstlane((w & 1) ? ~0u : 0u), mA = ~mB;
    char* kvg = ws + WS_KV + (size_t)(((w & 1) * 32 + head) * 8 + rt) * 32768;
    const unsigned kvo = (w >> 1) * 4096 + l16;
    {
        const int wn = w & 1;
        const unsigned kl = lds0 + (wn ? P2_B : P2_AK + 16384), vl = lds0 + (wn ? P2_B + 16384 : P2_AV + 16384), ql_ = lds0 + P2_Q + wn * 16384;
#pragma unroll
        for (int r = 0; r < 2; ++r)
#pragma unroll
            for (int s = 0; s < 2; ++s) {
                const unsigned o = kvo + r * 2048 + s * 1024;
                keep[r * 2 + s] = __builtin_bit_cast(u32x4, pack8(acc[r][1], s, 1.0f)); keep[4 + r * 2 + s] = __builtin_bit_cast(u32x4, pack8(acc[r][2], s, 1.0f));
                st16_wtm(o, kvg, keep[r * 2 + s], mB); st16_wtm(16384 + o, kvg, keep[4 + r * 2 + s], mB);
            }
#pragma unroll
        for (int r = 0; r < 2; ++r)
#pragma unroll
            for (int s = 0; s < 2; ++s) {
                const unsigned o = kvo + r * 2048 + s * 1024;
                st128(kl + o, keep[r * 2 + s]); st128(vl + o, keep[4 + r * 2 + s]);
                st128(ql_ + o, __builtin_bit_cast(u32x4, pack8(acc[r][0], s, QSCALE)));
            }
    }
    LBAR();
    STAMP_AT(4);

    const int rb = rt * 8 + w;
    const int ql = lane & 31, hh = lane >> 5;
    const char* kvA = ws + WS_KV + (size_t)(head * 8) * 32768;
    const char* kvB = ws + WS_KV + (size_t)((32 + head) * 8) * 32768;
    unsigned* fKB = flags + 64 + head * 8; unsigned* fO = flags + 64 + 256; unsigned* fKA = flags + 64 + 512 + head * 8;
    const int rtp = rt > 0 ? rt - 1 : 0;
    const unsigned* fKAp = fKA + rtp;
    bf16x8 sel;
    { const short o = (((lane >> 4) & 1) == ((lane >> 2) & 1)) ? (short)0x3F80 : (short)0;
#pragma unroll
      for (int i = 0; i < 8; ++i) sel[i] = o; }
    const bf16x8 qa0 = ld128(lds0 + P2_Q + w * 2048 + l16), qa1 = ld128(lds0 + P2_Q + w * 2048 + 1024 + l16);
    const bf16x8 qb0 = ld128(lds0 + P2_Q + 16384 + w * 2048 + l16), qb1 = ld128(lds0 + P2_Q + 16384 + w * 2048 + 1024 + l16);
    char* og = ws + WS_O + ((size_t)rb * 64 + head) * 2048;
    const unsigned oo = (ql >> 4) * 1024 + (hh * 16 + (ql & 15)) * 16;
    const int rt3 = (xcd & 3) * 4 + (idx & 3), ct = (xcd >> 2) * 8 + (idx >> 2);
    const int p3 = w & 3, half = w >> 2;
    const char* wsrc = (const char*)(half ? woB : woA) + ct * 128;
    const unsigned vw0 = (unsigned)((16 * p3 + (lane >> 3)) * 2048 + (((lane & 7) ^ (((lane >> 4) & 3) << 1)) << 4)), vw1 = vw0 + 8 * 2048;
    const unsigned wdst3 = lds0 + half * 8192 + p3 * 2048;
#define P3_SOFF(s) ((s) < 6 ? 65536 + (s) * P3_SLOT : (s) < 10 ? ((s) - 6) * P3_SLOT : 65536 + ((s) - 10) * P3_SLOT)
#define P3_GW(st, i) glds16((i) ? vw1 : vw0, wsrc + (size_t)(st) * (64 * 2048), wdst3 + P3_SOFF(st) + (i) * 1024)

#define EX(x) __builtin_amdgcn_exp2f(x)
#define P8(x) __builtin_bit_cast(bf16x8, x)
#define STEP(SC, SN, Q0, Q1, AK0, AK1, AV1, AV0, X) do { \
        SN = MFMA32(k0, Q0, negm); k0 = ld128(AK0); \
        phi[2] = cvtpk(t0, t1); phi[3] = cvtpk(t2, t3); t0 = EX(SC[0]); t1 = EX(SC[1]); PIN(phi); PIN(t0); PIN(t1); SBAR(); \
        O = MFMA32(v1, P8(phi), O); v1 = ld128(AV1); \
        t2 = EX(SC[2]); t3 = EX(SC[3]); plo[0] = cvtpk(t0, t1); PIN(t2); PIN(t3); PIN(plo); SBAR(); \
        L = MFMA16(sel, P8(phi), L); \
        plo[1] = cvtpk(t2, t3); t0 = EX(SC[4]); PIN(plo); PIN(t0); SBAR(); \
        SN = MFMA32(k1, Q1, SN); k1 = ld128(AK1); \
        t1 = EX(SC[5]); t2 = EX(SC[6]); t3 = EX(SC[7]); PIN(t1); PIN(t2); PIN(t3); SBAR(); \
        X; SBAR(); \
        plo[2] = cvtpk(t0, t1); t0 = EX(SC[8]); plo[3] = cvtpk(t2, t3); t1 = EX(SC[9]); PIN(plo); PIN(t0); PIN(t1); SBAR(); \
        O = MFMA32(v0, P8(plo), O); v0 = ld128(AV0); \
        t2 = EX(SC[10]); t3 = EX(SC[11]); phi[0] = cvtpk(t0, t1); PIN(t2); PIN(t3); PIN(phi); SBAR(); \
        L = MFMA16(sel, P8(plo), L); \
        phi[1] = cvtpk(t2, t3); \
        t0 = EX(SC[12]); t1 = EX(SC[13]); t2 = EX(SC[14]); t3 = EX(SC[15]); PIN(phi); PIN(t0); PIN(t1); PIN(t2); PIN(t3); SBAR(); \
    } while (0)
#define SG_OFF(i) (((i) >> 1) * 16384 + ((i) & 1) * 1024)
#define SG_LD(i, src, dK, dV) glds16c(l16, (src) + w * 2048 + SG_OFF(i), ((i) >> 1 ? (dV) : (dK)) + w * 2048 + ((i) & 1) * 1024)
    {
        f32x16 O = {}, negm, SA, SB; f32x4 L = {};
        bf16x8 k0, k1, v0, v1; u32x4 plo, phi; float t0 = 0.f, t1 = 0.f, t2 = 0.f, t3 = 0.f;
        unsigned cur = lds0 + P2_B + l16, nxt = cur + P2_SLOT;
        {
            k0 = ld128(cur); k1 = ld128(cur + 1024);
            SA = f32x16{}; SA = MFMA32(k0, qb0, SA); SA = MFMA32(k1, qb1, SA);
            const float m = rowmax16(SA);
#pragma unroll
            for (int r = 0; r < 16; ++r) { negm[r] = -m; SA[r] -= m; }
            k0 = ld128(cur + 2048); k1 = ld128(cur + 2048 + 1024);
            v0 = ld128(cur + 16384);
            v1 = bf16x8{}; phi = u32x4{}; plo = u32x4{};
        }
#define KAD(j, s) (((j) < 8 ? cur + (j) * 2048 : nxt + ((j) - 8) * 2048) + (s) * 1024)
#define BSTEP(SC, SN, j, X) STEP(SC, SN, qb0, qb1, KAD((j) + 2, 0), KAD((j) + 2, 1), KAD(j, 1) + 16384, KAD((j) + 1, 0) + 16384, X)
#define KEEP_A(i) st16_wtm(kvo + ((i) >> 2) * 16384 + (((i) >> 1) & 1) * 2048 + ((i) & 1) * 1024, kvg, keep[i], mA)
        unsigned pv;
        BSTEP(SA, SB, 0, NOP_);
        STAMP_AT(5);
        WAIT_BAR(0);
        STAMP_AT(6);
        if (tid == 0) flag_set(fKB + rt, token);
        BSTEP(SB, SA, 1, POLL_ISSUE(pv, fKB, 8));
        { const char* s1 = kvB + ((rt + 1) & 7) * 32768; const unsigned d = lds0 + P2_B + P2_SLOT;
          BSTEP(SA, SB, 2, POLL_CHECK(pv, fKB, 8); SG_LD(0, s1, d, d + 16384); SG_LD(1, s1, d, d + 16384); SG_LD(2, s1, d, d + 16384); SG_LD(3, s1, d, d + 16384)); }
        STAMP_AT(7);
        BSTEP(SB, SA, 3, KEEP_A(0); KEEP_A(1); KEEP_A(2); KEEP_A(3));
        BSTEP(SA, SB, 4, KEEP_A(4); KEEP_A(5); KEEP_A(6); KEEP_A(7));
        STAMP_AT(8);
        BSTEP(SB, SA, 5, NOP_);
        STAMP_AT(9);
        WAIT_BAR(0);
        STAMP_AT(10);
        if (tid == 0) flag_set(fKA + rt, token);
        int slot = 0;
        for (int i = 0; i < 7; ++i) {
            const int s1 = slot == 2 ? 0 : slot + 1, s2 = s1 == 2 ? 0 : s1 + 1;
            const char* src = i < 6 ? kvB + ((rt + i + 2) & 7) * 32768 : kvA + rtp * 32768;
            const unsigned dK = i < 6 ? lds0 + P2_B + s2 * P2_SLOT : lds0 + P2_AK, dV = i < 6 ? dK + 16384 : lds0 + P2_AV;
            if (i == 6) POLL_CHECK(pv, fKAp, 1);
            BSTEP(SA, SB, 6, SG_LD(0, src, dK, dV)); BSTEP(SB, SA, 7, SG_LD(1, src, dK, dV));
            cur = nxt; nxt = lds0 + P2_B + s2 * P2_SLOT + l16;
            BSTEP(SA, SB, 0, SG_LD(2, src, dK, dV)); BSTEP(SB, SA, 1, SG_LD(3, src, dK, dV)); BSTEP(SA, SB, 2, NOP_);
            BSTEP(SB, SA, 3, NOP_);
            BSTEP(SA, SB, 4, if (i == 5) { POLL_ISSUE(pv, fKAp, 1); });
            BSTEP(SB, SA, 5, NOP_);
            WAIT_BAR(0);
            slot = s1;
        }
        BSTEP(SA, SB, 6, NOP_); BSTEP(SB, SA, 7, NOP_);
        phi[2] = cvtpk(t0, t1); phi[3] = cvtpk(t2, t3);
        O = MFMA32(v1, P8(phi), O); L = MFMA16(sel, P8(phi), L);
        float l = L[0];
        if (!unit_ok(O, l)) attn_safe<false>(kvB, qb0, qb1, rb, lane, O, l);
        const float f = 1.0f / l;
        st16_wt(oo, og + 32 * 2048, __builtin_bit_cast(u32x4, pack8(O, 0, f))); st16_wt(oo + 512, og + 32 * 2048, __builtin_bit_cast(u32x4, pack8(O, 1, f)));
#undef KEEP_A
#undef BSTEP
#undef KAD
    }
    STAMP_AT(11);
    LBAR();
    {
        f32x16 O = {}, negm, SA, SB; f32x4 L = {};
        bf16x8 k0, k1, v0, v1; u32x4 plo, phi; float t0 = 0.f, t1 = 0.f, t2 = 0.f, t3 = 0.f;
        const unsigned ab = lds0 + P2_AK + w * 2048 + l16;
        const int jmax = rt > 0 ? 8 : w;
#define AAD(j, s) (ab + (8 - ((j) > 8 ? 8 : (j))) * 2048 + (s) * 1024)
        {
            k0 = ld128(AAD(0, 0)); k1 = ld128(AAD(0, 1));
            SA = f32x16{}; SA = MFMA32(k0, qa0, SA); SA = MFMA32(k1, qa1, SA);
#pragma unroll
            for (int r = 0; r < 16; ++r) if (keyof(r, hh) > ql) SA[r] = -INFINITY;
            const float m = rowmax16(SA);
#pragma unroll
            for (int r = 0; r < 16; ++r) { negm[r] = -m; SA[r] -= m; }
            k0 = ld128(AAD(1, 0)); k1 = ld128(AAD(1, 1));
            v0 = ld128(AAD(0, 0) + 32768);
            v1 = bf16x8{}; phi = u32x4{}; plo = u32x4{};
        }
#define ASTEP(SC, SN, j, X) do { if ((j) + 1 > jmax) { _Pragma("unroll") for (int r_ = 0; r_ < 16; ++r_) negm[r_] = -INFINITY; } \
        STEP(SC, SN, qa0, qa1, AAD((j) + 2, 0), AAD((j) + 2, 1), AAD(j, 1) + 32768, AAD((j) + 1, 0) + 32768, X); } while (0)
        ASTEP(SA, SB, 0, P3_GW(0, 0); P3_GW(0, 1)); ASTEP(SB, SA, 1, P3_GW(1, 0); P3_GW(1, 1)); ASTEP(SA, SB, 2, P3_GW(2, 0); P3_GW(2, 1)); ASTEP(SB, SA, 3, P3_GW(3, 0); P3_GW(3, 1));
        ASTEP(SA, SB, 4, P3_GW(4, 0); P3_GW(4, 1)); ASTEP(SB, SA, 5, P3_GW(5, 0); P3_GW(5, 1)); ASTEP(SA, SB, 6, NOP_); ASTEP(SB, SA, 7, NOP_);
#pragma unroll
        for (int r = 0; r < 16; ++r) if (keyof(r, hh) <= ql) SA[r] = -INFINITY;
        ASTEP(SA, SB, 8, NOP_);
        phi[2] = cvtpk(t0, t1); phi[3] = cvtpk(t2, t3);
        O = MFMA32(v1, P8(phi), O); L = MFMA16(sel, P8(phi), L);
        float l = L[0];
        if (!unit_ok(O, l)) attn_safe<true>(kvA, qa0, qa1, rb, lane, O, l);
        const float f = 1.0f / l;
        st16_wt(oo, og, __builtin_bit_cast(u32x4, pack8(O, 0, f))); st16_wt(oo + 512, og, __builtin_bit_cast(u32x4, pack8(O, 1, f)));
        if (lane == 0) __hip_atomic_store((GAS float*)(ws + WS_G) + rt * 256 + head * 8 + w, gate, __ATOMIC_RELAXED, __HIP_MEMORY_SCOPE_AGENT);
#undef ASTEP
#undef AAD
    }
#undef STEP
#undef EX
#undef P8
#undef SG_LD
#undef SG_OFF
    STAMP_AT(12);
    WAIT_BAR(0);
    if (tid == 0) flag_set(fO + rt * 32 + head, token);
    STAMP_AT(13);

    {
#pragma unroll
        for (int st = 6; st < 10; ++st) { P3_GW(st, 0); P3_GW(st, 1); }
        poll_spin(fO + (rt3 >> 1) * 32, 32, token, lane);
        STAMP_AT(14);
        const char* osrc = ws + WS_O + ((size_t)(rt3 * 4 + p3) * 64 + half * 32) * 2048;
        bf16x8 of[8][4];
#define SC1_ " sc1"
#define P3_GO(st, i) asm volatile("global_load_dwordx4 %0, %1, %2 offset:" #i "*1024" SC1_ : "=v"(of[(st) & 7][i]) : "v"(l16), "s"(osrc + (st) * 4096) : "memory")
#define P3_GO4(st) do { P3_GO(st, 0); P3_GO(st, 1); P3_GO(st, 2); P3_GO(st, 3); } while (0)
        P3_GO4(0); P3_GO4(1); P3_GO4(2); P3_GO4(3); P3_GO4(4); P3_GO4(5); P3_GO4(6);
        float gq[2];
        { const float* gp = (const float*)(ws + WS_G) + rt3 * 128 + p3 * 32; const unsigned go = (lane & 15) * 4;
          asm volatile("global_load_dword %0, %2, %3 sc1\n\tglobal_load_dword %1, %2, %3 offset:64 sc1" : "=&v"(gq[0]), "=&v"(gq[1]) : "v"(go), "s"(gp) : "memory"); }
        const int g4 = lane >> 4, q4 = (lane & 15) >> 2;
        unsigned wa[4];
        { const unsigned a0 = (16 * (g4 >> 1) + 4 * (g4 & 1) + q4) * 128 + ((2 * (g4 & 1) + (q4 >> 1)) * 32) + (lane & 3) * 8;
#pragma unroll
          for (int n = 0; n < 4; ++n) wa[n] = lds0 + half * 8192 + (a0 ^ (n << 5)) + P3_SOFF(0); }
        f32x4 ac[2][4];
#pragma unroll
        for (int i = 0; i < 2; ++i)
#pragma unroll
            for (int j = 0; j < 4; ++j) ac[i][j] = f32x4{};
        s16x4 wl[2][4], wh[2][4];
#define P3_RW(buf, k_, n_) do { wl[buf][n_] = ldtr(wa[n_] + (k_) * 4096); wh[buf][n_] = ldtr(wa[n_] + (k_) * 4096 + 1024); } while (0)
#define P3_K32(st, k_, X0, X1, X2, X3) do { \
            if ((k_) == 1 && (st) + 1 < 16) { _Pragma("unroll") for (int n_ = 0; n_ < 4; ++n_) { wa[n_] += (unsigned)(P3_SOFF((st) + 1) - P3_SOFF(st)); PIN(wa[n_]); } } \
            _Pragma("unroll") for (int n_ = 0; n_ < 4; ++n_) { \
                const bf16x8 wf_ = cat8(wl[k_][n_], wh[k_][n_]); \
                ac[0][n_] = MFMA16(wf_, of[(st) & 7][2 * (k_)], ac[0][n_]); ac[1][n_] = MFMA16(wf_, of[(st) & 7][2 * (k_) + 1], ac[1][n_]); \
                if ((k_) == 0) P3_RW(1, 1, n_); else if ((st) + 1 < 16) P3_RW(0, 0, n_); \
                if (n_ == 0) { X0; } if (n_ == 1) { X1; } if (n_ == 2) { X2; } if (n_ == 3) { X3; } \
                SBAR(); } } while (0)
#define P3_OW(st, NW) asm volatile("s_waitcnt vmcnt(" #NW ")" : "+v"(of[(st) & 7][0]), "+v"(of[(st) & 7][1]), "+v"(of[(st) & 7][2]), "+v"(of[(st) & 7][3])::"memory")
#define P3_STAGE(st, NW, BAR) do { \
            P3_OW(st, NW); if (BAR) LBAR(); \
            P3_K32(st, 0, NOP_, NOP_, NOP_, NOP_); \
            if ((st) + 7 < 16) { P3_K32(st, 1, P3_GO((st) + 7, 0), P3_GO((st) + 7, 1), P3_GO((st) + 7, 2), P3_GO((st) + 7, 3)); } \
            else { P3_K32(st, 1, NOP_, NOP_, NOP_, NOP_); } \
        } while (0)
        LBAR();
#pragma unroll
        for (int n_ = 0; n_ < 4; ++n_) P3_RW(0, 0, n_);
        P3_STAGE(0, 26, 0); P3_STAGE(1, 26, 0); P3_STAGE(2, 26, 0);
        P3_OW(3, 26); LBAR();
        P3_K32(3, 0, P3_GW(10, 0), P3_GW(10, 1), P3_GW(11, 0), P3_GW(11, 1)); P3_K32(3, 1, P3_GW(12, 0); P3_GW(12, 1); P3_GO(10, 0), P3_GO(10, 1), P3_GO(10, 2), P3_GO(10, 3));
        P3_STAGE(4, 32, 0); P3_STAGE(5, 32, 0);
        P3_OW(6, 32); LBAR();
        P3_K32(6, 0, P3_GW(13, 0), P3_GW(13, 1), P3_GW(14, 0), P3_GW(14, 1)); P3_K32(6, 1, P3_GW(15, 0); P3_GW(15, 1); P3_GO(13, 0), P3_GO(13, 1), P3_GO(13, 2), P3_GO(13, 3));
        P3_STAGE(7, 36, 0); P3_STAGE(8, 36, 0);
        P3_STAGE(9, 30, 1); P3_STAGE(10, 26, 0); P3_STAGE(11, 22, 0);
        P3_STAGE(12, 12, 1); P3_STAGE(13, 8, 0); P3_STAGE(14, 4, 0); P3_STAGE(15, 0, 0);
        asm volatile("" : "+v"(gq[0]), "+v"(gq[1]));
        const float f0 = half ? 1.0f - gq[0] : gq[0], f1 = half ? 1.0f - gq[1] : gq[1];
#pragma unroll
        for (int n = 0; n < 4; ++n) { ac[0][n] *= f0; ac[1][n] *= f1; }
        LBAR();
        LAS f32x4* mine = (LAS f32x4*)(uintptr_t)(lds0 + w * 4096 + l16);
        LAS f32x4* theirs = (LAS f32x4*)(uintptr_t)(lds0 + (w ^ 4) * 4096 + l16);
#pragma unroll
        for (int q = 0; q < 2; ++q) { mine[(q * 2) * 64] = half ? ac[q][0] : ac[q][2]; mine[(q * 2 + 1) * 64] = half ? ac[q][1] : ac[q][3]; }
        LBAR();
#pragma unroll
        for (int q = 0; q < 2; ++q)
#pragma unroll
            for (int e = 0; e < 2; ++e) {
                const f32x4 r = (half ? ac[q][2 + e] : ac[q][e]) + theirs[(q * 2 + e) * 64];
                u16* op = out + (size_t)(rt3 * 128 + p3 * 32 + q * 16 + (lane & 15)) * 1024 + ct * 64 + half * 32 + e * 16 + g4 * 4;
                *(u32x2*)op = u32x2{cvtpk(r[0], r[1]), cvtpk(r[2], r[3])};
            }
#undef P3_GO
#undef P3_GO4
#undef P3_RW
#undef P3_K32
#undef P3_OW
#undef P3_STAGE
    }
#undef P3_GW
#undef P3_SOFF
    if (bid == 0 && w == 0) {
        const unsigned long long t0 = __builtin_amdgcn_s_memrealtime();
        for (;;) {
            bool ok = true;
#pragma unroll
            for (int i = 0; i < 4; ++i) ok = ok && (__hip_atomic_load((GAS unsigned*)(fO + i * 64 + lane), __ATOMIC_RELAXED, __HIP_MEMORY_SCOPE_AGENT) == token);
            if (__all(ok)) break;
            if (__builtin_amdgcn_s_memrealtime() - t0 > 400000ull) break;
            __builtin_amdgcn_s_sleep(1);
        }
        if (lane == 0) flag_set(flags, token);
    }
    STAMP_AT(15);
    STAMP_FLUSH(ws, 16);
}

extern "C" void kernel_launch(void* const* d_in, const int* in_sizes, int n_in, void* d_out, int out_size, void* d_ws, size_t ws_size, hipStream_t stream) {
    (void)in_sizes; (void)n_in; (void)out_size; (void)ws_size;
    const u16* x = (const u16*)d_in[0]; const u16* wqkvA = (const u16*)d_in[1]; const u16* woA = (const u16*)d_in[2];
    const u16* wqkvB = (const u16*)d_in[4]; const u16* woB = (const u16*)d_in[5]; const u16* gw = (const u16*)d_in[6];
    (void)hipFuncSetAttribute((const void*)k_fused, hipFuncAttributeMaxDynamicSharedMemorySize, LDS_BYTES);
    k_fused<<<256, 512, LDS_BYTES, stream>>>(x, wqkvA, wqkvB, woA, woB, gw, (char*)d_ws, (u16*)d_out);
}
```

```cpp
#include <hip/hip_runtime.h>
#include <hip/hip_bf16.h>
#include <stdint.h>
#pragma clang diagnostic ignored "-Winline-asm"

typedef unsigned short u16;
typedef short bf16x8 __attribute__((ext_vector_type(8)));
typedef short s16x4 __attribute__((ext_vector_type(4)));
typedef float f32x16 __attribute__((ext_vector_type(16)));
typedef float f32x4 __attribute__((ext_vector_type(4)));
typedef float f32x2 __attribute__((ext_vector_type(2)));
typedef __bf16 bf16x2_t __attribute__((ext_vector_type(2)));
typedef unsigned u32x4 __attribute__((ext_vector_type(4)));
typedef unsigned u32x2 __attribute__((ext_vector_type(2)));
#define LAS __attribute__((address_space(3)))
#define GAS __attribute__((address_space(1)))
#define DI __device__ __forceinline__
#define MFMA32(a, b, c) __builtin_amdgcn_mfma_f32_32x32x16_bf16((a), (b), (c), 0, 0, 0)
#define MFMA16(a, b, c) __builtin_amdgcn_mfma_f32_16x16x32_bf16((a), (b), (c), 0, 0, 0)
#define SBAR() __builtin_amdgcn_sched_barrier(0)
#define PIN(x) asm volatile("" : "+v"(x))
#define WAITCNT_VM_LGKM0(N) __builtin_amdgcn_s_waitcnt(((N) & 15) | (((N) >> 4) << 14) | 0x70)
#define WAIT_BAR(N) do { asm volatile("" ::: "memory"); WAITCNT_VM_LGKM0(N); __builtin_amdgcn_s_barrier(); asm volatile("" ::: "memory"); } while (0)
#define LBAR() WAIT_BAR(63)
#define NOP_ do {} while (0)

constexpr float QSCALE = 0.17677669529663687f * 1.4426950408889634f;
constexpr size_t WS_KV = 0;
constexpr size_t WS_O = 16u << 20;
constexpr size_t WS_G = 24u << 20;
constexpr size_t WS_F = (24u << 20) + 8192;
constexpr int LDS_BYTES = 163840;

DI unsigned cvtpk(float lo, float hi) { f32x2 v = {lo, hi}; bf16x2_t b = __builtin_convertvector(v, bf16x2_t); return __builtin_bit_cast(unsigned, b); }
DI bf16x8 pack8(const f32x16& x, int s, float sc) {
    u32x4 w = {cvtpk(x[8 * s] * sc, x[8 * s + 1] * sc), cvtpk(x[8 * s + 2] * sc, x[8 * s + 3] * sc), cvtpk(x[8 * s + 4] * sc, x[8 * s + 5] * sc), cvtpk(x[8 * s + 6] * sc, x[8 * s + 7] * sc)};
    return __builtin_bit_cast(bf16x8, w);
}
DI u32x4 pack44(const f32x4& a, const f32x4& b, float sc) { return u32x4{cvtpk(a[0] * sc, a[1] * sc), cvtpk(a[2] * sc, a[3] * sc), cvtpk(b[0] * sc, b[1] * sc), cvtpk(b[2] * sc, b[3] * sc)}; }
DI void glds16(unsigned voff, const void* sbase, unsigned lds_dst) {
    asm volatile("s_mov_b32 m0, %2\n\ts_nop 0\n\tglobal_load_lds_dwordx4 %0, %1" ::"v"(voff), "s"(sbase), "s"(lds_dst) : "memory", "m0");
}
DI void glds16c(unsigned voff, const void* sbase, unsigned lds_dst) {
    asm volatile("s_mov_b32 m0, %2\n\ts_nop 0\n\tglobal_load_lds_dwordx4 %0, %1 sc1" ::"v"(voff), "s"(sbase), "s"(lds_dst) : "memory", "m0");
}
DI void glds16m(unsigned voff, const void* sbase, unsigned lds_dst, unsigned mask) {
    asm volatile("s_mov_b32 m0, %2\n\ts_mov_b32 exec_lo, %3\n\ts_mov_b32 exec_hi, %3\n\tglobal_load_lds_dwordx4 %0, %1\n\ts_mov_b64 exec, -1" ::"v"(voff), "s"(sbase), "s"(lds_dst), "s"(mask) : "memory", "m0");
}
DI void st16_wt(unsigned voff, void* sbase, u32x4 v) { asm volatile("global_store_dwordx4 %0, %1, %2 sc1\n\ts_nop 1" ::"v"(voff), "v"(v), "s"(sbase) : "memory"); }
DI void st16_wtm(unsigned voff, void* sbase, u32x4 v, unsigned mask) {
    asm volatile("s_mov_b32 exec_lo, %3\n\ts_mov_b32 exec_hi, %3\n\tglobal_store_dwordx4 %0, %1, %2 sc1\n\ts_mov_b64 exec, -1\n\ts_nop 0" ::"v"(voff), "v"(v), "s"(sbase), "s"(mask) : "memory");
}
typedef short v4i16_t __attribute__((ext_vector_type(4)));
DI s16x4 ldtr(unsigned a) { return __builtin_bit_cast(s16x4, __builtin_amdgcn_ds_read_tr16_b64_v4i16((LAS v4i16_t*)(uintptr_t)a)); }
DI bf16x8 ld128(unsigned a) { return *(const LAS bf16x8*)(uintptr_t)a; }
DI void st128(unsigned a, u32x4 v) { *(LAS u32x4*)(uintptr_t)a = v; }
DI bf16x8 cat8(s16x4 lo, s16x4 hi) { return __builtin_shufflevector(lo, hi, 0, 1, 2, 3, 4, 5, 6, 7); }

#define STAMP_DECL do {} while (0)
#define STAMP_AT(i) do {} while (0)
#define STAMP_FLUSH(wsp, n) do {} while (0)

DI void flag_set(unsigned* f, unsigned v) { __hip_atomic_store((GAS unsigned*)f, v, __ATOMIC_RELAXED, __HIP_MEMORY_SCOPE_AGENT); }
DI void poll_spin(const unsigned* f, int n, unsigned want, int lane) {
    const unsigned long long t0 = __builtin_amdgcn_s_memrealtime();
    for (;;) {
        unsigned v = want;
        if (lane < n) v = __hip_atomic_load((GAS unsigned*)(f + lane), __ATOMIC_RELAXED, __HIP_MEMORY_SCOPE_AGENT);
        if (__all(v == want)) break;
        if (__builtin_amdgcn_s_memrealtime() - t0 > 400000ull) break;
        __builtin_amdgcn_s_sleep(1);
    }
    __builtin_amdgcn_fence(__ATOMIC_ACQUIRE, "wavefront");
}
#define POLL_ISSUE(pv, f, n) do { const unsigned* p_ = (f) + (lane < (n) ? lane : 0); asm volatile("global_load_dword %0, %1, off sc1" : "=v"(pv) : "v"(p_) : "memory"); } while (0)
#define POLL_CHECK(pv, f, n) do { asm volatile("s_waitcnt vmcnt(0)" : "+v"(pv)::"memory"); if (!__all((pv) == token)) poll_spin(f, n, token, lane); } while (0)

DI float xhalf_max(float m) { auto rr = __builtin_amdgcn_permlane32_swap(__float_as_uint(m), __float_as_uint(m), false, false); return fmaxf(__uint_as_float(rr[0]), __uint_as_float(rr[1])); }
DI float xhalf_sum(float m) { auto rr = __builtin_amdgcn_permlane32_swap(__float_as_uint(m), __float_as_uint(m), false, false); return __uint_as_float(rr[0]) + __uint_as_float(rr[1]); }
DI int keyof(int r, int h) { return (r & 3) + 8 * (r >> 2) + 4 * h; }
DI float rowmax16(const f32x16& s) {
    float a = fmaxf(fmaxf(s[0], s[1]), s[2]), b = fmaxf(fmaxf(s[3], s[4]), s[5]);
#pragma unroll
    for (int r = 6; r < 16; r += 2) a = fmaxf(fmaxf(a, s[r]), s[r + 1]);
    return xhalf_max(fmaxf(a, b));
}
DI bf16x8 packp(const f32x16& p, int s) {
    u32x4 w = {cvtpk(p[8 * s], p[8 * s + 1]), cvtpk(p[8 * s + 2], p[8 * s + 3]), cvtpk(p[8 * s + 4], p[8 * s + 5]), cvtpk(p[8 * s + 6], p[8 * s + 7])};
    return __builtin_bit_cast(bf16x8, w);
}
template <bool WIN>
DI void attn_safe(const char* kv, bf16x8 q0, bf16x8 q1, int rb, int lane, f32x16& Oout, float& lout) {
    const int ql = lane & 31, h = lane >> 5;
    __builtin_amdgcn_fence(__ATOMIC_ACQUIRE, "agent");
    asm volatile("s_waitcnt vmcnt(0)" ::: "memory");
    float m = -INFINITY, l = 0.f; f32x16 O = {};
    const int nb = WIN ? (rb < 8 ? rb + 1 : 9) : 64;
    for (int j = 0; j < nb; ++j) {
        const int kb = WIN ? rb - j : j;
        const char* kp = kv + (kb >> 3) * 32768 + (kb & 7) * 2048 + lane * 16;
        const bf16x8 k0 = *(const bf16x8*)kp, k1 = *(const bf16x8*)(kp + 1024), v0 = *(const bf16x8*)(kp + 16384), v1 = *(const bf16x8*)(kp + 16384 + 1024);
        f32x16 s = {}; s = MFMA32(k0, q0, s); s = MFMA32(k1, q1, s);
        if (WIN) {
#pragma unroll
            for (int r = 0; r < 16; ++r) { const int kl = keyof(r, h); if ((j == 0 && kl > ql) || (j == 8 && kl <= ql)) s[r] = -INFINITY; }
        }
        const float mn = fmaxf(m, rowmax16(s));
        const float al = __builtin_amdgcn_exp2f(m - mn); m = mn;
        float ps = 0.f;
#pragma unroll
        for (int r = 0; r < 16; ++r) { s[r] = __builtin_amdgcn_exp2f(s[r] - mn); }
        const bf16x8 p0 = packp(s, 0), p1 = packp(s, 1);
#pragma unroll
        for (int r = 0; r < 8; ++r) { ps += __uint_as_float((unsigned)(u16)p0[r] << 16) + __uint_as_float((unsigned)(u16)p1[r] << 16); }
        l = l * al + ps;
#pragma unroll
        for (int r = 0; r < 16; ++r) O[r] *= al;
        O = MFMA32(v0, p0, O); O = MFMA32(v1, p1, O);
    }
    Oout = O; lout = xhalf_sum(l);
}
DI bool unit_ok(const f32x16& O, float l) {
    bool ok = l <= 1.0995116e12f;
    float a = 0.f;
#pragma unroll
    for (int r = 0; r < 16; ++r) a = fmaxf(a, __builtin_fabsf(O[r]));
    bool okO = a <= 3.0e38f;
#pragma unroll
    for (int r = 0; r < 16; ++r) okO = okO && (O[r] == O[r]);
    return __all(ok && okO);
}

constexpr int P1_XSLOT = 32768, P1_WBASE = 3 * P1_XSLOT, P1_WSLOT = 12288;
constexpr int P2_AK = 0, P2_AV = 32768, P2_B = 65536, P2_SLOT = 32768;
constexpr int P2_Q = P2_B + 2 * P2_SLOT;
constexpr int P3_SLOT = 16384;

__global__ __launch_bounds__(512, 2) void k_fused(const u16* __restrict__ x, const u16* __restrict__ wA, const u16* __restrict__ wB, const u16* __restrict__ woA,
                                                  const u16* __restrict__ woB, const u16* __restrict__ gw, char* __restrict__ ws, u16* __restrict__ out) {
    extern __shared__ __attribute__((aligned(16))) char lds[];
    const unsigned lds0 = (unsigned)(uintptr_t)(LAS char*)lds;
    STAMP_DECL; STAMP_AT(0);
    const int tid = threadIdx.x, lane = tid & 63;
    const int w = __builtin_amdgcn_readfirstlane(tid >> 6);
    const int bid = blockIdx.x, xcd = bid & 7, idx = bid >> 3;
    const int head = xcd * 4 + (idx >> 3), rt = idx & 7;
    unsigned* flags = (unsigned*)(ws + WS_F);
    const unsigned l16 = lane * 16;
    float gate;
    unsigned token;

    f32x16 acc[2][3];
    {
        const char* xg = (const char*)x + (size_t)rt * 256 * 2048;
        const bool w0 = w == 0, w7 = w == 7;
        const int whalf = w >= 4;
        unsigned vo[4], ve[4];
        const char* ebA; const char* ebB; unsigned estride, edst;
        {
            const int r8 = lane >> 3, c8 = lane & 7;
            const int sc = (lane & 3) ^ (((lane >> 5) & 1) << 1);
#pragma unroll
            for (int j = 0; j < 4; ++j) {
                const int p = 4 * (w7 ? 0 : w) + j;
                vo[j] = (unsigned)((8 * p + r8) * 2048 + ((c8 ^ ((4 * p + (lane >> 4)) & 7)) << 4));
                const int pe = 28 + j;
                const unsigned vxe = (unsigned)((8 * pe + r8) * 2048 + ((c8 ^ ((4 * pe + (lane >> 4)) & 7)) << 4));
                const int i = 4 * ((w7 || w0 ? 1 : w) - 1) + j, ii = i >= 12 ? i - 12 : i, seg = ii >> 1, rg = ii & 1, type = seg >= 3 ? seg - 3 : seg;
                const unsigned vwe = (unsigned)((whalf * 32 + rg * 16 + (lane >> 2)) * 6144 + type * 2048 + (sc << 4));
                ve[j] = w0 ? vxe : vwe;
            }
            const int i0 = 4 * ((w7 || w0 ? 1 : w) - 1), ii0 = i0 >= 12 ? i0 - 12 : i0;
            ebA = w0 ? xg : (const char*)(ii0 >= 6 ? wB : wA) + head * 64;
            ebB = w0 ? xg : (const char*)(ii0 + 2 >= 6 ? wB : wA) + head * 64;
            estride = w0 ? 128u : 64u * 6144u;
            edst = w0 ? 28 * 1024 : P1_WBASE + ii0 * 1024;
        }
        const unsigned odst = lds0 + (w7 ? 0 : w) * 4096;
        const unsigned mstage = __builtin_amdgcn_readfirstlane(w7 ? 0u : ~0u), mwarm = ~mstage;
        unsigned pf = 0;
        const char* pfw = (const char*)(bid < 128 ? wA : wB);
        unsigned pfo;
        { const int L = (bid & 127) * 24 + (lane < 24 ? lane : 0); pfo = (unsigned)((L / 48) * 6144 + (L % 48) * 128); }
#define P1_PF(base, off) asm volatile("s_mov_b32 exec_lo, %3\n\ts_mov_b32 exec_hi, %3\n\tglobal_load_dword %0, %1, %2\n\ts_mov_b64 exec, -1" : "+v"(pf) : "v"(off), "s"(base), "s"(mwarm) : "memory")
#define P1_GO(og_, od_, j) glds16m(vo[j], og_, (od_) + (j) * 1024, mstage)
#define P1_GE(eA_, eB_, ed_, j) glds16m(ve[j], (j) < 2 ? (eA_) : (eB_), (ed_) + (j) * 1024, mstage)
#define P1_GO2 P1_GO
#define P1_GE2 P1_GE
#define P1_EDST(xo_, h0, h1) (lds0 + edst + (w0 ? (xo_) : (whalf ? (h1) : (h0))))
        u32x4 gx0, gx1, gw0, gw1;
        {
            const char* px = (const char*)x + (size_t)(rt * 256 + head * 8 + w) * 2048;
            const unsigned go = lane * 32, z = 0;
            asm volatile("global_load_dwordx4 %0, %5, %6\n\tglobal_load_dwordx4 %1, %5, %6 offset:16\n\tglobal_load_dwordx4 %2, %5, %7\n\tglobal_load_dwordx4 %3, %5, %7 offset:16\n\t"
                         "global_load_dword %4, %8, %9 sc1"
                         : "=&v"(gx0), "=&v"(gx1), "=&v"(gw0), "=&v"(gw1), "=&v"(token) : "v"(go), "s"(px), "s"(gw), "v"(z), "s"(flags) : "memory");
        }
#pragma unroll
        for (int j = 0; j < 4; ++j) P1_GO(xg, odst, j);
        { const unsigned ed = P1_EDST(0, 0, P1_WSLOT);
#pragma unroll
          for (int j = 0; j < 4; ++j) P1_GE(ebA, ebB, ed, j); }
#pragma unroll
        for (int j = 0; j < 4; ++j) P1_GO(xg + 128, odst + P1_XSLOT, j);
        { const unsigned ed = P1_EDST(P1_XSLOT, 2 * P1_WSLOT, 3 * P1_WSLOT);
#pragma unroll
          for (int j = 0; j < 4; ++j) P1_GE(ebA + estride, ebB + estride, ed, j); }
        ebA += 2 * (size_t)estride; ebB += 2 * (size_t)estride;
        {
            P1_PF(pfw + 2 * (64 * 6144), pfo); P1_PF(pfw + 3 * (64 * 6144), pfo);
            const char* xl = (const char*)x + (size_t)bid * 16384; const unsigned lo = lane * 128;
            P1_PF(xl, lo); P1_PF(xl + 8192, lo);
            P1_PF(pfw + 4 * (64 * 6144), pfo); P1_PF(pfw + 5 * (64 * 6144), pfo);
            if (w7) asm volatile("s_waitcnt vmcnt(6)" ::: "memory");
        }
        const int wm = w >> 1, wn = w & 1, hh1 = lane >> 5;
        unsigned xa[4], wa;
#pragma unroll
        for (int u = 0; u < 4; ++u) xa[u] = lds0 + (wm * 64 + (lane & 31)) * 128 + (((2 * u + hh1) ^ ((lane >> 1) & 7)) << 4);
        wa = lds0 + P1_WBASE + wn * 6144 + (8 * hh1 + ((lane & 15) >> 2)) * 64 + ((((lane >> 4) & 1) ^ hh1) * 32) + (lane & 3) * 8;
#pragma unroll
        for (int i = 0; i < 2; ++i)
#pragma unroll
            for (int j = 0; j < 3; ++j) acc[i][j] = f32x16{};
        bf16x8 xf[2][2][2]; s16x4 wl[2][2][3], wh[2][2][3];
#define P1_WF(b, k, t) cat8(wl[b][k][t], wh[b][k][t])
#define P1_MM(b, k, r, t) acc[r][t] = ((t) < 2) ? MFMA32(P1_WF(b, k, t), xf[b][k][r], acc[r][t]) : MFMA32(xf[b][k][r], P1_WF(b, k, t), acc[r][t])
#define P1_RW(b, wa_, k, t) do { wl[b][k][t] = ldtr((wa_) + (t) * 2048 + (k) * 1024); wh[b][k][t] = ldtr((wa_) + (t) * 2048 + (k) * 1024 + 256); } while (0)
#define P1_STEP(HF, c, n, xo, u, wo, RD, G0, G1, G2, G3) do { \
            const unsigned xa0_ = xa[2 * (u)] + (xo), xa1_ = xa[2 * (u) + 1] + (xo), wa_ = wa + (wo); \
            P1_MM(c, 0, 0, 0); if (RD) { xf[n][0][0] = ld128(xa0_); P1_RW(n, wa_, 0, 0); } if (HF == 0) { G0; } SBAR(); \
            P1_MM(c, 0, 0, 1); if (RD) { P1_RW(n, wa_, 0, 1); } SBAR(); \
            P1_MM(c, 0, 0, 2); if (RD) { P1_RW(n, wa_, 0, 2); } if (HF == 1) { G0; } SBAR(); \
            P1_MM(c, 0, 1, 0); if (RD) { xf[n][0][1] = ld128(xa0_ + 4096); } if (HF == 0) { G1; } SBAR(); \
            P1_MM(c, 0, 1, 1); if (RD) { xf[n][1][0] = ld128(xa1_); } SBAR(); \
            P1_MM(c, 0, 1, 2); if (RD) { P1_RW(n, wa_, 1, 0); } if (HF == 1) { G1; } SBAR(); \
            P1_MM(c, 1, 0, 0); if (RD) { P1_RW(n, wa_, 1, 1); } if (HF == 0) { G2; } SBAR(); \
            P1_MM(c, 1, 0, 1); if (RD) { P1_RW(n, wa_, 1, 2); } SBAR(); \
            P1_MM(c, 1, 0, 2); if (RD) { xf[n][1][1] = ld128(xa1_ + 4096); } if (HF == 1) { G2; } SBAR(); \
            P1_MM(c, 1, 1, 0); if (HF == 0) { G3; } SBAR(); \
            P1_MM(c, 1, 1, 1); if (HF == 1) { G3; } SBAR(); \
            P1_MM(c, 1, 1, 2); SBAR(); } while (0)
        STAMP_AT(1);
        asm volatile("s_waitcnt vmcnt(8) lgkmcnt(0)\n\ts_barrier" : "+v"(gx0), "+v"(gx1), "+v"(gw0), "+v"(gw1), "+v"(token)::"memory");
        STAMP_AT(2);
#pragma unroll
        for (int j = 0; j < 4; ++j) P1_GO(xg + 256, odst + 2 * P1_XSLOT, j);
        {
            float s = 0.f;
#pragma unroll
            for (int i = 0; i < 4; ++i) {
                s += __uint_as_float(gx0[i] << 16) * __uint_as_float(gw0[i] << 16) + __uint_as_float(gx0[i] & 0xffff0000u) * __uint_as_float(gw0[i] & 0xffff0000u);
                s += __uint_as_float(gx1[i] << 16) * __uint_as_float(gw1[i] << 16) + __uint_as_float(gx1[i] & 0xffff0000u) * __uint_as_float(gw1[i] & 0xffff0000u);
            }
            gate = s; PIN(gate);
            token = __builtin_amdgcn_readfirstlane(token) + 1u;
        }
        { xf[0][0][0] = ld128(xa[0]); xf[0][0][1] = ld128(xa[0] + 4096); xf[0][1][0] = ld128(xa[1]); xf[0][1][1] = ld128(xa[1] + 4096);
#pragma unroll
          for (int n = 0; n < 6; ++n) P1_RW(0, wa, n / 3, n % 3); }
        LBAR();
        unsigned xo0 = 0, xo1 = P1_XSLOT, xo2 = 2 * P1_XSLOT;
        unsigned wo0 = 0, wo1 = P1_WSLOT, wo2 = 2 * P1_WSLOT, wo3 = 3 * P1_WSLOT, wo4 = 4 * P1_WSLOT;
#define P1_TILE(HF, T, IE, IO, WA, WB, RDLAST, PFX) do { \
            const unsigned ed_ = P1_EDST(xo2, wo4, wo0), od_ = odst + xo0; const char* og_ = xg + ((T) + 3) * 128; \
            P1_STEP(HF, 0, 1, xo0, 1, wo1, P1_RD0, if (IE) P1_GE2(ebA, ebB, ed_, 0), if (IE) P1_GE2(ebA, ebB, ed_, 1), if (IE) P1_GE2(ebA, ebB, ed_, 2), if (IE) P1_GE2(ebA, ebB, ed_, 3)); \
            WA; \
            PFX; \
            P1_STEP(HF, 1, 0, xo1, 0, wo2, RDLAST, if (IO) P1_GO2(og_, od_, 0), if (IO) P1_GO2(og_, od_, 1), if (IO) P1_GO2(og_, od_, 2), if (IO) P1_GO2(og_, od_, 3)); \
            WB; \
            ebA += estride; ebB += estride; \
            { const unsigned t_ = xo0; xo0 = xo1; xo1 = xo2; xo2 = t_; const unsigned a_ = wo0, b_ = wo1; wo0 = wo2; wo1 = wo3; wo2 = wo4; wo3 = a_; wo4 = b_; } } while (0)
#define P1_WB(N) WAIT_BAR(N)
#define P1_RDL 1
#define P1_RD0 1
#define P1_LOOP(HF) do { \
            for (int T = 0; T < 13; ++T) { \
                  \
                const char* pb = T < 10 ? pfw + (size_t)(T + 6) * (64 * 6144) : (const char*)(bid < 128 ? woA : woB) + (bid & 127) * 16384 + (T & 1) * 8192; \
                const unsigned po = T < 10 ? pfo : lane * 128; \
                P1_TILE(HF, T, 1, 1, P1_WB(8), P1_WB(12), P1_RDL, if (HF == 1) P1_PF(pb, po)); \
            } \
            P1_TILE(HF, 13, 1, 0, WAIT_BAR(8), WAIT_BAR(8), 1, NOP_); \
            P1_TILE(HF, 14, 0, 0, WAIT_BAR(0), NOP_, 1, NOP_); \
            P1_TILE(HF, 15, 0, 0, NOP_, NOP_, 0, NOP_); } while (0)
        if (w < 4) P1_LOOP(0); else P1_LOOP(1);
        asm volatile("s_waitcnt vmcnt(0)" : "+v"(pf)::"memory");
#undef P1_LOOP
#undef P1_TILE
#undef P1_STEP
#undef P1_RW
#undef P1_MM
#undef P1_WF
#undef P1_GO
#undef P1_GE
#undef P1_EDST
#undef P1_PF
    }
    STAMP_AT(3);
#pragma unroll
    for (int o = 32; o >= 1; o >>= 1) gate += __shfl_xor(gate, o);
    gate = 1.0f / (1.0f + __expf(-gate));
    LBAR();
    u32x4 keep[8];
    const unsigned mB = __builtin_amdgcn_readfirstlane((w & 1) ? ~0u : 0u), mA = ~mB;
    char* kvg = ws + WS_KV + (size_t)(((w & 1) * 32 + head) * 8 + rt) * 32768;
    const unsigned kvo = (w >> 1) * 4096 + l16;
    {
        const int wn = w & 1;
        const unsigned kl = lds0 + (wn ? P2_B : P2_AK + 16384), vl = lds0 + (wn ? P2_B + 16384 : P2_AV + 16384), ql_ = lds0 + P2_Q + wn * 16384;
#pragma unroll
        for (int r = 0; r < 2; ++r)
#pragma unroll
            for (int s = 0; s < 2; ++s) {
                const unsigned o = kvo + r * 2048 + s * 1024;
                keep[r * 2 + s] = __builtin_bit_cast(u32x4, pack8(acc[r][1], s, 1.0f)); keep[4 + r * 2 + s] = __builtin_bit_cast(u32x4, pack8(acc[r][2], s, 1.0f));
                st16_wtm(o, kvg, keep[r * 2 + s], mB); st16_wtm(16384 + o, kvg, keep[4 + r * 2 + s], mB);
            }
#pragma unroll
        for (int r = 0; r < 2; ++r)
#pragma unroll
            for (int s = 0; s < 2; ++s) {
                const unsigned o = kvo + r * 2048 + s * 1024;
                st128(kl + o, keep[r * 2 + s]); st128(vl + o, keep[4 + r * 2 + s]);
                st128(ql_ + o, __builtin_bit_cast(u32x4, pack8(acc[r][0], s, QSCALE)));
            }
    }
    LBAR();
    STAMP_AT(4);

    const int rb = rt * 8 + w;
    const int ql = lane & 31, hh = lane >> 5;
    const char* kvA = ws + WS_KV + (size_t)(head * 8) * 32768;
    const char* kvB = ws + WS_KV + (size_t)((32 + head) * 8) * 32768;
    unsigned* fKB = flags + 64 + head * 8; unsigned* fO = flags + 64 + 256; unsigned* fKA = flags + 64 + 512 + head * 8; unsigned* fOB = flags + 64 + 768;
    const int rtp = rt > 0 ? rt - 1 : 0;
    const unsigned* fKAp = fKA + rtp;
    bf16x8 sel;
    { const short o = (((lane >> 4) & 1) == ((lane >> 2) & 1)) ? (short)0x3F80 : (short)0;
#pragma unroll
      for (int i = 0; i < 8; ++i) sel[i] = o; }
    const bf16x8 qa0 = ld128(lds0 + P2_Q + w * 2048 + l16), qa1 = ld128(lds0 + P2_Q + w * 2048 + 1024 + l16);
    const bf16x8 qb0 = ld128(lds0 + P2_Q + 16384 + w * 2048 + l16), qb1 = ld128(lds0 + P2_Q + 16384 + w * 2048 + 1024 + l16);
    char* og = ws + WS_O + ((size_t)rb * 64 + head) * 2048;
    const unsigned oo = (ql >> 4) * 1024 + (hh * 16 + (ql & 15)) * 16;
    const int rt3 = (xcd & 3) * 4 + (idx & 3), ct = (xcd >> 2) * 8 + (idx >> 2);
    const int p3 = w & 3, half = w >> 2;
    const char* wsrcB = (const char*)woB + ct * 128 + (size_t)half * 512 * 2048; const char* wsrcA = (const char*)woA + ct * 128 + (size_t)half * 512 * 2048;
    const unsigned vw0 = (unsigned)((16 * p3 + (lane >> 3)) * 2048 + (((lane & 7) ^ (((lane >> 4) & 3) << 1)) << 4)), vw1 = vw0 + 8 * 2048;
    const unsigned wdst3 = lds0 + half * 8192 + p3 * 2048;
#define P3_SOFF(s) ((s) < 6 ? 65536 + (s) * P3_SLOT : (s) < 10 ? ((s) - 6) * P3_SLOT : 65536 + ((s) - 10) * P3_SLOT)
#define P3_GW(st, i) glds16((i) ? vw1 : vw0, ((st) < 8 ? wsrcB : wsrcA) + (size_t)((st) & 7) * (64 * 2048), wdst3 + P3_SOFF(st) + (i) * 1024)

#define EX(x) __builtin_amdgcn_exp2f(x)
#define P8(x) __builtin_bit_cast(bf16x8, x)
#define STEP(SC, SN, Q0, Q1, AK0, AK1, AV1, AV0, X) do { \
        SN = MFMA32(k0, Q0, negm); k0 = ld128(AK0); \
        phi[2] = cvtpk(t0, t1); phi[3] = cvtpk(t2, t3); t0 = EX(SC[0]); t1 = EX(SC[1]); PIN(phi); PIN(t0); PIN(t1); SBAR(); \
        O = MFMA32(v1, P8(phi), O); v1 = ld128(AV1); \
        t2 = EX(SC[2]); t3 = EX(SC[3]); plo[0] = cvtpk(t0, t1); PIN(t2); PIN(t3); PIN(plo); SBAR(); \
        L = MFMA16(sel, P8(phi), L); \
        plo[1] = cvtpk(t2, t3); t0 = EX(SC[4]); PIN(plo); PIN(t0); SBAR(); \
        SN = MFMA32(k1, Q1, SN); k1 = ld128(AK1); \
        t1 = EX(SC[5]); t2 = EX(SC[6]); t3 = EX(SC[7]); PIN(t1); PIN(t2); PIN(t3); SBAR(); \
        X; SBAR(); \
        plo[2] = cvtpk(t0, t1); t0 = EX(SC[8]); plo[3] = cvtpk(t2, t3); t1 = EX(SC[9]); PIN(plo); PIN(t0); PIN(t1); SBAR(); \
        O = MFMA32(v0, P8(plo), O); v0 = ld128(AV0); \
        t2 = EX(SC[10]); t3 = EX(SC[11]); phi[0] = cvtpk(t0, t1); PIN(t2); PIN(t3); PIN(phi); SBAR(); \
        L = MFMA16(sel, P8(plo), L); \
        phi[1] = cvtpk(t2, t3); \
        t0 = EX(SC[12]); t1 = EX(SC[13]); t2 = EX(SC[14]); t3 = EX(SC[15]); PIN(phi); PIN(t0); PIN(t1); PIN(t2); PIN(t3); SBAR(); \
    } while (0)
#define SG_OFF(i) (((i) >> 1) * 16384 + ((i) & 1) * 1024)
#define SG_LD(i, src, dK, dV) glds16c(l16, (src) + w * 2048 + SG_OFF(i), ((i) >> 1 ? (dV) : (dK)) + w * 2048 + ((i) & 1) * 1024)
    {
        f32x16 O = {}, negm, SA, SB; f32x4 L = {};
        bf16x8 k0, k1, v0, v1; u32x4 plo, phi; float t0 = 0.f, t1 = 0.f, t2 = 0.f, t3 = 0.f;
        unsigned cur = lds0 + P2_B + l16, nxt = cur + P2_SLOT;
        {
            k0 = ld128(cur); k1 = ld128(cur + 1024);
            SA = f32x16{}; SA = MFMA32(k0, qb0, SA); SA = MFMA32(k1, qb1, SA);
            const float m = rowmax16(SA);
#pragma unroll
            for (int r = 0; r < 16; ++r) { negm[r] = -m; SA[r] -= m; }
            k0 = ld128(cur + 2048); k1 = ld128(cur + 2048 + 1024);
            v0 = ld128(cur + 16384);
            v1 = bf16x8{}; phi = u32x4{}; plo = u32x4{};
        }
#define KAD(j, s) (((j) < 8 ? cur + (j) * 2048 : nxt + ((j) - 8) * 2048) + (s) * 1024)
#define BSTEP(SC, SN, j, X) STEP(SC, SN, qb0, qb1, KAD((j) + 2, 0), KAD((j) + 2, 1), KAD(j, 1) + 16384, KAD((j) + 1, 0) + 16384, X)
#define KEEP_A(i) st16_wtm(kvo + ((i) >> 2) * 16384 + (((i) >> 1) & 1) * 2048 + ((i) & 1) * 1024, kvg, keep[i], mA)
        unsigned pv;
        BSTEP(SA, SB, 0, NOP_);
        STAMP_AT(5);
        WAIT_BAR(0);
        STAMP_AT(6);
        if (tid == 0) flag_set(fKB + rt, token);
        BSTEP(SB, SA, 1, POLL_ISSUE(pv, fKB, 8));
        { const char* s1 = kvB + ((rt + 1) & 7) * 32768; const unsigned d = lds0 + P2_B + P2_SLOT;
          BSTEP(SA, SB, 2, POLL_CHECK(pv, fKB, 8); SG_LD(0, s1, d, d + 16384); SG_LD(1, s1, d, d + 16384); SG_LD(2, s1, d, d + 16384); SG_LD(3, s1, d, d + 16384)); }
        STAMP_AT(7);
        BSTEP(SB, SA, 3, KEEP_A(0); KEEP_A(1); KEEP_A(2); KEEP_A(3));
        BSTEP(SA, SB, 4, KEEP_A(4); KEEP_A(5); KEEP_A(6); KEEP_A(7));
        STAMP_AT(8);
        BSTEP(SB, SA, 5, NOP_);
        STAMP_AT(9);
        WAIT_BAR(0);
        STAMP_AT(10);
        if (tid == 0) flag_set(fKA + rt, token);
        int slot = 0;
        for (int i = 0; i < 7; ++i) {
            const int s1 = slot == 2 ? 0 : slot + 1, s2 = s1 == 2 ? 0 : s1 + 1;
            const char* src = i < 6 ? kvB + ((rt + i + 2) & 7) * 32768 : kvA + rtp * 32768;
            const unsigned dK = i < 6 ? lds0 + P2_B + s2 * P2_SLOT : lds0 + P2_AK, dV = i < 6 ? dK + 16384 : lds0 + P2_AV;
            if (i == 6) POLL_CHECK(pv, fKAp, 1);
            BSTEP(SA, SB, 6, SG_LD(0, src, dK, dV)); BSTEP(SB, SA, 7, SG_LD(1, src, dK, dV));
            cur = nxt; nxt = lds0 + P2_B + s2 * P2_SLOT + l16;
            BSTEP(SA, SB, 0, SG_LD(2, src, dK, dV)); BSTEP(SB, SA, 1, SG_LD(3, src, dK, dV)); BSTEP(SA, SB, 2, NOP_);
            BSTEP(SB, SA, 3, NOP_);
            BSTEP(SA, SB, 4, if (i == 5) { POLL_ISSUE(pv, fKAp, 1); });
            BSTEP(SB, SA, 5, NOP_);
            WAIT_BAR(0);
            slot = s1;
        }
        BSTEP(SA, SB, 6, NOP_); BSTEP(SB, SA, 7, NOP_);
        phi[2] = cvtpk(t0, t1); phi[3] = cvtpk(t2, t3);
        O = MFMA32(v1, P8(phi), O); L = MFMA16(sel, P8(phi), L);
        float l = L[0];
        if (!unit_ok(O, l)) attn_safe<false>(kvB, qb0, qb1, rb, lane, O, l);
        const float f = 1.0f / l;
        st16_wt(oo, og + 32 * 2048, __builtin_bit_cast(u32x4, pack8(O, 0, f))); st16_wt(oo + 512, og + 32 * 2048, __builtin_bit_cast(u32x4, pack8(O, 1, f)));
#undef KEEP_A
#undef BSTEP
#undef KAD
    }
    STAMP_AT(11);
    LBAR();
    {
        f32x16 O = {}, negm, SA, SB; f32x4 L = {};
        bf16x8 k0, k1, v0, v1; u32x4 plo, phi; float t0 = 0.f, t1 = 0.f, t2 = 0.f, t3 = 0.f;
        const unsigned ab = lds0 + P2_AK + w * 2048 + l16;
        const int jmax = rt > 0 ? 8 : w;
#define AAD(j, s) (ab + (8 - ((j) > 8 ? 8 : (j))) * 2048 + (s) * 1024)
        {
            k0 = ld128(AAD(0, 0)); k1 = ld128(AAD(0, 1));
            SA = f32x16{}; SA = MFMA32(k0, qa0, SA); SA = MFMA32(k1, qa1, SA);
#pragma unroll
            for (int r = 0; r < 16; ++r) if (keyof(r, hh) > ql) SA[r] = -INFINITY;
            const float m = rowmax16(SA);
#pragma unroll
            for (int r = 0; r < 16; ++r) { negm[r] = -m; SA[r] -= m; }
            k0 = ld128(AAD(1, 0)); k1 = ld128(AAD(1, 1));
            v0 = ld128(AAD(0, 0) + 32768);
            v1 = bf16x8{}; phi = u32x4{}; plo = u32x4{};
        }
#define ASTEP(SC, SN, j, X) do { if ((j) + 1 > jmax) { _Pragma("unroll") for (int r_ = 0; r_ < 16; ++r_) negm[r_] = -INFINITY; } \
        STEP(SC, SN, qa0, qa1, AAD((j) + 2, 0), AAD((j) + 2, 1), AAD(j, 1) + 32768, AAD((j) + 1, 0) + 32768, X); } while (0)
        ASTEP(SA, SB, 0, P3_GW(0, 0); P3_GW(0, 1)); ASTEP(SB, SA, 1, P3_GW(1, 0); P3_GW(1, 1)); ASTEP(SA, SB, 2, P3_GW(2, 0); P3_GW(2, 1)); ASTEP(SB, SA, 3, P3_GW(3, 0); P3_GW(3, 1));
        ASTEP(SA, SB, 4, P3_GW(4, 0); P3_GW(4, 1));
        WAIT_BAR(10);
        if (tid == 0) flag_set(fOB + rt * 32 + head, token);
        ASTEP(SB, SA, 5, P3_GW(5, 0); P3_GW(5, 1)); ASTEP(SA, SB, 6, NOP_); ASTEP(SB, SA, 7, NOP_);
#pragma unroll
        for (int r = 0; r < 16; ++r) if (keyof(r, hh) <= ql) SA[r] = -INFINITY;
        ASTEP(SA, SB, 8, NOP_);
        phi[2] = cvtpk(t0, t1); phi[3] = cvtpk(t2, t3);
        O = MFMA32(v1, P8(phi), O); L = MFMA16(sel, P8(phi), L);
        float l = L[0];
        if (!unit_ok(O, l)) attn_safe<true>(kvA, qa0, qa1, rb, lane, O, l);
        const float f = 1.0f / l;
        st16_wt(oo, og, __builtin_bit_cast(u32x4, pack8(O, 0, f))); st16_wt(oo + 512, og, __builtin_bit_cast(u32x4, pack8(O, 1, f)));
        if (lane == 0) __hip_atomic_store((GAS float*)(ws + WS_G) + rt * 256 + head * 8 + w, gate, __ATOMIC_RELAXED, __HIP_MEMORY_SCOPE_AGENT);
#undef ASTEP
#undef AAD
    }
#undef STEP
#undef EX
#undef P8
#undef SG_LD
#undef SG_OFF
    STAMP_AT(12);
    WAIT_BAR(0);
    if (tid == 0) flag_set(fO + rt * 32 + head, token);
    STAMP_AT(13);

    {
#pragma unroll
        for (int st = 6; st < 10; ++st) { P3_GW(st, 0); P3_GW(st, 1); }
        poll_spin(fOB + (rt3 >> 1) * 32, 32, token, lane);
        STAMP_AT(14);
        const char* osrc = ws + WS_O + ((size_t)(rt3 * 4 + p3) * 64 + half * 16) * 2048;
        bf16x8 of[7][4];
#define P3_GO(st, i) asm volatile("global_load_dwordx4 %0, %1, %2 offset:" #i "*1024 sc1" : "=v"(of[(st) % 7][i]) : "v"(l16), "s"(osrc + ((st) < 8 ? 65536 : 0) + ((st) & 7) * 4096) : "memory")
#define P3_GO4(st) do { P3_GO(st, 0); P3_GO(st, 1); P3_GO(st, 2); P3_GO(st, 3); } while (0)
        P3_GO4(0); P3_GO4(1); P3_GO4(2); P3_GO4(3); P3_GO4(4); P3_GO4(5);
        float gq[2];
#define P3_GG() do { const float* gp_ = (const float*)(ws + WS_G) + rt3 * 128 + p3 * 32; const unsigned go_ = (lane & 15) * 4; \
          asm volatile("global_load_dword %0, %2, %3 sc1\n\tglobal_load_dword %1, %2, %3 offset:64 sc1" : "=&v"(gq[0]), "=&v"(gq[1]) : "v"(go_), "s"(gp_) : "memory"); } while (0)
        const int g4 = lane >> 4, q4 = (lane & 15) >> 2;
        unsigned wa[4];
        { const unsigned a0 = (16 * (g4 >> 1) + 4 * (g4 & 1) + q4) * 128 + ((2 * (g4 & 1) + (q4 >> 1)) * 32) + (lane & 3) * 8;
#pragma unroll
          for (int n = 0; n < 4; ++n) wa[n] = lds0 + half * 8192 + (a0 ^ (n << 5)) + P3_SOFF(0); }
        f32x4 ac[2][2][4];
#pragma unroll
        for (int i = 0; i < 4; ++i)
#pragma unroll
            for (int j = 0; j < 4; ++j) ac[i >> 1][i & 1][j] = f32x4{};
        s16x4 wl[2][4], wh[2][4];
#define P3_RW(buf, k_, n_) do { wl[buf][n_] = ldtr(wa[n_] + (k_) * 4096); wh[buf][n_] = ldtr(wa[n_] + (k_) * 4096 + 1024); } while (0)
#define P3_MMA(st, k_, n_, wf_) do { ac[(st) >> 3][0][n_] = MFMA16(wf_, of[(st) % 7][2 * (k_)], ac[(st) >> 3][0][n_]); ac[(st) >> 3][1][n_] = MFMA16(wf_, of[(st) % 7][2 * (k_) + 1], ac[(st) >> 3][1][n_]); } while (0)
#define P3_K32(st, k_, X0, X1, X2, X3) do { \
            if ((k_) == 1 && (st) + 1 < 16) { _Pragma("unroll") for (int n_ = 0; n_ < 4; ++n_) { wa[n_] += (unsigned)(P3_SOFF((st) + 1) - P3_SOFF(st)); PIN(wa[n_]); } } \
            _Pragma("unroll") for (int n_ = 0; n_ < 4; ++n_) { \
                const bf16x8 wf_ = cat8(wl[k_][n_], wh[k_][n_]); \
                P3_MMA(st, k_, n_, wf_); \
                if ((k_) == 0) P3_RW(1, 1, n_); else if ((st) + 1 < 16) P3_RW(0, 0, n_); \
                if (n_ == 0) { X0; } if (n_ == 1) { X1; } if (n_ == 2) { X2; } if (n_ == 3) { X3; } \
                SBAR(); } } while (0)
#define P3_OW(st, NW) asm volatile("s_waitcnt vmcnt(" #NW ")" : "+v"(of[(st) % 7][0]), "+v"(of[(st) % 7][1]), "+v"(of[(st) % 7][2]), "+v"(of[(st) % 7][3])::"memory")
#define P3_STAGE(st, NW, BAR, nx) do { \
            P3_OW(st, NW); if (BAR) LBAR(); \
            P3_K32(st, 0, NOP_, NOP_, NOP_, NOP_); \
            if ((nx) < 16) { P3_K32(st, 1, P3_GO(nx, 0), P3_GO(nx, 1), P3_GO(nx, 2), P3_GO(nx, 3)); } \
            else { P3_K32(st, 1, NOP_, NOP_, NOP_, NOP_); } \
        } while (0)
        LBAR();
#pragma unroll
        for (int n_ = 0; n_ < 4; ++n_) P3_RW(0, 0, n_);
        unsigned pv;
        const unsigned* fOAq = fO + (rt3 >> 1) * 32;
        P3_STAGE(0, 20, 0, 6); P3_STAGE(1, 20, 0, 7); P3_STAGE(2, 20, 0, 16);
        P3_OW(3, 16); LBAR();
        P3_K32(3, 0, POLL_ISSUE(pv, fOAq, 32), P3_GW(10, 0), P3_GW(10, 1), P3_GW(11, 0)); P3_K32(3, 1, P3_GW(11, 1), P3_GW(12, 0), P3_GW(12, 1), NOP_);
        asm volatile("s_waitcnt vmcnt(6)" : "+v"(pv), "+v"(of[4][0]), "+v"(of[4][1]), "+v"(of[4][2]), "+v"(of[4][3])::"memory");
        if (!__all(pv == token)) poll_spin(fOAq, 32, token, lane);
        P3_K32(4, 0, P3_GG(), P3_GO(8, 0); P3_GO(8, 1), P3_GO(8, 2); P3_GO(8, 3), P3_GO(9, 0); P3_GO(9, 1)); P3_K32(4, 1, P3_GO(9, 2); P3_GO(9, 3), P3_GO(10, 0); P3_GO(10, 1), P3_GO(10, 2), P3_GO(10, 3));
        P3_STAGE(5, 29, 0, 11);
        P3_OW(6, 29); LBAR();
        P3_K32(6, 0, P3_GW(13, 0), P3_GW(13, 1), P3_GW(14, 0), P3_GW(14, 1)); P3_K32(6, 1, P3_GW(15, 0); P3_GW(15, 1); P3_GO(12, 0), P3_GO(12, 1), P3_GO(12, 2), P3_GO(12, 3));
        P3_STAGE(7, 35, 0, 13); P3_STAGE(8, 26, 0, 14);
        P3_STAGE(9, 26, 1, 15); P3_STAGE(10, 26, 0, 16); P3_STAGE(11, 22, 0, 16);
        P3_STAGE(12, 12, 1, 16); P3_STAGE(13, 8, 0, 16); P3_STAGE(14, 4, 0, 16); P3_STAGE(15, 0, 0, 16);
        asm volatile("" : "+v"(gq[0]), "+v"(gq[1]));
        f32x4 r2[2][4];
#pragma unroll
        for (int n = 0; n < 4; ++n) { r2[0][n] = ac[1][0][n] * gq[0] + ac[0][0][n] * (1.0f - gq[0]); r2[1][n] = ac[1][1][n] * gq[1] + ac[0][1][n] * (1.0f - gq[1]); }
        LBAR();
        LAS f32x4* mine = (LAS f32x4*)(uintptr_t)(lds0 + w * 4096 + l16);
        LAS f32x4* theirs = (LAS f32x4*)(uintptr_t)(lds0 + (w ^ 4) * 4096 + l16);
#pragma unroll
        for (int q = 0; q < 2; ++q) { mine[(q * 2) * 64] = half ? r2[q][0] : r2[q][2]; mine[(q * 2 + 1) * 64] = half ? r2[q][1] : r2[q][3]; }
        LBAR();
#pragma unroll
        for (int q = 0; q < 2; ++q)
#pragma unroll
            for (int e = 0; e < 2; ++e) {
                const f32x4 r = (half ? r2[q][2 + e] : r2[q][e]) + theirs[(q * 2 + e) * 64];
                u16* op = out + (size_t)(rt3 * 128 + p3 * 32 + q * 16 + (lane & 15)) * 1024 + ct * 64 + half * 32 + e * 16 + g4 * 4;
                *(u32x2*)op = u32x2{cvtpk(r[0], r[1]), cvtpk(r[2], r[3])};
            }
#undef P3_GO
#undef P3_GO4
#undef P3_MMA
#undef P3_GG
#undef P3_RW
#undef P3_K32
#undef P3_OW
#undef P3_STAGE
    }
#undef P3_GW
#undef P3_SOFF
    if (bid == 0 && w == 0) {
        const unsigned long long t0 = __builtin_amdgcn_s_memrealtime();
        for (;;) {
            bool ok = true;
#pragma unroll
            for (int i = 0; i < 4; ++i) ok = ok && (__hip_atomic_load((GAS unsigned*)(fO + i * 64 + lane), __ATOMIC_RELAXED, __HIP_MEMORY_SCOPE_AGENT) == token);
            if (__all(ok)) break;
            if (__builtin_amdgcn_s_memrealtime() - t0 > 400000ull) break;
            __builtin_amdgcn_s_sleep(1);
        }
        if (lane == 0) flag_set(flags, token);
    }
    STAMP_AT(15);
    STAMP_FLUSH(ws, 16);
}

extern "C" void kernel_launch(void* const* d_in, const int* in_sizes, int n_in, void* d_out, int out_size, void* d_ws, size_t ws_size, hipStream_t stream) {
    (void)in_sizes; (void)n_in; (void)out_size; (void)ws_size;
    const u16* x = (const u16*)d_in[0]; const u16* wqkvA = (const u16*)d_in[1]; const u16* woA = (const u16*)d_in[2];
    const u16* wqkvB = (const u16*)d_in[4]; const u16* woB = (const u16*)d_in[5]; const u16* gw = (const u16*)d_in[6];
    (void)hipFuncSetAttribute((const void*)k_fused, hipFuncAttributeMaxDynamicSharedMemorySize, LDS_BYTES);
    k_fused<<<256, 512, LDS_BYTES, stream>>>(x, wqkvA, wqkvB, woA, woB, gw, (char*)d_ws, (u16*)d_out);
}
```

```cpp
#include <hip/hip_runtime.h>
#include <hip/hip_bf16.h>
#include <stdint.h>
#pragma clang diagnostic ignored "-Winline-asm"

typedef unsigned short u16;
typedef short bf16x8 __attribute__((ext_vector_type(8)));
typedef short s16x4 __attribute__((ext_vector_type(4)));
typedef float f32x16 __attribute__((ext_vector_type(16)));
typedef float f32x4 __attribute__((ext_vector_type(4)));
typedef float f32x2 __attribute__((ext_vector_type(2)));
typedef __bf16 bf16x2_t __attribute__((ext_vector_type(2)));
typedef unsigned u32x4 __attribute__((ext_vector_type(4)));
typedef unsigned u32x2 __attribute__((ext_vector_type(2)));
#define LAS __attribute__((address_space(3)))
#define GAS __attribute__((address_space(1)))
#define DI __device__ __forceinline__
#define MFMA32(a, b, c) __builtin_amdgcn_mfma_f32_32x32x16_bf16((a), (b), (c), 0, 0, 0)
#define MFMA16(a, b, c) __builtin_amdgcn_mfma_f32_16x16x32_bf16((a), (b), (c), 0, 0, 0)
#define SBAR() __builtin_amdgcn_sched_barrier(0)
#define PIN(x) asm volatile("" : "+v"(x))
#define WAITCNT_VM_LGKM0(N) __builtin_amdgcn_s_waitcnt(((N) & 15) | (((N) >> 4) << 14) | 0x70)
#define WAIT_BAR(N) do { asm volatile("" ::: "memory"); WAITCNT_VM_LGKM0(N); __builtin_amdgcn_s_barrier(); asm volatile("" ::: "memory"); } while (0)
#define LBAR() WAIT_BAR(63)
#define NOP_ do {} while (0)

constexpr float QSCALE = 0.17677669529663687f * 1.4426950408889634f;
constexpr size_t WS_KV = 0;
constexpr size_t WS_O = 16u << 20;
constexpr size_t WS_G = 24u << 20;
constexpr size_t WS_F = (24u << 20) + 8192;
constexpr int LDS_BYTES = 163840;

DI unsigned cvtpk(float lo, float hi) { f32x2 v = {lo, hi}; bf16x2_t b = __builtin_convertvector(v, bf16x2_t); return __builtin_bit_cast(unsigned, b); }
DI bf16x8 pack8(const f32x16& x, int s, float sc) {
    u32x4 w = {cvtpk(x[8 * s] * sc, x[8 * s + 1] * sc), cvtpk(x[8 * s + 2] * sc, x[8 * s + 3] * sc), cvtpk(x[8 * s + 4] * sc, x[8 * s + 5] * sc), cvtpk(x[8 * s + 6] * sc, x[8 * s + 7] * sc)};
    return __builtin_bit_cast(bf16x8, w);
}
DI u32x4 pack44(const f32x4& a, const f32x4& b, float sc) { return u32x4{cvtpk(a[0] * sc, a[1] * sc), cvtpk(a[2] * sc, a[3] * sc), cvtpk(b[0] * sc, b[1] * sc), cvtpk(b[2] * sc, b[3] * sc)}; }
DI void glds16(unsigned voff, const void* sbase, unsigned lds_dst) {
    asm volatile("s_mov_b32 m0, %2\n\ts_nop 0\n\tglobal_load_lds_dwordx4 %0, %1" ::"v"(voff), "s"(sbase), "s"(lds_dst) : "memory", "m0");
}
DI void glds16c(unsigned voff, const void* sbase, unsigned lds_dst) {
    asm volatile("s_mov_b32 m0, %2\n\ts_nop 0\n\tglobal_load_lds_dwordx4 %0, %1 sc1" ::"v"(voff), "s"(sbase), "s"(lds_dst) : "memory", "m0");
}
DI void glds16m(unsigned voff, const void* sbase, unsigned lds_dst, unsigned mask) {
    asm volatile("s_mov_b32 m0, %2\n\ts_mov_b32 exec_lo, %3\n\ts_mov_b32 exec_hi, %3\n\tglobal_load_lds_dwordx4 %0, %1\n\ts_mov_b64 exec, -1" ::"v"(voff), "s"(sbase), "s"(lds_dst), "s"(mask) : "memory", "m0");
}
DI void st16_wt(unsigned voff, void* sbase, u32x4 v) { asm volatile("global_store_dwordx4 %0, %1, %2 sc1\n\ts_nop 1" ::"v"(voff), "v"(v), "s"(sbase) : "memory"); }
DI void st16_wtm(unsigned voff, void* sbase, u32x4 v, unsigned mask) {
    asm volatile("s_mov_b32 exec_lo, %3\n\ts_mov_b32 exec_hi, %3\n\tglobal_store_dwordx4 %0, %1, %2 sc1\n\ts_mov_b64 exec, -1\n\ts_nop 0" ::"v"(voff), "v"(v), "s"(sbase), "s"(mask) : "memory");
}
typedef short v4i16_t __attribute__((ext_vector_type(4)));
DI s16x4 ldtr(unsigned a) { return __builtin_bit_cast(s16x4, __builtin_amdgcn_ds_read_tr16_b64_v4i16((LAS v4i16_t*)(uintptr_t)a)); }
DI bf16x8 ld128(unsigned a) { return *(const LAS bf16x8*)(uintptr_t)a; }
DI void st128(unsigned a, u32x4 v) { *(LAS u32x4*)(uintptr_t)a = v; }
DI bf16x8 cat8(s16x4 lo, s16x4 hi) { return __builtin_shufflevector(lo, hi, 0, 1, 2, 3, 4, 5, 6, 7); }

#define STAMP_DECL do {} while (0)
#define STAMP_AT(i) do {} while (0)
#define STAMP_FLUSH(wsp, n) do {} while (0)

DI void flag_set(unsigned* f, unsigned v) { __hip_atomic_store((GAS unsigned*)f, v, __ATOMIC_RELAXED, __HIP_MEMORY_SCOPE_AGENT); }
DI void poll_spin(const unsigned* f, int n, unsigned want, int lane) {
    const unsigned long long t0 = __builtin_amdgcn_s_memrealtime();
    for (;;) {
        unsigned v = want;
        if (lane < n) v = __hip_atomic_load((GAS unsigned*)(f + lane), __ATOMIC_RELAXED, __HIP_MEMORY_SCOPE_AGENT);
        if (__all(v == want)) break;
        if (__builtin_amdgcn_s_memrealtime() - t0 > 400000ull) break;
        __builtin_amdgcn_s_sleep(1);
    }
    __builtin_amdgcn_fence(__ATOMIC_ACQUIRE, "wavefront");
}
#define POLL_ISSUE(pv, f, n) do { const unsigned* p_ = (f) + (lane < (n) ? lane : 0); asm volatile("global_load_dword %0, %1, off sc1" : "=v"(pv) : "v"(p_) : "memory"); } while (0)
#define POLL_CHECK(pv, f, n) do { asm volatile("s_waitcnt vmcnt(0)" : "+v"(pv)::"memory"); if (!__all((pv) == token)) poll_spin(f, n, token, lane); } while (0)

DI float xhalf_max(float m) { auto rr = __builtin_amdgcn_permlane32_swap(__float_as_uint(m), __float_as_uint(m), false, false); return fmaxf(__uint_as_float(rr[0]), __uint_as_float(rr[1])); }
DI float xhalf_sum(float m) { auto rr = __builtin_amdgcn_permlane32_swap(__float_as_uint(m), __float_as_uint(m), false, false); return __uint_as_float(rr[0]) + __uint_as_float(rr[1]); }
DI int keyof(int r, int h) { return (r & 3) + 8 * (r >> 2) + 4 * h; }
DI float rowmax16(const f32x16& s) {
    float a = fmaxf(fmaxf(s[0], s[1]), s[2]), b = fmaxf(fmaxf(s[3], s[4]), s[5]);
#pragma unroll
    for (int r = 6; r < 16; r += 2) a = fmaxf(fmaxf(a, s[r]), s[r + 1]);
    return xhalf_max(fmaxf(a, b));
}
DI bf16x8 packp(const f32x16& p, int s) {
    u32x4 w = {cvtpk(p[8 * s], p[8 * s + 1]), cvtpk(p[8 * s + 2], p[8 * s + 3]), cvtpk(p[8 * s + 4], p[8 * s + 5]), cvtpk(p[8 * s + 6], p[8 * s + 7])};
    return __builtin_bit_cast(bf16x8, w);
}
template <bool WIN>
DI void attn_safe(const char* kv, bf16x8 q0, bf16x8 q1, int rb, int lane, f32x16& Oout, float& lout) {
    const int ql = lane & 31, h = lane >> 5;
    __builtin_amdgcn_fence(__ATOMIC_ACQUIRE, "agent");
    asm volatile("s_waitcnt vmcnt(0)" ::: "memory");
    float m = -INFINITY, l = 0.f; f32x16 O = {};
    const int nb = WIN ? (rb < 8 ? rb + 1 : 9) : 64;
    for (int j = 0; j < nb; ++j) {
        const int kb = WIN ? rb - j : j;
        const char* kp = kv + (kb >> 3) * 32768 + (kb & 7) * 2048 + lane * 16;
        const bf16x8 k0 = *(const bf16x8*)kp, k1 = *(const bf16x8*)(kp + 1024), v0 = *(const bf16x8*)(kp + 16384), v1 = *(const bf16x8*)(kp + 16384 + 1024);
        f32x16 s = {}; s = MFMA32(k0, q0, s); s = MFMA32(k1, q1, s);
        if (WIN) {
#pragma unroll
            for (int r = 0; r < 16; ++r) { const int kl = keyof(r, h); if ((j == 0 && kl > ql) || (j == 8 && kl <= ql)) s[r] = -INFINITY; }
        }
        const float mn = fmaxf(m, rowmax16(s));
        const float al = __builtin_amdgcn_exp2f(m - mn); m = mn;
        float ps = 0.f;
#pragma unroll
        for (int r = 0; r < 16; ++r) { s[r] = __builtin_amdgcn_exp2f(s[r] - mn); }
        const bf16x8 p0 = packp(s, 0), p1 = packp(s, 1);
#pragma unroll
        for (int r = 0; r < 8; ++r) { ps += __uint_as_float((unsigned)(u16)p0[r] << 16) + __uint_as_float((unsigned)(u16)p1[r] << 16); }
        l = l * al + ps;
#pragma unroll
        for (int r = 0; r < 16; ++r) O[r] *= al;
        O = MFMA32(v0, p0, O); O = MFMA32(v1, p1, O);
    }
    Oout = O; lout = xhalf_sum(l);
}
DI bool unit_ok(const f32x16& O, float l) {
    bool ok = l <= 1.0995116e12f;
    float a = 0.f;
#pragma unroll
    for (int r = 0; r < 16; ++r) a = fmaxf(a, __builtin_fabsf(O[r]));
    bool okO = a <= 3.0e38f;
#pragma unroll
    for (int r = 0; r < 16; ++r) okO = okO && (O[r] == O[r]);
    return __all(ok && okO);
}

constexpr int P1_XSLOT = 32768, P1_WBASE = 3 * P1_XSLOT, P1_WSLOT = 12288;
constexpr int P2_AK = 0, P2_AV = 32768, P2_B = 65536, P2_SLOT = 32768;
constexpr int P2_Q = P2_B + 2 * P2_SLOT;
constexpr int P3_SLOT = 16384;

__global__ __launch_bounds__(512, 2) void k_fused(const u16* __restrict__ x, const u16* __restrict__ wA, const u16* __restrict__ wB, const u16* __restrict__ woA,
                                                  const u16* __restrict__ woB, const u16* __restrict__ gw, char* __restrict__ ws, u16* __restrict__ out) {
    extern __shared__ __attribute__((aligned(16))) char lds[];
    const unsigned lds0 = (unsigned)(uintptr_t)(LAS char*)lds;
    STAMP_DECL; STAMP_AT(0);
    const int tid = threadIdx.x, lane = tid & 63;
    const int w = __builtin_amdgcn_readfirstlane(tid >> 6);
    const int bid = blockIdx.x, xcd = bid & 7, idx = bid >> 3;
    const int head = xcd * 4 + (idx >> 3), rt = idx & 7;
    const int wm = (w & 1) | ((w >> 2) << 1), wn = (w >> 1) & 1, qw = 2 * wm + wn;
    unsigned* flags = (unsigned*)(ws + WS_F);
    const unsigned l16 = lane * 16;
    float gate;
    unsigned token;

    f32x16 acc[2][3];
    {
        const char* xg = (const char*)x + (size_t)rt * 256 * 2048;
        const bool w0 = w == 0, w7 = w == 7;
        const int whalf = w >= 4;
        unsigned vo[4], ve[4];
        const char* ebA; const char* ebB; unsigned estride, edst;
        {
            const int r8 = lane >> 3, c8 = lane & 7;
            const int sc = (lane & 3) ^ (((lane >> 5) & 1) << 1);
#pragma unroll
            for (int j = 0; j < 4; ++j) {
                const int p = 4 * (w7 ? 0 : w) + j;
                vo[j] = (unsigned)((8 * p + r8) * 2048 + ((c8 ^ ((4 * p + (lane >> 4)) & 7)) << 4));
                const int pe = 28 + j;
                const unsigned vxe = (unsigned)((8 * pe + r8) * 2048 + ((c8 ^ ((4 * pe + (lane >> 4)) & 7)) << 4));
                const int i = 4 * ((w7 || w0 ? 1 : w) - 1) + j, ii = i >= 12 ? i - 12 : i, seg = ii >> 1, rg = ii & 1, type = seg >= 3 ? seg - 3 : seg;
                const unsigned vwe = (unsigned)((whalf * 32 + rg * 16 + (lane >> 2)) * 6144 + type * 2048 + (sc << 4));
                ve[j] = w0 ? vxe : vwe;
            }
            const int i0 = 4 * ((w7 || w0 ? 1 : w) - 1), ii0 = i0 >= 12 ? i0 - 12 : i0;
            ebA = w0 ? xg : (const char*)(ii0 >= 6 ? wB : wA) + head * 64;
            ebB = w0 ? xg : (const char*)(ii0 + 2 >= 6 ? wB : wA) + head * 64;
            estride = w0 ? 128u : 64u * 6144u;
            edst = w0 ? 28 * 1024 : P1_WBASE + ii0 * 1024;
        }
        const unsigned odst = lds0 + (w7 ? 0 : w) * 4096;
        const unsigned mstage = __builtin_amdgcn_readfirstlane(w7 ? 0u : ~0u), mwarm = ~mstage;
        unsigned pf = 0;
        const char* pfw = (const char*)(bid < 128 ? wA : wB);
        unsigned pfo;
        { const int L = (bid & 127) * 24 + (lane < 24 ? lane : 0); pfo = (unsigned)((L / 48) * 6144 + (L % 48) * 128); }
#define P1_PF(base, off) asm volatile("s_mov_b32 exec_lo, %3\n\ts_mov_b32 exec_hi, %3\n\tglobal_load_dword %0, %1, %2\n\ts_mov_b64 exec, -1" : "+v"(pf) : "v"(off), "s"(base), "s"(mwarm) : "memory")
#define P1_GO(og_, od_, j) glds16m(vo[j], og_, (od_) + (j) * 1024, mstage)
#define P1_GE(eA_, eB_, ed_, j) glds16m(ve[j], (j) < 2 ? (eA_) : (eB_), (ed_) + (j) * 1024, mstage)
#define P1_GO2 P1_GO
#define P1_GE2 P1_GE
#define P1_EDST(xo_, h0, h1) (lds0 + edst + (w0 ? (xo_) : (whalf ? (h1) : (h0))))
#pragma unroll
        for (int j = 0; j < 4; ++j) P1_GO(xg, odst, j);
        { const unsigned ed = P1_EDST(0, 0, P1_WSLOT);
#pragma unroll
          for (int j = 0; j < 4; ++j) P1_GE(ebA, ebB, ed, j); }
        u32x4 gx0, gx1, gw0, gw1;
        {
            const char* px = (const char*)x + (size_t)(rt * 256 + head * 8 + w) * 2048;
            const unsigned go = lane * 32, z = 0;
            asm volatile("global_load_dwordx4 %0, %5, %6\n\tglobal_load_dwordx4 %1, %5, %6 offset:16\n\tglobal_load_dwordx4 %2, %5, %7\n\tglobal_load_dwordx4 %3, %5, %7 offset:16\n\t"
                         "global_load_dword %4, %8, %9 sc1"
                         : "=&v"(gx0), "=&v"(gx1), "=&v"(gw0), "=&v"(gw1), "=&v"(token) : "v"(go), "s"(px), "s"(gw), "v"(z), "s"(flags) : "memory");
        }
#pragma unroll
        for (int j = 0; j < 4; ++j) P1_GO(xg + 128, odst + P1_XSLOT, j);
        { const unsigned ed = P1_EDST(P1_XSLOT, 2 * P1_WSLOT, 3 * P1_WSLOT);
#pragma unroll
          for (int j = 0; j < 4; ++j) P1_GE(ebA + estride, ebB + estride, ed, j); }
        ebA += 2 * (size_t)estride; ebB += 2 * (size_t)estride;
        const int hh1 = lane >> 5;
        unsigned xa[4], wa;
#pragma unroll
        for (int u = 0; u < 4; ++u) xa[u] = lds0 + (wm * 64 + (lane & 31)) * 128 + (((2 * u + hh1) ^ ((lane >> 1) & 7)) << 4);
        wa = lds0 + P1_WBASE + wn * 6144 + (8 * hh1 + ((lane & 15) >> 2)) * 64 + ((((lane >> 4) & 1) ^ hh1) * 32) + (lane & 3) * 8;
#pragma unroll
        for (int i = 0; i < 2; ++i)
#pragma unroll
            for (int j = 0; j < 3; ++j) acc[i][j] = f32x16{};
        bf16x8 xf[2][2][2]; s16x4 wl[2][2][3], wh[2][2][3];
#define P1_WF(b, k, t) cat8(wl[b][k][t], wh[b][k][t])
#define P1_MM(b, k, r, t) acc[r][t] = ((t) < 2) ? MFMA32(P1_WF(b, k, t), xf[b][k][r], acc[r][t]) : MFMA32(xf[b][k][r], P1_WF(b, k, t), acc[r][t])
#define P1_RW(b, wa_, k, t) do { wl[b][k][t] = ldtr((wa_) + (t) * 2048 + (k) * 1024); wh[b][k][t] = ldtr((wa_) + (t) * 2048 + (k) * 1024 + 256); } while (0)
#define P1_STEP(HF, c, n, xo, u, wo, RD, G0, G1, G2, G3) do { \
            const unsigned xa0_ = xa[2 * (u)] + (xo), xa1_ = xa[2 * (u) + 1] + (xo), wa_ = wa + (wo); \
            P1_MM(c, 0, 0, 0); if (RD) { xf[n][0][0] = ld128(xa0_); P1_RW(n, wa_, 0, 0); } if (HF == 0) { G0; } SBAR(); \
            P1_MM(c, 0, 0, 1); if (RD) { P1_RW(n, wa_, 0, 1); } SBAR(); \
            P1_MM(c, 0, 0, 2); if (RD) { P1_RW(n, wa_, 0, 2); } if (HF == 1) { G0; } SBAR(); \
            P1_MM(c, 0, 1, 0); if (RD) { xf[n][0][1] = ld128(xa0_ + 4096); } if (HF == 0) { G1; } SBAR(); \
            P1_MM(c, 0, 1, 1); if (RD) { xf[n][1][0] = ld128(xa1_); } SBAR(); \
            P1_MM(c, 0, 1, 2); if (RD) { P1_RW(n, wa_, 1, 0); } if (HF == 1) { G1; } SBAR(); \
            P1_MM(c, 1, 0, 0); if (RD) { P1_RW(n, wa_, 1, 1); } if (HF == 0) { G2; } SBAR(); \
            P1_MM(c, 1, 0, 1); if (RD) { P1_RW(n, wa_, 1, 2); } SBAR(); \
            P1_MM(c, 1, 0, 2); if (RD) { xf[n][1][1] = ld128(xa1_ + 4096); } if (HF == 1) { G2; } SBAR(); \
            P1_MM(c, 1, 1, 0); if (HF == 0) { G3; } SBAR(); \
            P1_MM(c, 1, 1, 1); if (HF == 1) { G3; } SBAR(); \
            P1_MM(c, 1, 1, 2); SBAR(); } while (0)
#define P1_GATE() do {     \
            asm volatile("" : "+v"(gx0), "+v"(gx1), "+v"(gw0), "+v"(gw1), "+v"(token)); \
            float s_ = 0.f; \
            _Pragma("unroll") for (int i = 0; i < 4; ++i) { \
                s_ += __uint_as_float(gx0[i] << 16) * __uint_as_float(gw0[i] << 16) + __uint_as_float(gx0[i] & 0xffff0000u) * __uint_as_float(gw0[i] & 0xffff0000u); \
                s_ += __uint_as_float(gx1[i] << 16) * __uint_as_float(gw1[i] << 16) + __uint_as_float(gx1[i] & 0xffff0000u) * __uint_as_float(gw1[i] & 0xffff0000u); } \
            gate = s_; PIN(gate); \
            token = __builtin_amdgcn_readfirstlane(token) + 1u; } while (0)
        STAMP_AT(1);
        if (w7) asm volatile("s_waitcnt vmcnt(0)" ::: "memory");
        WAIT_BAR(8);
        STAMP_AT(2);
#pragma unroll
        for (int j = 0; j < 4; ++j) P1_GO(xg + 256, odst + 2 * P1_XSLOT, j);
        P1_GATE();
        if (w7) {
            P1_PF(pfw + 2 * (64 * 6144), pfo); P1_PF(pfw + 3 * (64 * 6144), pfo);
            const char* xl = (const char*)x + (size_t)bid * 16384; const unsigned lo = lane * 128;
            P1_PF(xl, lo); P1_PF(xl + 8192, lo);
            P1_PF(pfw + 4 * (64 * 6144), pfo); P1_PF(pfw + 5 * (64 * 6144), pfo);
            {
                unsigned long long pc; unsigned csz;
                asm volatile(".Lgm_code_begin:\n\ts_getpc_b64 %0\n\ts_mov_b32 %1, .Lgm_code_end-.Lgm_code_begin" : "=s"(pc), "=s"(csz));
#pragma unroll
                for (int k = 0; k < 4; ++k) { const unsigned o = (unsigned)(k * 64 + lane) * 128; const unsigned oc = o < csz ? o : 0u; P1_PF((const char*)pc, oc); }
            }
            {
                const int pg = idx * 64 + lane;
                const char* pa = pg < 1537 ? ws + (size_t)pg * 16384 : pg < 1793 ? (const char*)out + (size_t)(pg - 1537) * 16384 : ws;
                asm volatile("global_load_dword %0, %1, off" : "+v"(pf) : "v"(pa) : "memory");
            }
        }
        { xf[0][0][0] = ld128(xa[0]); xf[0][0][1] = ld128(xa[0] + 4096); xf[0][1][0] = ld128(xa[1]); xf[0][1][1] = ld128(xa[1] + 4096);
#pragma unroll
          for (int n = 0; n < 6; ++n) P1_RW(0, wa, n / 3, n % 3); }
        LBAR();
        unsigned xo0 = 0, xo1 = P1_XSLOT, xo2 = 2 * P1_XSLOT;
        unsigned wo0 = 0, wo1 = P1_WSLOT, wo2 = 2 * P1_WSLOT, wo3 = 3 * P1_WSLOT, wo4 = 4 * P1_WSLOT;
#define P1_TILE(HF, T, IE, IO, WA, WB, RDLAST, PFX) do { \
            const unsigned ed_ = P1_EDST(xo2, wo4, wo0), od_ = odst + xo0; const char* og_ = xg + ((T) + 3) * 128; \
            P1_STEP(HF, 0, 1, xo0, 1, wo1, P1_RD0, if (IE) P1_GE2(ebA, ebB, ed_, 0), if (IE) P1_GE2(ebA, ebB, ed_, 1), if (IE) P1_GE2(ebA, ebB, ed_, 2), if (IE) P1_GE2(ebA, ebB, ed_, 3)); \
            WA; \
            PFX; \
            P1_STEP(HF, 1, 0, xo1, 0, wo2, RDLAST, if (IO) P1_GO2(og_, od_, 0), if (IO) P1_GO2(og_, od_, 1), if (IO) P1_GO2(og_, od_, 2), if (IO) P1_GO2(og_, od_, 3)); \
            WB; \
            ebA += estride; ebB += estride; \
            { const unsigned t_ = xo0; xo0 = xo1; xo1 = xo2; xo2 = t_; const unsigned a_ = wo0, b_ = wo1; wo0 = wo2; wo1 = wo3; wo2 = wo4; wo3 = a_; wo4 = b_; } } while (0)
#define P1_WB(N) WAIT_BAR(N)
#define P1_RDL 1
#define P1_RD0 1
#define P1_W(HF, N) P1_WB(N)
#define P1_LOOP(HF) do { \
            for (int T = 0; T < 13; ++T) { \
                  \
                const char* pb = T < 10 ? pfw + (size_t)(T + 6) * (64 * 6144) : (const char*)(bid < 128 ? woA : woB) + (bid & 127) * 16384 + (T & 1) * 8192; \
                const unsigned po = T < 10 ? pfo : lane * 128; \
                P1_TILE(HF, T, 1, 1, P1_W(HF, 8), P1_W(HF, 12), P1_RDL, if (HF == 1) P1_PF(pb, po)); \
            } \
            P1_TILE(HF, 13, 1, 0, P1_W(HF, 8), P1_W(HF, 8), 1, NOP_); \
            P1_TILE(HF, 14, 0, 0, P1_W(HF, 0), NOP_, 1, NOP_); \
            P1_TILE(HF, 15, 0, 0, NOP_, NOP_, 0, NOP_); } while (0)
        if (w < 4) P1_LOOP(0); else P1_LOOP(1);
        asm volatile("s_waitcnt vmcnt(0)" : "+v"(pf)::"memory");
#undef P1_LOOP
#undef P1_GATE
#undef P1_W
#undef P1_TILE
#undef P1_STEP
#undef P1_RW
#undef P1_MM
#undef P1_WF
#undef P1_GO
#undef P1_GE
#undef P1_EDST
#undef P1_PF
    }
    STAMP_AT(3);
#pragma unroll
    for (int o = 32; o >= 1; o >>= 1) gate += __shfl_xor(gate, o);
    gate = 1.0f / (1.0f + __expf(-gate));
    LBAR();
    u32x4 keep[8];
    bf16x8 qown0, qown1;
    const unsigned mB = __builtin_amdgcn_readfirstlane(wn ? ~0u : 0u), mA = ~mB;
    char* kvg = ws + WS_KV + (size_t)((wn * 32 + head) * 8 + rt) * 32768;
    const unsigned kvo = wm * 4096 + l16;
    {
#pragma unroll
        for (int r = 0; r < 2; ++r)
#pragma unroll
            for (int s = 0; s < 2; ++s) {
                const unsigned o = kvo + r * 2048 + s * 1024;
                keep[r * 2 + s] = __builtin_bit_cast(u32x4, pack8(acc[r][1], s, 1.0f)); keep[4 + r * 2 + s] = __builtin_bit_cast(u32x4, pack8(acc[r][2], s, 1.0f));
                st16_wtm(o, kvg, keep[r * 2 + s], mB); st16_wtm(16384 + o, kvg, keep[4 + r * 2 + s], mB);
            }
        const f32x16 qk = wn ? acc[1][0] : acc[0][0], qs = wn ? acc[0][0] : acc[1][0];
        qown0 = pack8(qk, 0, QSCALE); qown1 = pack8(qk, 1, QSCALE);
        st128(lds0 + P2_Q + w * 2048 + l16, __builtin_bit_cast(u32x4, pack8(qs, 0, QSCALE))); st128(lds0 + P2_Q + w * 2048 + 1024 + l16, __builtin_bit_cast(u32x4, pack8(qs, 1, QSCALE)));
        if (wn) {
#pragma unroll
            for (int i = 0; i < 4; ++i) { const unsigned o = kvo + (i >> 1) * 2048 + (i & 1) * 1024; st128(lds0 + P2_B + o, keep[i]); st128(lds0 + P2_B + 16384 + o, keep[4 + i]); }
        }
    }
    LBAR();
    STAMP_AT(4);

    const int rb = rt * 8 + qw;
    const int ql = lane & 31, hh = lane >> 5;
    const char* kvA = ws + WS_KV + (size_t)(head * 8) * 32768;
    const char* kvB = ws + WS_KV + (size_t)((32 + head) * 8) * 32768;
    unsigned* fKB = flags + 64 + head * 8; unsigned* fO = flags + 64 + 256; unsigned* fKA = flags + 64 + 512 + head * 8; unsigned* fOB = flags + 64 + 768;
    const int rtp = rt > 0 ? rt - 1 : 0;
    const unsigned* fKAp = fKA + rtp;
    bf16x8 sel;
    { const short o = (((lane >> 4) & 1) == ((lane >> 2) & 1)) ? (short)0x3F80 : (short)0;
#pragma unroll
      for (int i = 0; i < 8; ++i) sel[i] = o; }
    const bf16x8 qx0 = ld128(lds0 + P2_Q + (w ^ 2) * 2048 + l16), qx1 = ld128(lds0 + P2_Q + (w ^ 2) * 2048 + 1024 + l16);
    const bf16x8 qa0 = wn ? qx0 : qown0, qa1 = wn ? qx1 : qown1, qb0 = wn ? qown0 : qx0, qb1 = wn ? qown1 : qx1;
    char* og = ws + WS_O + ((size_t)rb * 64 + head) * 2048;
    const unsigned oo = (ql >> 4) * 1024 + (hh * 16 + (ql & 15)) * 16;
    const int rt3 = (xcd & 3) * 4 + (idx & 3), ct = (xcd >> 2) * 8 + (idx >> 2);
    const int p3 = w & 3, half = w >> 2;
    const char* wsrcB = (const char*)woB + ct * 128 + (size_t)half * 512 * 2048; const char* wsrcA = (const char*)woA + ct * 128 + (size_t)half * 512 * 2048;
    const unsigned vw0 = (unsigned)((16 * p3 + (lane >> 3)) * 2048 + (((lane & 7) ^ (((lane >> 4) & 3) << 1)) << 4)), vw1 = vw0 + 8 * 2048;
    const unsigned wdst3 = lds0 + half * 8192 + p3 * 2048;
#define P3_SOFF(s) ((s) < 6 ? 65536 + (s) * P3_SLOT : (s) < 10 ? ((s) - 6) * P3_SLOT : 65536 + ((s) - 10) * P3_SLOT)
#define P3_GW(st, i) glds16((i) ? vw1 : vw0, ((st) < 8 ? wsrcB : wsrcA) + (size_t)((st) & 7) * (64 * 2048), wdst3 + P3_SOFF(st) + (i) * 1024)

#define EX(x) __builtin_amdgcn_exp2f(x)
#define P8(x) __builtin_bit_cast(bf16x8, x)
#define STEP(SC, SN, Q0, Q1, AK0, AK1, AV1, AV0, X) do { \
        SN = MFMA32(k0, Q0, negm); k0 = ld128(AK0); \
        phi[2] = cvtpk(t0, t1); phi[3] = cvtpk(t2, t3); t0 = EX(SC[0]); t1 = EX(SC[1]); PIN(phi); PIN(t0); PIN(t1); SBAR(); \
        O = MFMA32(v1, P8(phi), O); v1 = ld128(AV1); \
        t2 = EX(SC[2]); t3 = EX(SC[3]); plo[0] = cvtpk(t0, t1); PIN(t2); PIN(t3); PIN(plo); SBAR(); \
        L = MFMA16(sel, P8(phi), L); \
        plo[1] = cvtpk(t2, t3); t0 = EX(SC[4]); PIN(plo); PIN(t0); SBAR(); \
        SN = MFMA32(k1, Q1, SN); k1 = ld128(AK1); \
        t1 = EX(SC[5]); t2 = EX(SC[6]); t3 = EX(SC[7]); PIN(t1); PIN(t2); PIN(t3); SBAR(); \
        X; SBAR(); \
        plo[2] = cvtpk(t0, t1); t0 = EX(SC[8]); plo[3] = cvtpk(t2, t3); t1 = EX(SC[9]); PIN(plo); PIN(t0); PIN(t1); SBAR(); \
        O = MFMA32(v0, P8(plo), O); v0 = ld128(AV0); \
        t2 = EX(SC[10]); t3 = EX(SC[11]); phi[0] = cvtpk(t0, t1); PIN(t2); PIN(t3); PIN(phi); SBAR(); \
        L = MFMA16(sel, P8(plo), L); \
        phi[1] = cvtpk(t2, t3); \
        t0 = EX(SC[12]); t1 = EX(SC[13]); t2 = EX(SC[14]); t3 = EX(SC[15]); PIN(phi); PIN(t0); PIN(t1); PIN(t2); PIN(t3); SBAR(); \
    } while (0)
#define SG_OFF(i) (((i) >> 1) * 16384 + ((i) & 1) * 1024)
#define SG_LD(i, src, dK, dV) glds16c(l16, (src) + w * 2048 + SG_OFF(i), ((i) >> 1 ? (dV) : (dK)) + w * 2048 + ((i) & 1) * 1024)
    {
        f32x16 O = {}, negm, SA, SB; f32x4 L = {};
        bf16x8 k0, k1, v0, v1; u32x4 plo, phi; float t0 = 0.f, t1 = 0.f, t2 = 0.f, t3 = 0.f;
        unsigned cur = lds0 + P2_B + l16, nxt = cur + P2_SLOT;
        {
            k0 = ld128(cur); k1 = ld128(cur + 1024);
            SA = f32x16{}; SA = MFMA32(k0, qb0, SA); SA = MFMA32(k1, qb1, SA);
            const float m = rowmax16(SA);
#pragma unroll
            for (int r = 0; r < 16; ++r) { negm[r] = -m; SA[r] -= m; }
            k0 = ld128(cur + 2048); k1 = ld128(cur + 2048 + 1024);
            v0 = ld128(cur + 16384);
            v1 = bf16x8{}; phi = u32x4{}; plo = u32x4{};
        }
#define KAD(j, s) (((j) < 8 ? cur + (j) * 2048 : nxt + ((j) - 8) * 2048) + (s) * 1024)
#define BSTEP(SC, SN, j, X) STEP(SC, SN, qb0, qb1, KAD((j) + 2, 0), KAD((j) + 2, 1), KAD(j, 1) + 16384, KAD((j) + 1, 0) + 16384, X)
#define KEEP_A(i) st16_wtm(kvo + ((i) >> 2) * 16384 + (((i) >> 1) & 1) * 2048 + ((i) & 1) * 1024, kvg, keep[i], mA)
        unsigned pv;
        BSTEP(SA, SB, 0, NOP_);
        STAMP_AT(5);
        WAIT_BAR(0);
        STAMP_AT(6);
        if (tid == 0) flag_set(fKB + rt, token);
        BSTEP(SB, SA, 1, POLL_ISSUE(pv, fKB, 8));
        { const char* s1 = kvB + ((rt + 1) & 7) * 32768; const unsigned d = lds0 + P2_B + P2_SLOT;
          BSTEP(SA, SB, 2, POLL_CHECK(pv, fKB, 8); SG_LD(0, s1, d, d + 16384); SG_LD(1, s1, d, d + 16384); SG_LD(2, s1, d, d + 16384); SG_LD(3, s1, d, d + 16384)); }
        STAMP_AT(7);
        BSTEP(SB, SA, 3, KEEP_A(0); KEEP_A(1); KEEP_A(2); KEEP_A(3));
        BSTEP(SA, SB, 4, KEEP_A(4); KEEP_A(5); KEEP_A(6); KEEP_A(7));
        STAMP_AT(8);
        BSTEP(SB, SA, 5, NOP_);
        STAMP_AT(9);
        WAIT_BAR(0);
        STAMP_AT(10);
        if (tid == 0) flag_set(fKA + rt, token);
        const char* aown = kvA + rt * 32768;
        int slot = 0;
        for (int i = 0; i < 7; ++i) {
            const int s1 = slot == 2 ? 0 : slot + 1, s2 = s1 == 2 ? 0 : s1 + 1;
            const char* src = i < 6 ? kvB + ((rt + i + 2) & 7) * 32768 : kvA + rtp * 32768;
            const unsigned dK = i < 6 ? lds0 + P2_B + s2 * P2_SLOT : lds0 + P2_AK, dV = i < 6 ? dK + 16384 : lds0 + P2_AV;
            if (i == 6) POLL_CHECK(pv, fKAp, 1);
            BSTEP(SA, SB, 6, SG_LD(0, src, dK, dV)); BSTEP(SB, SA, 7, SG_LD(1, src, dK, dV));
            cur = nxt; nxt = lds0 + P2_B + s2 * P2_SLOT + l16;
            BSTEP(SA, SB, 0, SG_LD(2, src, dK, dV)); BSTEP(SB, SA, 1, SG_LD(3, src, dK, dV));
            BSTEP(SA, SB, 2, if (i == 0) { SG_LD(0, aown, lds0 + P2_AK + 16384, lds0 + P2_AV + 16384); SG_LD(1, aown, lds0 + P2_AK + 16384, lds0 + P2_AV + 16384); });
            BSTEP(SB, SA, 3, if (i == 0) { SG_LD(2, aown, lds0 + P2_AK + 16384, lds0 + P2_AV + 16384); SG_LD(3, aown, lds0 + P2_AK + 16384, lds0 + P2_AV + 16384); });
            BSTEP(SA, SB, 4, if (i == 5) { POLL_ISSUE(pv, fKAp, 1); });
            BSTEP(SB, SA, 5, NOP_);
            WAIT_BAR(0);
            slot = s1;
        }
        BSTEP(SA, SB, 6, NOP_); BSTEP(SB, SA, 7, NOP_);
        phi[2] = cvtpk(t0, t1); phi[3] = cvtpk(t2, t3);
        O = MFMA32(v1, P8(phi), O); L = MFMA16(sel, P8(phi), L);
        float l = L[0];
        if (!unit_ok(O, l)) attn_safe<false>(kvB, qb0, qb1, rb, lane, O, l);
        const float f = 1.0f / l;
        st16_wt(oo, og + 32 * 2048, __builtin_bit_cast(u32x4, pack8(O, 0, f))); st16_wt(oo + 512, og + 32 * 2048, __builtin_bit_cast(u32x4, pack8(O, 1, f)));
#undef KEEP_A
#undef BSTEP
#undef KAD
    }
    STAMP_AT(11);
    LBAR();
    {
        f32x16 O = {}, negm, SA, SB; f32x4 L = {};
        bf16x8 k0, k1, v0, v1; u32x4 plo, phi; float t0 = 0.f, t1 = 0.f, t2 = 0.f, t3 = 0.f;
        const unsigned ab = lds0 + P2_AK + qw * 2048 + l16;
        const int jmax = rt > 0 ? 8 : qw;
#define AAD(j, s) (ab + (8 - ((j) > 8 ? 8 : (j))) * 2048 + (s) * 1024)
        {
            k0 = ld128(AAD(0, 0)); k1 = ld128(AAD(0, 1));
            SA = f32x16{}; SA = MFMA32(k0, qa0, SA); SA = MFMA32(k1, qa1, SA);
#pragma unroll
            for (int r = 0; r < 16; ++r) if (keyof(r, hh) > ql) SA[r] = -INFINITY;
            const float m = rowmax16(SA);
#pragma unroll
            for (int r = 0; r < 16; ++r) { negm[r] = -m; SA[r] -= m; }
            k0 = ld128(AAD(1, 0)); k1 = ld128(AAD(1, 1));
            v0 = ld128(AAD(0, 0) + 32768);
            v1 = bf16x8{}; phi = u32x4{}; plo = u32x4{};
        }
#define ASTEP(SC, SN, j, X) do { if ((j) + 1 > jmax) { _Pragma("unroll") for (int r_ = 0; r_ < 16; ++r_) negm[r_] = -INFINITY; } \
        STEP(SC, SN, qa0, qa1, AAD((j) + 2, 0), AAD((j) + 2, 1), AAD(j, 1) + 32768, AAD((j) + 1, 0) + 32768, X); } while (0)
        ASTEP(SA, SB, 0, P3_GW(0, 0); P3_GW(0, 1)); ASTEP(SB, SA, 1, P3_GW(1, 0); P3_GW(1, 1)); ASTEP(SA, SB, 2, P3_GW(2, 0); P3_GW(2, 1)); ASTEP(SB, SA, 3, P3_GW(3, 0); P3_GW(3, 1));
        ASTEP(SA, SB, 4, P3_GW(4, 0); P3_GW(4, 1));
        WAIT_BAR(10);
        if (tid == 0) flag_set(fOB + rt * 32 + head, token);
        ASTEP(SB, SA, 5, P3_GW(5, 0); P3_GW(5, 1)); ASTEP(SA, SB, 6, NOP_); ASTEP(SB, SA, 7, NOP_);
#pragma unroll
        for (int r = 0; r < 16; ++r) if (keyof(r, hh) <= ql) SA[r] = -INFINITY;
        ASTEP(SA, SB, 8, NOP_);
        phi[2] = cvtpk(t0, t1); phi[3] = cvtpk(t2, t3);
        O = MFMA32(v1, P8(phi), O); L = MFMA16(sel, P8(phi), L);
        float l = L[0];
        if (!unit_ok(O, l)) attn_safe<true>(kvA, qa0, qa1, rb, lane, O, l);
        const float f = 1.0f / l;
        st16_wt(oo, og, __builtin_bit_cast(u32x4, pack8(O, 0, f))); st16_wt(oo + 512, og, __builtin_bit_cast(u32x4, pack8(O, 1, f)));
        if (lane == 0) __hip_atomic_store((GAS float*)(ws + WS_G) + rt * 256 + head * 8 + w, gate, __ATOMIC_RELAXED, __HIP_MEMORY_SCOPE_AGENT);
#undef ASTEP
#undef AAD
    }
#undef STEP
#undef EX
#undef P8
#undef SG_LD
#undef SG_OFF
    STAMP_AT(12);
    WAIT_BAR(0);
    if (tid == 0) flag_set(fO + rt * 32 + head, token);
    STAMP_AT(13);

    {
#pragma unroll
        for (int st = 6; st < 10; ++st) { P3_GW(st, 0); P3_GW(st, 1); }
        poll_spin(fOB + (rt3 >> 1) * 32, 32, token, lane);
        STAMP_AT(14);
        const char* osrc = ws + WS_O + ((size_t)(rt3 * 4 + p3) * 64 + half * 16) * 2048;
        bf16x8 of[7][4];
#define P3_GO(st, i) asm volatile("global_load_dwordx4 %0, %1, %2 offset:" #i "*1024 sc1" : "=v"(of[(st) % 7][i]) : "v"(l16), "s"(osrc + ((st) < 8 ? 65536 : 0) + ((st) & 7) * 4096) : "memory")
#define P3_GO4(st) do { P3_GO(st, 0); P3_GO(st, 1); P3_GO(st, 2); P3_GO(st, 3); } while (0)
        P3_GO4(0); P3_GO4(1); P3_GO4(2); P3_GO4(3); P3_GO4(4); P3_GO4(5);
        float gq[2];
#define P3_GG() do { const float* gp_ = (const float*)(ws + WS_G) + rt3 * 128 + p3 * 32; const unsigned go_ = (lane & 15) * 4; \
          asm volatile("global_load_dword %0, %2, %3 sc1\n\tglobal_load_dword %1, %2, %3 offset:64 sc1" : "=&v"(gq[0]), "=&v"(gq[1]) : "v"(go_), "s"(gp_) : "memory"); } while (0)
        const int g4 = lane >> 4, q4 = (lane & 15) >> 2;
        unsigned wa[4];
        { const unsigned a0 = (16 * (g4 >> 1) + 4 * (g4 & 1) + q4) * 128 + ((2 * (g4 & 1) + (q4 >> 1)) * 32) + (lane & 3) * 8;
#pragma unroll
          for (int n = 0; n < 4; ++n) wa[n] = lds0 + half * 8192 + (a0 ^ (n << 5)) + P3_SOFF(0); }
        f32x4 ac[2][2][4];
#pragma unroll
        for (int i = 0; i < 4; ++i)
#pragma unroll
            for (int j = 0; j < 4; ++j) ac[i >> 1][i & 1][j] = f32x4{};
        s16x4 wl[2][4], wh[2][4];
#define P3_RW(buf, k_, n_) do { wl[buf][n_] = ldtr(wa[n_] + (k_) * 4096); wh[buf][n_] = ldtr(wa[n_] + (k_) * 4096 + 1024); } while (0)
#define P3_MMA(st, k_, n_, wf_) do { ac[(st) >> 3][0][n_] = MFMA16(wf_, of[(st) % 7][2 * (k_)], ac[(st) >> 3][0][n_]); ac[(st) >> 3][1][n_] = MFMA16(wf_, of[(st) % 7][2 * (k_) + 1], ac[(st) >> 3][1][n_]); } while (0)
#define P3_K32(st, k_, X0, X1, X2, X3) do { \
            if ((k_) == 1 && (st) + 1 < 16) { _Pragma("unroll") for (int n_ = 0; n_ < 4; ++n_) { wa[n_] += (unsigned)(P3_SOFF((st) + 1) - P3_SOFF(st)); PIN(wa[n_]); } } \
            _Pragma("unroll") for (int n_ = 0; n_ < 4; ++n_) { \
                const bf16x8 wf_ = cat8(wl[k_][n_], wh[k_][n_]); \
                P3_MMA(st, k_, n_, wf_); \
                if ((k_) == 0) P3_RW(1, 1, n_); else if ((st) + 1 < 16) P3_RW(0, 0, n_); \
                if (n_ == 0) { X0; } if (n_ == 1) { X1; } if (n_ == 2) { X2; } if (n_ == 3) { X3; } \
                SBAR(); } } while (0)
#define P3_OW(st, NW) asm volatile("s_waitcnt vmcnt(" #NW ")" : "+v"(of[(st) % 7][0]), "+v"(of[(st) % 7][1]), "+v"(of[(st) % 7][2]), "+v"(of[(st) % 7][3])::"memory")
#define P3_STAGE(st, NW, BAR, nx) do { \
            P3_OW(st, NW); if (BAR) LBAR(); \
            P3_K32(st, 0, NOP_, NOP_, NOP_, NOP_); \
            if ((nx) < 16) { P3_K32(st, 1, P3_GO(nx, 0), P3_GO(nx, 1), P3_GO(nx, 2), P3_GO(nx, 3)); } \
            else { P3_K32(st, 1, NOP_, NOP_, NOP_, NOP_); } \
        } while (0)
        LBAR();
#pragma unroll
        for (int n_ = 0; n_ < 4; ++n_) P3_RW(0, 0, n_);
        unsigned pv;
        const unsigned* fOAq = fO + (rt3 >> 1) * 32;
        P3_STAGE(0, 20, 0, 6); P3_STAGE(1, 20, 0, 7); P3_STAGE(2, 20, 0, 16);
        P3_OW(3, 16); LBAR();
        P3_K32(3, 0, POLL_ISSUE(pv, fOAq, 32), P3_GW(10, 0), P3_GW(10, 1), P3_GW(11, 0)); P3_K32(3, 1, P3_GW(11, 1), P3_GW(12, 0), P3_GW(12, 1), NOP_);
        asm volatile("s_waitcnt vmcnt(6)" : "+v"(pv), "+v"(of[4][0]), "+v"(of[4][1]), "+v"(of[4][2]), "+v"(of[4][3])::"memory");
        if (!__all(pv == token)) poll_spin(fOAq, 32, token, lane);
        P3_K32(4, 0, P3_GG(), P3_GO(8, 0); P3_GO(8, 1), P3_GO(8, 2); P3_GO(8, 3), P3_GO(9, 0); P3_GO(9, 1)); P3_K32(4, 1, P3_GO(9, 2); P3_GO(9, 3), P3_GO(10, 0); P3_GO(10, 1), P3_GO(10, 2), P3_GO(10, 3));
        P3_STAGE(5, 29, 0, 11);
        P3_OW(6, 29); LBAR();
        P3_K32(6, 0, P3_GW(13, 0), P3_GW(13, 1), P3_GW(14, 0), P3_GW(14, 1)); P3_K32(6, 1, P3_GW(15, 0); P3_GW(15, 1); P3_GO(12, 0), P3_GO(12, 1), P3_GO(12, 2), P3_GO(12, 3));
        P3_STAGE(7, 35, 0, 13); P3_STAGE(8, 26, 0, 14);
        P3_STAGE(9, 26, 1, 15); P3_STAGE(10, 26, 0, 16); P3_STAGE(11, 22, 0, 16);
        P3_STAGE(12, 12, 1, 16); P3_STAGE(13, 8, 0, 16); P3_STAGE(14, 4, 0, 16); P3_STAGE(15, 0, 0, 16);
        asm volatile("" : "+v"(gq[0]), "+v"(gq[1]));
        f32x4 r2[2][4];
#pragma unroll
        for (int n = 0; n < 4; ++n) { r2[0][n] = ac[1][0][n] * gq[0] + ac[0][0][n] * (1.0f - gq[0]); r2[1][n] = ac[1][1][n] * gq[1] + ac[0][1][n] * (1.0f - gq[1]); }
        LBAR();
        LAS f32x4* mine = (LAS f32x4*)(uintptr_t)(lds0 + w * 4096 + l16);
        LAS f32x4* theirs = (LAS f32x4*)(uintptr_t)(lds0 + (w ^ 4) * 4096 + l16);
#pragma unroll
        for (int q = 0; q < 2; ++q) { mine[(q * 2) * 64] = half ? r2[q][0] : r2[q][2]; mine[(q * 2 + 1) * 64] = half ? r2[q][1] : r2[q][3]; }
        LBAR();
#pragma unroll
        for (int q = 0; q < 2; ++q)
#pragma unroll
            for (int e = 0; e < 2; ++e) {
                const f32x4 r = (half ? r2[q][2 + e] : r2[q][e]) + theirs[(q * 2 + e) * 64];
                u16* op = out + (size_t)(rt3 * 128 + p3 * 32 + q * 16 + (lane & 15)) * 1024 + ct * 64 + half * 32 + e * 16 + g4 * 4;
                *(u32x2*)op = u32x2{cvtpk(r[0], r[1]), cvtpk(r[2], r[3])};
            }
#undef P3_GO
#undef P3_GO4
#undef P3_MMA
#undef P3_GG
#undef P3_RW
#undef P3_K32
#undef P3_OW
#undef P3_STAGE
    }
#undef P3_GW
#undef P3_SOFF
    if (bid == 0 && w == 0) {
        const unsigned long long t0 = __builtin_amdgcn_s_memrealtime();
        for (;;) {
            bool ok = true;
#pragma unroll
            for (int i = 0; i < 4; ++i) ok = ok && (__hip_atomic_load((GAS unsigned*)(fO + i * 64 + lane), __ATOMIC_RELAXED, __HIP_MEMORY_SCOPE_AGENT) == token);
            if (__all(ok)) break;
            if (__builtin_amdgcn_s_memrealtime() - t0 > 400000ull) break;
            __builtin_amdgcn_s_sleep(1);
        }
        if (lane == 0) flag_set(flags, token);
    }
    asm volatile(".Lgm_code_end:");
    STAMP_AT(15);
    STAMP_FLUSH(ws, 16);
}

extern "C" void kernel_launch(void* const* d_in, const int* in_sizes, int n_in, void* d_out, int out_size, void* d_ws, size_t ws_size, hipStream_t stream) {
    (void)in_sizes; (void)n_in; (void)out_size; (void)ws_size;
    const u16* x = (const u16*)d_in[0]; const u16* wqkvA = (const u16*)d_in[1]; const u16* woA = (const u16*)d_in[2];
    const u16* wqkvB = (const u16*)d_in[4]; const u16* woB = (const u16*)d_in[5]; const u16* gw = (const u16*)d_in[6];
    (void)hipFuncSetAttribute((const void*)k_fused, hipFuncAttributeMaxDynamicSharedMemorySize, LDS_BYTES);
    k_fused<<<256, 512, LDS_BYTES, stream>>>(x, wqkvA, wqkvB, woA, woB, gw, (char*)d_ws, (u16*)d_out);
}
```

```cpp
#include <hip/hip_runtime.h>
#include <hip/hip_bf16.h>
#include <stdint.h>
#pragma clang diagnostic ignored "-Winline-asm"

typedef unsigned short u16;
typedef short bf16x8 __attribute__((ext_vector_type(8)));
typedef short s16x4 __attribute__((ext_vector_type(4)));
typedef float f32x16 __attribute__((ext_vector_type(16)));
typedef float f32x4 __attribute__((ext_vector_type(4)));
typedef float f32x2 __attribute__((ext_vector_type(2)));
typedef __bf16 bf16x2_t __attribute__((ext_vector_type(2)));
typedef unsigned u32x4 __attribute__((ext_vector_type(4)));
typedef unsigned u32x2 __attribute__((ext_vector_type(2)));
#define LAS __attribute__((address_space(3)))
#define GAS __attribute__((address_space(1)))
#define DI __device__ __forceinline__
#define MFMA32(a, b, c) __builtin_amdgcn_mfma_f32_32x32x16_bf16((a), (b), (c), 0, 0, 0)
#define MFMA16(a, b, c) __builtin_amdgcn_mfma_f32_16x16x32_bf16((a), (b), (c), 0, 0, 0)
#define SBAR() __builtin_amdgcn_sched_barrier(0)
#define PIN(x) asm volatile("" : "+v"(x))
#define WAITCNT_VM_LGKM0(N) __builtin_amdgcn_s_waitcnt(((N) & 15) | (((N) >> 4) << 14) | 0x70)
#define WAIT_BAR(N) do { asm volatile("" ::: "memory"); WAITCNT_VM_LGKM0(N); __builtin_amdgcn_s_barrier(); asm volatile("" ::: "memory"); } while (0)
#define LBAR() WAIT_BAR(63)
#define NOP_ do {} while (0)

constexpr float QSCALE = 0.17677669529663687f * 1.4426950408889634f;
constexpr size_t WS_KV = 0;
constexpr size_t WS_O = 16u << 20;
constexpr size_t WS_G = 24u << 20;
constexpr size_t WS_F = (24u << 20) + 8192;
constexpr int LDS_BYTES = 163840;

DI unsigned cvtpk(float lo, float hi) { f32x2 v = {lo, hi}; bf16x2_t b = __builtin_convertvector(v, bf16x2_t); return __builtin_bit_cast(unsigned, b); }
DI bf16x8 pack8(const f32x16& x, int s, float sc) {
    u32x4 w = {cvtpk(x[8 * s] * sc, x[8 * s + 1] * sc), cvtpk(x[8 * s + 2] * sc, x[8 * s + 3] * sc), cvtpk(x[8 * s + 4] * sc, x[8 * s + 5] * sc), cvtpk(x[8 * s + 6] * sc, x[8 * s + 7] * sc)};
    return __builtin_bit_cast(bf16x8, w);
}
DI u32x4 pack44(const f32x4& a, const f32x4& b, float sc) { return u32x4{cvtpk(a[0] * sc, a[1] * sc), cvtpk(a[2] * sc, a[3] * sc), cvtpk(b[0] * sc, b[1] * sc), cvtpk(b[2] * sc, b[3] * sc)}; }
DI void glds16(unsigned voff, const void* sbase, unsigned lds_dst) {
    asm volatile("s_mov_b32 m0, %2\n\ts_nop 0\n\tglobal_load_lds_dwordx4 %0, %1" ::"v"(voff), "s"(sbase), "s"(lds_dst) : "memory", "m0");
}
DI void glds16c(unsigned voff, const void* sbase, unsigned lds_dst) {
    asm volatile("s_mov_b32 m0, %2\n\ts_nop 0\n\tglobal_load_lds_dwordx4 %0, %1 sc1" ::"v"(voff), "s"(sbase), "s"(lds_dst) : "memory", "m0");
}
DI void glds16m(unsigned voff, const void* sbase, unsigned lds_dst, unsigned mask) {
    asm volatile("s_mov_b32 m0, %2\n\ts_mov_b32 exec_lo, %3\n\ts_mov_b32 exec_hi, %3\n\tglobal_load_lds_dwordx4 %0, %1\n\ts_mov_b64 exec, -1" ::"v"(voff), "s"(sbase), "s"(lds_dst), "s"(mask) : "memory", "m0");
}
DI void st16_wt(unsigned voff, void* sbase, u32x4 v) { asm volatile("global_store_dwordx4 %0, %1, %2 sc1\n\ts_nop 1" ::"v"(voff), "v"(v), "s"(sbase) : "memory"); }
DI void st16_wtm(unsigned voff, void* sbase, u32x4 v, unsigned mask) {
    asm volatile("s_mov_b32 exec_lo, %3\n\ts_mov_b32 exec_hi, %3\n\tglobal_store_dwordx4 %0, %1, %2 sc1\n\ts_mov_b64 exec, -1\n\ts_nop 0" ::"v"(voff), "v"(v), "s"(sbase), "s"(mask) : "memory");
}
typedef short v4i16_t __attribute__((ext_vector_type(4)));
DI s16x4 ldtr(unsigned a) { return __builtin_bit_cast(s16x4, __builtin_amdgcn_ds_read_tr16_b64_v4i16((LAS v4i16_t*)(uintptr_t)a)); }
DI bf16x8 ld128(unsigned a) { return *(const LAS bf16x8*)(uintptr_t)a; }
DI void st128(unsigned a, u32x4 v) { *(LAS u32x4*)(uintptr_t)a = v; }
DI bf16x8 cat8(s16x4 lo, s16x4 hi) { return __builtin_shufflevector(lo, hi, 0, 1, 2, 3, 4, 5, 6, 7); }

#define STAMP_DECL do {} while (0)
#define STAMP_AT(i) do {} while (0)
#define STAMP_FLUSH(wsp, n) do {} while (0)

DI void flag_set(unsigned* f, unsigned v) { __hip_atomic_store((GAS unsigned*)f, v, __ATOMIC_RELAXED, __HIP_MEMORY_SCOPE_AGENT); }
DI void poll_spin(const unsigned* f, int n, unsigned want, int lane) {
    const unsigned long long t0 = __builtin_amdgcn_s_memrealtime();
    for (;;) {
        unsigned v = want;
        if (lane < n) v = __hip_atomic_load((GAS unsigned*)(f + lane), __ATOMIC_RELAXED, __HIP_MEMORY_SCOPE_AGENT);
        if (__all(v == want)) break;
        if (__builtin_amdgcn_s_memrealtime() - t0 > 400000ull) break;
        __builtin_amdgcn_s_sleep(1);
    }
    __builtin_amdgcn_fence(__ATOMIC_ACQUIRE, "wavefront");
}
#define POLL_ISSUE(pv, f, n) do { const unsigned* p_ = (f) + (lane < (n) ? lane : 0); asm volatile("global_load_dword %0, %1, off sc1" : "=v"(pv) : "v"(p_) : "memory"); } while (0)
#define POLL_CHECK(pv, f, n) do { asm volatile("s_waitcnt vmcnt(0)" : "+v"(pv)::"memory"); if (!__all((pv) == token)) poll_spin(f, n, token, lane); } while (0)

DI float xhalf_max(float m) { auto rr = __builtin_amdgcn_permlane32_swap(__float_as_uint(m), __float_as_uint(m), false, false); return fmaxf(__uint_as_float(rr[0]), __uint_as_float(rr[1])); }
DI float xhalf_sum(float m) { auto rr = __builtin_amdgcn_permlane32_swap(__float_as_uint(m), __float_as_uint(m), false, false); return __uint_as_float(rr[0]) + __uint_as_float(rr[1]); }
DI int keyof(int r, int h) { return (r & 3) + 8 * (r >> 2) + 4 * h; }
DI float rowmax16(const f32x16& s) {
    float a = fmaxf(fmaxf(s[0], s[1]), s[2]), b = fmaxf(fmaxf(s[3], s[4]), s[5]);
#pragma unroll
    for (int r = 6; r < 16; r += 2) a = fmaxf(fmaxf(a, s[r]), s[r + 1]);
    return xhalf_max(fmaxf(a, b));
}
DI bf16x8 packp(const f32x16& p, int s) {
    u32x4 w = {cvtpk(p[8 * s], p[8 * s + 1]), cvtpk(p[8 * s + 2], p[8 * s + 3]), cvtpk(p[8 * s + 4], p[8 * s + 5]), cvtpk(p[8 * s + 6], p[8 * s + 7])};
    return __builtin_bit_cast(bf16x8, w);
}
template <bool WIN>
DI void attn_safe(const char* kv, bf16x8 q0, bf16x8 q1, int rb, int lane, f32x16& Oout, float& lout) {
    const int ql = lane & 31, h = lane >> 5;
    __builtin_amdgcn_fence(__ATOMIC_ACQUIRE, "agent");
    asm volatile("s_waitcnt vmcnt(0)" ::: "memory");
    float m = -INFINITY, l = 0.f; f32x16 O = {};
    const int nb = WIN ? (rb < 8 ? rb + 1 : 9) : 64;
    for (int j = 0; j < nb; ++j) {
        const int kb = WIN ? rb - j : j;
        const char* kp = kv + (kb >> 3) * 32768 + (kb & 7) * 2048 + lane * 16;
        const bf16x8 k0 = *(const bf16x8*)kp, k1 = *(const bf16x8*)(kp + 1024), v0 = *(const bf16x8*)(kp + 16384), v1 = *(const bf16x8*)(kp + 16384 + 1024);
        f32x16 s = {}; s = MFMA32(k0, q0, s); s = MFMA32(k1, q1, s);
        if (WIN) {
#pragma unroll
            for (int r = 0; r < 16; ++r) { const int kl = keyof(r, h); if ((j == 0 && kl > ql) || (j == 8 && kl <= ql)) s[r] = -INFINITY; }
        }
        const float mn = fmaxf(m, rowmax16(s));
        const float al = __builtin_amdgcn_exp2f(m - mn); m = mn;
        float ps = 0.f;
#pragma unroll
        for (int r = 0; r < 16; ++r) { s[r] = __builtin_amdgcn_exp2f(s[r] - mn); }
        const bf16x8 p0 = packp(s, 0), p1 = packp(s, 1);
#pragma unroll
        for (int r = 0; r < 8; ++r) { ps += __uint_as_float((unsigned)(u16)p0[r] << 16) + __uint_as_float((unsigned)(u16)p1[r] << 16); }
        l = l * al + ps;
#pragma unroll
        for (int r = 0; r < 16; ++r) O[r] *= al;
        O = MFMA32(v0, p0, O); O = MFMA32(v1, p1, O);
    }
    Oout = O; lout = xhalf_sum(l);
}
DI bool unit_ok(const f32x16& O, float l) {
    bool ok = l <= 1.0995116e12f;
    float a = 0.f;
#pragma unroll
    for (int r = 0; r < 16; ++r) a = fmaxf(a, __builtin_fabsf(O[r]));
    bool okO = a <= 3.0e38f;
#pragma unroll
    for (int r = 0; r < 16; ++r) okO = okO && (O[r] == O[r]);
    return __all(ok && okO);
}

constexpr int P1_XSLOT = 32768, P1_WBASE = 3 * P1_XSLOT, P1_WSLOT = 12288;
constexpr int P2_AK = 0, P2_AV = 32768, P2_B = 65536, P2_SLOT = 32768;
constexpr int P2_Q = P2_B + 2 * P2_SLOT;
constexpr int P3_SLOT = 16384;

__global__ __launch_bounds__(512, 2) void k_fused(const u16* __restrict__ x, const u16* __restrict__ wA, const u16* __restrict__ wB, const u16* __restrict__ woA,
                                                  const u16* __restrict__ woB, const u16* __restrict__ gw, char* __restrict__ ws, u16* __restrict__ out) {
    extern __shared__ __attribute__((aligned(16))) char lds[];
    const unsigned lds0 = (unsigned)(uintptr_t)(LAS char*)lds;
    STAMP_DECL; STAMP_AT(0);
    const int tid = threadIdx.x, lane = tid & 63;
    const int w = __builtin_amdgcn_readfirstlane(tid >> 6);
    const int bid = blockIdx.x, xcd = bid & 7, idx = bid >> 3;
    const int head = xcd * 4 + (idx >> 3), rt = idx & 7;
    const int wm = (w & 1) | ((w >> 2) << 1), wn = (w >> 1) & 1, qw = 2 * wm + wn;
    unsigned* flags = (unsigned*)(ws + WS_F);
    const unsigned l16 = lane * 16;
    float gate;
    unsigned token;

    f32x16 acc[2][3];
    {
        const char* xg = (const char*)x + (size_t)rt * 256 * 2048;
        const bool w0 = w == 0, w7 = w == 7;
        const int whalf = w >= 4;
        unsigned vo[4], ve[4];
        const char* ebA; const char* ebB; unsigned estride, edst;
        {
            const int r8 = lane >> 3, c8 = lane & 7;
            const int sc = (lane & 3) ^ (((lane >> 5) & 1) << 1);
#pragma unroll
            for (int j = 0; j < 4; ++j) {
                const int p = 4 * (w7 ? 0 : w) + j;
                vo[j] = (unsigned)((8 * p + r8) * 2048 + ((c8 ^ ((4 * p + (lane >> 4)) & 7)) << 4));
                const int pe = 28 + j;
                const unsigned vxe = (unsigned)((8 * pe + r8) * 2048 + ((c8 ^ ((4 * pe + (lane >> 4)) & 7)) << 4));
                const int i = 4 * ((w7 || w0 ? 1 : w) - 1) + j, ii = i >= 12 ? i - 12 : i, seg = ii >> 1, rg = ii & 1, type = seg >= 3 ? seg - 3 : seg;
                const unsigned vwe = (unsigned)((whalf * 32 + rg * 16 + (lane >> 2)) * 6144 + type * 2048 + (sc << 4));
                ve[j] = w0 ? vxe : vwe;
            }
            const int i0 = 4 * ((w7 || w0 ? 1 : w) - 1), ii0 = i0 >= 12 ? i0 - 12 : i0;
            ebA = w0 ? xg : (const char*)(ii0 >= 6 ? wB : wA) + head * 64;
            ebB = w0 ? xg : (const char*)(ii0 + 2 >= 6 ? wB : wA) + head * 64;
            estride = w0 ? 128u : 64u * 6144u;
            edst = w0 ? 28 * 1024 : P1_WBASE + ii0 * 1024;
        }
        const unsigned odst = lds0 + (w7 ? 0 : w) * 4096;
        const unsigned mstage = __builtin_amdgcn_readfirstlane(w7 ? 0u : ~0u), mwarm = ~mstage;
        const char* pfw = (const char*)(bid < 128 ? wA : wB);
        unsigned pfo;
        { const int L = (bid & 127) * 24 + (lane < 24 ? lane : 0); pfo = (unsigned)((L / 48) * 6144 + (L % 48) * 128); }
#define P1_PF(base, off) asm volatile("s_mov_b32 m0, %3\n\ts_mov_b32 exec_lo, %2\n\ts_mov_b32 exec_hi, %2\n\tglobal_load_lds_dword %0, %1\n\ts_mov_b64 exec, -1" ::"v"(off), "s"(base), "s"(mwarm), "s"(lds0 + P1_WBASE + 5 * P1_WSLOT) : "memory", "m0")
#define P1_GO(og_, od_, j) glds16m(vo[j], og_, (od_) + (j) * 1024, mstage)
#define P1_GE(eA_, eB_, ed_, j) glds16m(ve[j], (j) < 2 ? (eA_) : (eB_), (ed_) + (j) * 1024, mstage)
#define P1_GO2 P1_GO
#define P1_GE2 P1_GE
#define P1_EDST(xo_, h0, h1) (lds0 + edst + (w0 ? (xo_) : (whalf ? (h1) : (h0))))
#pragma unroll
        for (int j = 0; j < 4; ++j) P1_GO(xg, odst, j);
        { const unsigned ed = P1_EDST(0, 0, P1_WSLOT);
#pragma unroll
          for (int j = 0; j < 4; ++j) P1_GE(ebA, ebB, ed, j); }
        u32x4 gx0, gx1, gw0, gw1;
        {
            const char* px = (const char*)x + (size_t)(rt * 256 + head * 8 + w) * 2048;
            const unsigned go = lane * 32, z = 0;
            asm volatile("global_load_dwordx4 %0, %5, %6\n\tglobal_load_dwordx4 %1, %5, %6 offset:16\n\tglobal_load_dwordx4 %2, %5, %7\n\tglobal_load_dwordx4 %3, %5, %7 offset:16\n\t"
                         "global_load_dword %4, %8, %9 sc1"
                         : "=&v"(gx0), "=&v"(gx1), "=&v"(gw0), "=&v"(gw1), "=&v"(token) : "v"(go), "s"(px), "s"(gw), "v"(z), "s"(flags) : "memory");
        }
#pragma unroll
        for (int j = 0; j < 4; ++j) P1_GO(xg + 128, odst + P1_XSLOT, j);
        { const unsigned ed = P1_EDST(P1_XSLOT, 2 * P1_WSLOT, 3 * P1_WSLOT);
#pragma unroll
          for (int j = 0; j < 4; ++j) P1_GE(ebA + estride, ebB + estride, ed, j); }
        ebA += 2 * (size_t)estride; ebB += 2 * (size_t)estride;
        const int hh1 = lane >> 5;
        unsigned xa[4], wa;
#pragma unroll
        for (int u = 0; u < 4; ++u) xa[u] = lds0 + (wm * 64 + (lane & 31)) * 128 + (((2 * u + hh1) ^ ((lane >> 1) & 7)) << 4);
        wa = lds0 + P1_WBASE + wn * 6144 + (8 * hh1 + ((lane & 15) >> 2)) * 64 + ((((lane >> 4) & 1) ^ hh1) * 32) + (lane & 3) * 8;
#pragma unroll
        for (int i = 0; i < 2; ++i)
#pragma unroll
            for (int j = 0; j < 3; ++j) acc[i][j] = f32x16{};
        bf16x8 xf[2][2][2]; s16x4 wl[2][2][3], wh[2][2][3];
#define P1_WF(b, k, t) cat8(wl[b][k][t], wh[b][k][t])
#define P1_MM(b, k, r, t) acc[r][t] = ((t) < 2) ? MFMA32(P1_WF(b, k, t), xf[b][k][r], acc[r][t]) : MFMA32(xf[b][k][r], P1_WF(b, k, t), acc[r][t])
#define P1_RW(b, wa_, k, t) do { wl[b][k][t] = ldtr((wa_) + (t) * 2048 + (k) * 1024); wh[b][k][t] = ldtr((wa_) + (t) * 2048 + (k) * 1024 + 256); } while (0)
#define P1_STEP(HF, c, n, xo, u, wo, RD, G0, G1, G2, G3) do { \
            const unsigned xa0_ = xa[2 * (u)] + (xo), xa1_ = xa[2 * (u) + 1] + (xo), wa_ = wa + (wo); \
            P1_MM(c, 0, 0, 0); if (RD) { xf[n][0][0] = ld128(xa0_); P1_RW(n, wa_, 0, 0); } if (HF == 0) { G0; } SBAR(); \
            P1_MM(c, 0, 0, 1); if (RD) { P1_RW(n, wa_, 0, 1); } SBAR(); \
            P1_MM(c, 0, 0, 2); if (RD) { P1_RW(n, wa_, 0, 2); } if (HF == 1) { G0; } SBAR(); \
            P1_MM(c, 0, 1, 0); if (RD) { xf[n][0][1] = ld128(xa0_ + 4096); } if (HF == 0) { G1; } SBAR(); \
            P1_MM(c, 0, 1, 1); if (RD) { xf[n][1][0] = ld128(xa1_); } SBAR(); \
            P1_MM(c, 0, 1, 2); if (RD) { P1_RW(n, wa_, 1, 0); } if (HF == 1) { G1; } SBAR(); \
            P1_MM(c, 1, 0, 0); if (RD) { P1_RW(n, wa_, 1, 1); } if (HF == 0) { G2; } SBAR(); \
            P1_MM(c, 1, 0, 1); if (RD) { P1_RW(n, wa_, 1, 2); } SBAR(); \
            P1_MM(c, 1, 0, 2); if (RD) { xf[n][1][1] = ld128(xa1_ + 4096); } if (HF == 1) { G2; } SBAR(); \
            P1_MM(c, 1, 1, 0); if (HF == 0) { G3; } SBAR(); \
            P1_MM(c, 1, 1, 1); if (HF == 1) { G3; } SBAR(); \
            P1_MM(c, 1, 1, 2); SBAR(); } while (0)
#define P1_GATE() do {     \
            asm volatile("" : "+v"(gx0), "+v"(gx1), "+v"(gw0), "+v"(gw1), "+v"(token)); \
            float s_ = 0.f; \
            _Pragma("unroll") for (int i = 0; i < 4; ++i) { \
                s_ += __uint_as_float(gx0[i] << 16) * __uint_as_float(gw0[i] << 16) + __uint_as_float(gx0[i] & 0xffff0000u) * __uint_as_float(gw0[i] & 0xffff0000u); \
                s_ += __uint_as_float(gx1[i] << 16) * __uint_as_float(gw1[i] << 16) + __uint_as_float(gx1[i] & 0xffff0000u) * __uint_as_float(gw1[i] & 0xffff0000u); } \
            gate = s_; PIN(gate); \
            token = __builtin_amdgcn_readfirstlane(token) + 1u; } while (0)
        STAMP_AT(1);
        if (w7) asm volatile("s_waitcnt vmcnt(0)" ::: "memory");
        WAIT_BAR(8);
        STAMP_AT(2);
#pragma unroll
        for (int j = 0; j < 4; ++j) P1_GO(xg + 256, odst + 2 * P1_XSLOT, j);
        P1_GATE();
        if (w7) {
            P1_PF(pfw + 2 * (64 * 6144), pfo); P1_PF(pfw + 3 * (64 * 6144), pfo);
            const char* xl = (const char*)x + (size_t)bid * 16384; const unsigned lo = lane * 128;
            P1_PF(xl, lo); P1_PF(xl + 8192, lo);
            P1_PF(pfw + 4 * (64 * 6144), pfo); P1_PF(pfw + 5 * (64 * 6144), pfo);
            {
                unsigned long long pc; unsigned csz;
                asm volatile(".Lgm_code_begin:\n\ts_getpc_b64 %0\n\ts_mov_b32 %1, .Lgm_code_end-.Lgm_code_begin" : "=s"(pc), "=s"(csz));
#pragma unroll
                for (int k = 0; k < 4; ++k) { const unsigned o = (unsigned)(k * 64 + lane) * 128; const unsigned oc = o < csz ? o : 0u; P1_PF((const char*)pc, oc); }
            }
            {
                const int pg = idx * 64 + lane;
                const char* pa = pg < 1537 ? ws + (size_t)pg * 16384 : pg < 1793 ? (const char*)out + (size_t)(pg - 1537) * 16384 : ws;
                asm volatile("s_mov_b32 m0, %1\n\ts_nop 0\n\tglobal_load_lds_dword %0, off" ::"v"(pa), "s"(lds0 + P1_WBASE + 5 * P1_WSLOT) : "memory", "m0");
            }
        }
        { xf[0][0][0] = ld128(xa[0]); xf[0][0][1] = ld128(xa[0] + 4096); xf[0][1][0] = ld128(xa[1]); xf[0][1][1] = ld128(xa[1] + 4096);
#pragma unroll
          for (int n = 0; n < 6; ++n) P1_RW(0, wa, n / 3, n % 3); }
        LBAR();
        unsigned xo0 = 0, xo1 = P1_XSLOT, xo2 = 2 * P1_XSLOT;
        unsigned wo0 = 0, wo1 = P1_WSLOT, wo2 = 2 * P1_WSLOT, wo3 = 3 * P1_WSLOT, wo4 = 4 * P1_WSLOT;
#define P1_TILE(HF, T, IE, IO, WA, WB, RDLAST, PFX) do { \
            const unsigned ed_ = P1_EDST(xo2, wo4, wo0), od_ = odst + xo0; const char* og_ = xg + ((T) + 3) * 128; \
            P1_STEP(HF, 0, 1, xo0, 1, wo1, P1_RD0, if (IE) P1_GE2(ebA, ebB, ed_, 0), if (IE) P1_GE2(ebA, ebB, ed_, 1), if (IE) P1_GE2(ebA, ebB, ed_, 2), if (IE) P1_GE2(ebA, ebB, ed_, 3)); \
            WA; \
            PFX; \
            P1_STEP(HF, 1, 0, xo1, 0, wo2, RDLAST, if (IO) P1_GO2(og_, od_, 0), if (IO) P1_GO2(og_, od_, 1), if (IO) P1_GO2(og_, od_, 2), if (IO) P1_GO2(og_, od_, 3)); \
            WB; \
            ebA += estride; ebB += estride; \
            { const unsigned t_ = xo0; xo0 = xo1; xo1 = xo2; xo2 = t_; const unsigned a_ = wo0, b_ = wo1; wo0 = wo2; wo1 = wo3; wo2 = wo4; wo3 = a_; wo4 = b_; } } while (0)
#define P1_WB(N) WAIT_BAR(N)
#define P1_RDL 1
#define P1_RD0 1
#define P1_LOOP(HF) do { \
            for (int T = 0; T < 13; ++T) { \
                  \
                const char* pb = T < 10 ? pfw + (size_t)(T + 6) * (64 * 6144) : (const char*)(bid < 128 ? woA : woB) + (bid & 127) * 16384 + (T & 1) * 8192; \
                const unsigned po = T < 10 ? pfo : lane * 128; \
                P1_TILE(HF, T, 1, 1, P1_WB(8), P1_WB(12), P1_RDL, if (HF == 1) P1_PF(pb, po)); \
            } \
            P1_TILE(HF, 13, 1, 0, P1_WB(8), P1_WB(8), 1, NOP_); \
            P1_TILE(HF, 14, 0, 0, P1_WB(0), NOP_, 1, NOP_); \
            P1_TILE(HF, 15, 0, 0, NOP_, NOP_, 0, NOP_); } while (0)
        if (w < 4) P1_LOOP(0); else P1_LOOP(1);
        asm volatile("s_waitcnt vmcnt(0)" ::: "memory");
#undef P1_LOOP
#undef P1_GATE
#undef P1_TILE
#undef P1_STEP
#undef P1_RW
#undef P1_MM
#undef P1_WF
#undef P1_GO
#undef P1_GE
#undef P1_EDST
#undef P1_PF
    }
    STAMP_AT(3);
#pragma unroll
    for (int o = 32; o >= 1; o >>= 1) gate += __shfl_xor(gate, o);
    gate = 1.0f / (1.0f + __expf(-gate));
    LBAR();
    u32x4 keep[8];
    bf16x8 qown0, qown1;
    const unsigned mB = __builtin_amdgcn_readfirstlane(wn ? ~0u : 0u), mA = ~mB;
    char* kvg = ws + WS_KV + (size_t)((wn * 32 + head) * 8 + rt) * 32768;
    const unsigned kvo = wm * 4096 + l16;
    {
#pragma unroll
        for (int r = 0; r < 2; ++r)
#pragma unroll
            for (int s = 0; s < 2; ++s) {
                const unsigned o = kvo + r * 2048 + s * 1024;
                keep[r * 2 + s] = __builtin_bit_cast(u32x4, pack8(acc[r][1], s, 1.0f)); keep[4 + r * 2 + s] = __builtin_bit_cast(u32x4, pack8(acc[r][2], s, 1.0f));
                st16_wtm(o, kvg, keep[r * 2 + s], mB); st16_wtm(16384 + o, kvg, keep[4 + r * 2 + s], mB);
            }
        const f32x16 qk = wn ? acc[1][0] : acc[0][0], qs = wn ? acc[0][0] : acc[1][0];
        qown0 = pack8(qk, 0, QSCALE); qown1 = pack8(qk, 1, QSCALE);
        st128(lds0 + P2_Q + w * 2048 + l16, __builtin_bit_cast(u32x4, pack8(qs, 0, QSCALE))); st128(lds0 + P2_Q + w * 2048 + 1024 + l16, __builtin_bit_cast(u32x4, pack8(qs, 1, QSCALE)));
        if (wn) {
#pragma unroll
            for (int i = 0; i < 4; ++i) { const unsigned o = kvo + (i >> 1) * 2048 + (i & 1) * 1024; st128(lds0 + P2_B + o, keep[i]); st128(lds0 + P2_B + 16384 + o, keep[4 + i]); }
        }
    }
    LBAR();
    STAMP_AT(4);

    const int rb = rt * 8 + qw;
    const int ql = lane & 31, hh = lane >> 5;
    const char* kvA = ws + WS_KV + (size_t)(head * 8) * 32768;
    const char* kvB = ws + WS_KV + (size_t)((32 + head) * 8) * 32768;
    unsigned* fKB = flags + 64 + head * 8; unsigned* fO = flags + 64 + 256; unsigned* fKA = flags + 64 + 512 + head * 8; unsigned* fOB = flags + 64 + 768;
    const int rtp = rt > 0 ? rt - 1 : 0;
    const unsigned* fKAp = fKA + rtp;
    bf16x8 sel;
    { const short o = (((lane >> 4) & 1) == ((lane >> 2) & 1)) ? (short)0x3F80 : (short)0;
#pragma unroll
      for (int i = 0; i < 8; ++i) sel[i] = o; }
    const bf16x8 qx0 = ld128(lds0 + P2_Q + (w ^ 2) * 2048 + l16), qx1 = ld128(lds0 + P2_Q + (w ^ 2) * 2048 + 1024 + l16);
    const bf16x8 qa0 = wn ? qx0 : qown0, qa1 = wn ? qx1 : qown1, qb0 = wn ? qown0 : qx0, qb1 = wn ? qown1 : qx1;
    char* og = ws + WS_O + ((size_t)rb * 64 + head) * 2048;
    const unsigned oo = (ql >> 4) * 1024 + (hh * 16 + (ql & 15)) * 16;
    const int rt3 = (xcd & 3) * 4 + (idx & 3), ct = (xcd >> 2) * 8 + (idx >> 2);
    const int p3 = w & 3, half = w >> 2;
    const char* wsrcB = (const char*)woB + ct * 128 + (size_t)half * 512 * 2048; const char* wsrcA = (const char*)woA + ct * 128 + (size_t)half * 512 * 2048;
    const unsigned vw0 = (unsigned)((16 * p3 + (lane >> 3)) * 2048 + (((lane & 7) ^ (((lane >> 4) & 3) << 1)) << 4)), vw1 = vw0 + 8 * 2048;
    const unsigned wdst3 = lds0 + half * 8192 + p3 * 2048;
#define P3_SOFF(s) ((s) < 6 ? 65536 + (s) * P3_SLOT : (s) < 10 ? ((s) - 6) * P3_SLOT : 65536 + ((s) - 10) * P3_SLOT)
#define P3_GW(st, i) glds16((i) ? vw1 : vw0, ((st) < 8 ? wsrcB : wsrcA) + (size_t)((st) & 7) * (64 * 2048), wdst3 + P3_SOFF(st) + (i) * 1024)

#define EX(x) __builtin_amdgcn_exp2f(x)
#define P8(x) __builtin_bit_cast(bf16x8, x)
#define STEP(SC, SN, Q0, Q1, AK0, AK1, AV1, AV0, X) do { \
        SN = MFMA32(k0, Q0, negm); k0 = ld128(AK0); \
        phi[2] = cvtpk(t0, t1); phi[3] = cvtpk(t2, t3); t0 = EX(SC[0]); t1 = EX(SC[1]); PIN(phi); PIN(t0); PIN(t1); SBAR(); \
        O = MFMA32(v1, P8(phi), O); v1 = ld128(AV1); \
        t2 = EX(SC[2]); t3 = EX(SC[3]); plo[0] = cvtpk(t0, t1); PIN(t2); PIN(t3); PIN(plo); SBAR(); \
        L = MFMA16(sel, P8(phi), L); \
        plo[1] = cvtpk(t2, t3); t0 = EX(SC[4]); PIN(plo); PIN(t0); SBAR(); \
        SN = MFMA32(k1, Q1, SN); k1 = ld128(AK1); \
        t1 = EX(SC[5]); t2 = EX(SC[6]); t3 = EX(SC[7]); PIN(t1); PIN(t2); PIN(t3); SBAR(); \
        X; SBAR(); \
        plo[2] = cvtpk(t0, t1); t0 = EX(SC[8]); plo[3] = cvtpk(t2, t3); t1 = EX(SC[9]); PIN(plo); PIN(t0); PIN(t1); SBAR(); \
        O = MFMA32(v0, P8(plo), O); v0 = ld128(AV0); \
        t2 = EX(SC[10]); t3 = EX(SC[11]); phi[0] = cvtpk(t0, t1); PIN(t2); PIN(t3); PIN(phi); SBAR(); \
        L = MFMA16(sel, P8(plo), L); \
        phi[1] = cvtpk(t2, t3); \
        t0 = EX(SC[12]); t1 = EX(SC[13]); t2 = EX(SC[14]); t3 = EX(SC[15]); PIN(phi); PIN(t0); PIN(t1); PIN(t2); PIN(t3); SBAR(); \
    } while (0)
#define SG_OFF(i) (((i) >> 1) * 16384 + ((i) & 1) * 1024)
#define SG_LD(i, src, dK, dV) glds16c(l16, (src) + w * 2048 + SG_OFF(i), ((i) >> 1 ? (dV) : (dK)) + w * 2048 + ((i) & 1) * 1024)
    {
        f32x16 O = {}, negm, SA, SB; f32x4 L = {};
        bf16x8 k0, k1, v0, v1; u32x4 plo, phi; float t0 = 0.f, t1 = 0.f, t2 = 0.f, t3 = 0.f;
        unsigned cur = lds0 + P2_B + l16, nxt = cur + P2_SLOT;
        {
            k0 = ld128(cur); k1 = ld128(cur + 1024);
            SA = f32x16{}; SA = MFMA32(k0, qb0, SA); SA = MFMA32(k1, qb1, SA);
            const float m = rowmax16(SA);
#pragma unroll
            for (int r = 0; r < 16; ++r) { negm[r] = -m; SA[r] -= m; }
            k0 = ld128(cur + 2048); k1 = ld128(cur + 2048 + 1024);
            v0 = ld128(cur + 16384);
            v1 = bf16x8{}; phi = u32x4{}; plo = u32x4{};
        }
#define KAD(j, s) (((j) < 8 ? cur + (j) * 2048 : nxt + ((j) - 8) * 2048) + (s) * 1024)
#define BSTEP(SC, SN, j, X) STEP(SC, SN, qb0, qb1, KAD((j) + 2, 0), KAD((j) + 2, 1), KAD(j, 1) + 16384, KAD((j) + 1, 0) + 16384, X)
#define KEEP_A(i) st16_wtm(kvo + ((i) >> 2) * 16384 + (((i) >> 1) & 1) * 2048 + ((i) & 1) * 1024, kvg, keep[i], mA)
        unsigned pv;
        BSTEP(SA, SB, 0, NOP_);
        STAMP_AT(5);
        WAIT_BAR(0);
        STAMP_AT(6);
        if (tid == 0) flag_set(fKB + rt, token);
        BSTEP(SB, SA, 1, POLL_ISSUE(pv, fKB, 8));
        { const char* s1 = kvB + ((rt + 1) & 7) * 32768; const unsigned d = lds0 + P2_B + P2_SLOT;
          BSTEP(SA, SB, 2, POLL_CHECK(pv, fKB, 8); SG_LD(0, s1, d, d + 16384); SG_LD(1, s1, d, d + 16384); SG_LD(2, s1, d, d + 16384); SG_LD(3, s1, d, d + 16384);
                           KEEP_A(0); KEEP_A(1); KEEP_A(2); KEEP_A(3)); }
        STAMP_AT(7);
        BSTEP(SB, SA, 3, KEEP_A(4); KEEP_A(5); KEEP_A(6); KEEP_A(7));
        BSTEP(SA, SB, 4, NOP_);
        STAMP_AT(8);
        BSTEP(SB, SA, 5, NOP_);
        STAMP_AT(9);
        WAIT_BAR(0);
        STAMP_AT(10);
        if (tid == 0) flag_set(fKA + rt, token);
        const char* aown = kvA + rt * 32768;
        int slot = 0;
        for (int i = 0; i < 7; ++i) {
            const int s1 = slot == 2 ? 0 : slot + 1, s2 = s1 == 2 ? 0 : s1 + 1;
            const char* src = i < 6 ? kvB + ((rt + i + 2) & 7) * 32768 : kvA + rtp * 32768;
            const unsigned dK = i < 6 ? lds0 + P2_B + s2 * P2_SLOT : lds0 + P2_AK, dV = i < 6 ? dK + 16384 : lds0 + P2_AV;
            if (i == 6) POLL_CHECK(pv, fKAp, 1);
            BSTEP(SA, SB, 6, SG_LD(0, src, dK, dV)); BSTEP(SB, SA, 7, SG_LD(1, src, dK, dV));
            cur = nxt; nxt = lds0 + P2_B + s2 * P2_SLOT + l16;
            BSTEP(SA, SB, 0, SG_LD(2, src, dK, dV)); BSTEP(SB, SA, 1, SG_LD(3, src, dK, dV));
            BSTEP(SA, SB, 2, if (i == 0) { SG_LD(0, aown, lds0 + P2_AK + 16384, lds0 + P2_AV + 16384); SG_LD(1, aown, lds0 + P2_AK + 16384, lds0 + P2_AV + 16384); });
            BSTEP(SB, SA, 3, if (i == 0) { SG_LD(2, aown, lds0 + P2_AK + 16384, lds0 + P2_AV + 16384); SG_LD(3, aown, lds0 + P2_AK + 16384, lds0 + P2_AV + 16384); });
            BSTEP(SA, SB, 4, if (i == 5) { POLL_ISSUE(pv, fKAp, 1); });
            BSTEP(SB, SA, 5, NOP_);
            WAIT_BAR(0);
            slot = s1;
        }
        BSTEP(SA, SB, 6, NOP_); BSTEP(SB, SA, 7, NOP_);
        phi[2] = cvtpk(t0, t1); phi[3] = cvtpk(t2, t3);
        O = MFMA32(v1, P8(phi), O); L = MFMA16(sel, P8(phi), L);
        float l = L[0];
        if (!unit_ok(O, l)) attn_safe<false>(kvB, qb0, qb1, rb, lane, O, l);
        const float f = 1.0f / l;
        st16_wt(oo, og + 32 * 2048, __builtin_bit_cast(u32x4, pack8(O, 0, f))); st16_wt(oo + 512, og + 32 * 2048, __builtin_bit_cast(u32x4, pack8(O, 1, f)));
#undef KEEP_A
#undef BSTEP
#undef KAD
    }
    STAMP_AT(11);
    LBAR();
    {
        f32x16 O = {}, negm, SA, SB; f32x4 L = {};
        bf16x8 k0, k1, v0, v1; u32x4 plo, phi; float t0 = 0.f, t1 = 0.f, t2 = 0.f, t3 = 0.f;
        const unsigned ab = lds0 + P2_AK + qw * 2048 + l16;
        const int jmax = rt > 0 ? 8 : qw;
#define AAD(j, s) (ab + (8 - ((j) > 8 ? 8 : (j))) * 2048 + (s) * 1024)
        {
            k0 = ld128(AAD(0, 0)); k1 = ld128(AAD(0, 1));
            SA = f32x16{}; SA = MFMA32(k0, qa0, SA); SA = MFMA32(k1, qa1, SA);
#pragma unroll
            for (int r = 0; r < 16; ++r) if (keyof(r, hh) > ql) SA[r] = -INFINITY;
            const float m = rowmax16(SA);
#pragma unroll
            for (int r = 0; r < 16; ++r) { negm[r] = -m; SA[r] -= m; }
            k0 = ld128(AAD(1, 0)); k1 = ld128(AAD(1, 1));
            v0 = ld128(AAD(0, 0) + 32768);
            v1 = bf16x8{}; phi = u32x4{}; plo = u32x4{};
        }
#define ASTEP(SC, SN, j, X) do { if ((j) + 1 > jmax) { _Pragma("unroll") for (int r_ = 0; r_ < 16; ++r_) negm[r_] = -INFINITY; } \
        STEP(SC, SN, qa0, qa1, AAD((j) + 2, 0), AAD((j) + 2, 1), AAD(j, 1) + 32768, AAD((j) + 1, 0) + 32768, X); } while (0)
        ASTEP(SA, SB, 0, P3_GW(0, 0); P3_GW(0, 1)); ASTEP(SB, SA, 1, P3_GW(1, 0); P3_GW(1, 1)); ASTEP(SA, SB, 2, P3_GW(2, 0); P3_GW(2, 1)); ASTEP(SB, SA, 3, P3_GW(3, 0); P3_GW(3, 1));
        ASTEP(SA, SB, 4, P3_GW(4, 0); P3_GW(4, 1));
        WAIT_BAR(10);
        if (tid == 0) flag_set(fOB + rt * 32 + head, token);
        ASTEP(SB, SA, 5, P3_GW(5, 0); P3_GW(5, 1)); ASTEP(SA, SB, 6, NOP_); ASTEP(SB, SA, 7, NOP_);
#pragma unroll
        for (int r = 0; r < 16; ++r) if (keyof(r, hh) <= ql) SA[r] = -INFINITY;
        ASTEP(SA, SB, 8, NOP_);
        phi[2] = cvtpk(t0, t1); phi[3] = cvtpk(t2, t3);
        O = MFMA32(v1, P8(phi), O); L = MFMA16(sel, P8(phi), L);
        float l = L[0];
        if (!unit_ok(O, l)) attn_safe<true>(kvA, qa0, qa1, rb, lane, O, l);
        const float f = 1.0f / l;
        st16_wt(oo, og, __builtin_bit_cast(u32x4, pack8(O, 0, f))); st16_wt(oo + 512, og, __builtin_bit_cast(u32x4, pack8(O, 1, f)));
        if (lane == 0) __hip_atomic_store((GAS float*)(ws + WS_G) + rt * 256 + head * 8 + w, gate, __ATOMIC_RELAXED, __HIP_MEMORY_SCOPE_AGENT);
#undef ASTEP
#undef AAD
    }
#undef STEP
#undef EX
#undef P8
#undef SG_LD
#undef SG_OFF
    STAMP_AT(12);
    WAIT_BAR(0);
    if (tid == 0) flag_set(fO + rt * 32 + head, token);
    STAMP_AT(13);

    {
#pragma unroll
        for (int st = 6; st < 10; ++st) { P3_GW(st, 0); P3_GW(st, 1); }
        poll_spin(fOB + (rt3 >> 1) * 32, 32, token, lane);
        STAMP_AT(14);
        const char* osrc = ws + WS_O + ((size_t)(rt3 * 4 + p3) * 64 + half * 16) * 2048;
        bf16x8 of[7][4];
#define P3_GO(st, i) asm volatile("global_load_dwordx4 %0, %1, %2 offset:" #i "*1024 sc1" : "=v"(of[(st) % 7][i]) : "v"(l16), "s"(osrc + ((st) < 8 ? 65536 : 0) + ((st) & 7) * 4096) : "memory")
#define P3_GO4(st) do { P3_GO(st, 0); P3_GO(st, 1); P3_GO(st, 2); P3_GO(st, 3); } while (0)
        P3_GO4(0); P3_GO4(1); P3_GO4(2);
        float gq[2];
#define P3_GG() do { const float* gp_ = (const float*)(ws + WS_G) + rt3 * 128 + p3 * 32; const unsigned go_ = (lane & 15) * 4; \
          asm volatile("global_load_dword %0, %2, %3 sc1\n\tglobal_load_dword %1, %2, %3 offset:64 sc1" : "=&v"(gq[0]), "=&v"(gq[1]) : "v"(go_), "s"(gp_) : "memory"); } while (0)
        const int g4 = lane >> 4, q4 = (lane & 15) >> 2;
        unsigned wa[4];
        { const unsigned a0 = (16 * (g4 >> 1) + 4 * (g4 & 1) + q4) * 128 + ((2 * (g4 & 1) + (q4 >> 1)) * 32) + (lane & 3) * 8;
#pragma unroll
          for (int n = 0; n < 4; ++n) wa[n] = lds0 + half * 8192 + (a0 ^ (n << 5)) + P3_SOFF(0); }
        f32x4 ac[2][2][4];
#pragma unroll
        for (int i = 0; i < 4; ++i)
#pragma unroll
            for (int j = 0; j < 4; ++j) ac[i >> 1][i & 1][j] = f32x4{};
        s16x4 wl[2][4], wh[2][4];
#define P3_RW(buf, k_, n_) do { wl[buf][n_] = ldtr(wa[n_] + (k_) * 4096); wh[buf][n_] = ldtr(wa[n_] + (k_) * 4096 + 1024); } while (0)
#define P3_MMA(st, k_, n_, wf_) do { ac[(st) >> 3][0][n_] = MFMA16(wf_, of[(st) % 7][2 * (k_)], ac[(st) >> 3][0][n_]); ac[(st) >> 3][1][n_] = MFMA16(wf_, of[(st) % 7][2 * (k_) + 1], ac[(st) >> 3][1][n_]); } while (0)
#define P3_K32(st, k_, X0, X1, X2, X3) do { \
            if ((k_) == 1 && (st) + 1 < 16) { _Pragma("unroll") for (int n_ = 0; n_ < 4; ++n_) { wa[n_] += (unsigned)(P3_SOFF((st) + 1) - P3_SOFF(st)); PIN(wa[n_]); } } \
            _Pragma("unroll") for (int n_ = 0; n_ < 4; ++n_) { \
                const bf16x8 wf_ = cat8(wl[k_][n_], wh[k_][n_]); \
                P3_MMA(st, k_, n_, wf_); \
                if ((k_) == 0) P3_RW(1, 1, n_); else if ((st) + 1 < 16) P3_RW(0, 0, n_); \
                if (n_ == 0) { X0; } if (n_ == 1) { X1; } if (n_ == 2) { X2; } if (n_ == 3) { X3; } \
                SBAR(); } } while (0)
#define P3_OW(st, NW) asm volatile("s_waitcnt vmcnt(" #NW ")" : "+v"(of[(st) % 7][0]), "+v"(of[(st) % 7][1]), "+v"(of[(st) % 7][2]), "+v"(of[(st) % 7][3])::"memory")
#define P3_STAGE(st, NW, BAR, nx) do { \
            P3_OW(st, NW); if (BAR) LBAR(); \
            P3_K32(st, 0, NOP_, NOP_, NOP_, NOP_); \
            if ((nx) < 16) { P3_K32(st, 1, P3_GO(nx, 0), P3_GO(nx, 1), P3_GO(nx, 2), P3_GO(nx, 3)); } \
            else { P3_K32(st, 1, NOP_, NOP_, NOP_, NOP_); } \
        } while (0)
        LBAR();
#pragma unroll
        for (int n_ = 0; n_ < 4; ++n_) P3_RW(0, 0, n_);
        unsigned pv;
        const unsigned* fOAq = fO + (rt3 >> 1) * 32;
        P3_OW(0, 8);
        P3_K32(0, 0, P3_GO(3, 0), P3_GO(3, 1), P3_GO(3, 2), P3_GO(3, 3)); P3_K32(0, 1, P3_GO(4, 0), P3_GO(4, 1), P3_GO(4, 2), P3_GO(4, 3));
        P3_OW(1, 12);
        P3_K32(1, 0, P3_GO(5, 0), P3_GO(5, 1), P3_GO(5, 2), P3_GO(5, 3)); P3_K32(1, 1, P3_GO(6, 0), P3_GO(6, 1), P3_GO(6, 2), P3_GO(6, 3));
        P3_STAGE(2, 16, 0, 7);
        P3_OW(3, 16); LBAR();
        P3_K32(3, 0, POLL_ISSUE(pv, fOAq, 32), P3_GW(10, 0), P3_GW(10, 1), P3_GW(11, 0)); P3_K32(3, 1, P3_GW(11, 1), P3_GW(12, 0), P3_GW(12, 1), NOP_);
        asm volatile("s_waitcnt vmcnt(6)" : "+v"(pv), "+v"(of[4][0]), "+v"(of[4][1]), "+v"(of[4][2]), "+v"(of[4][3])::"memory");
        if (!__all(pv == token)) poll_spin(fOAq, 32, token, lane);
        P3_K32(4, 0, P3_GG(), P3_GO(8, 0); P3_GO(8, 1), P3_GO(8, 2); P3_GO(8, 3), P3_GO(9, 0); P3_GO(9, 1)); P3_K32(4, 1, P3_GO(9, 2); P3_GO(9, 3), P3_GO(10, 0); P3_GO(10, 1), P3_GO(10, 2), P3_GO(10, 3));
        P3_STAGE(5, 29, 0, 11);
        P3_OW(6, 29); LBAR();
        P3_K32(6, 0, P3_GW(13, 0), P3_GW(13, 1), P3_GW(14, 0), P3_GW(14, 1)); P3_K32(6, 1, P3_GW(15, 0); P3_GW(15, 1); P3_GO(12, 0), P3_GO(12, 1), P3_GO(12, 2), P3_GO(12, 3));
        P3_STAGE(7, 35, 0, 13); P3_STAGE(8, 26, 0, 14);
        P3_STAGE(9, 26, 1, 15); P3_STAGE(10, 26, 0, 16); P3_STAGE(11, 22, 0, 16);
        P3_STAGE(12, 12, 1, 16); P3_STAGE(13, 8, 0, 16); P3_STAGE(14, 4, 0, 16); P3_STAGE(15, 0, 0, 16);
        asm volatile("" : "+v"(gq[0]), "+v"(gq[1]));
        f32x4 r2[2][4];
#pragma unroll
        for (int n = 0; n < 4; ++n) { r2[0][n] = ac[1][0][n] * gq[0] + ac[0][0][n] * (1.0f - gq[0]); r2[1][n] = ac[1][1][n] * gq[1] + ac[0][1][n] * (1.0f - gq[1]); }
        LBAR();
        LAS f32x4* mine = (LAS f32x4*)(uintptr_t)(lds0 + w * 4096 + l16);
        LAS f32x4* theirs = (LAS f32x4*)(uintptr_t)(lds0 + (w ^ 4) * 4096 + l16);
#pragma unroll
        for (int q = 0; q < 2; ++q) { mine[(q * 2) * 64] = half ? r2[q][0] : r2[q][2]; mine[(q * 2 + 1) * 64] = half ? r2[q][1] : r2[q][3]; }
        LBAR();
#pragma unroll
        for (int q = 0; q < 2; ++q)
#pragma unroll
            for (int e = 0; e < 2; ++e) {
                const f32x4 r = (half ? r2[q][2 + e] : r2[q][e]) + theirs[(q * 2 + e) * 64];
                *(LAS u32x2*)(uintptr_t)(lds0 + 32768 + (p3 * 32 + q * 16 + (lane & 15)) * 144 + half * 64 + e * 32 + g4 * 8) = u32x2{cvtpk(r[0], r[1]), cvtpk(r[2], r[3])};
            }
        LBAR();
#pragma unroll
        for (int i = 0; i < 2; ++i) {
            const int row = w * 16 + i * 8 + (lane >> 3);
            const u32x4 v = *(const LAS u32x4*)(uintptr_t)(lds0 + 32768 + row * 144 + (lane & 7) * 16);
            char* op = (char*)out + (size_t)(rt3 * 128) * 2048 + ct * 128;
            *(u32x4*)(op + row * 2048 + (lane & 7) * 16) = v;
        }
#undef P3_GO
#undef P3_GO4
#undef P3_MMA
#undef P3_GG
#undef P3_RW
#undef P3_K32
#undef P3_OW
#undef P3_STAGE
    }
#undef P3_GW
#undef P3_SOFF
    if (bid == 0 && w == 0) {
        const unsigned long long t0 = __builtin_amdgcn_s_memrealtime();
        for (;;) {
            bool ok = true;
#pragma unroll
            for (int i = 0; i < 4; ++i) ok = ok && (__hip_atomic_load((GAS unsigned*)(fO + i * 64 + lane), __ATOMIC_RELAXED, __HIP_MEMORY_SCOPE_AGENT) == token);
            if (__all(ok)) break;
            if (__builtin_amdgcn_s_memrealtime() - t0 > 400000ull) break;
            __builtin_amdgcn_s_sleep(1);
        }
        if (lane == 0) flag_set(flags, token);
    }
    asm volatile(".Lgm_code_end:");
    STAMP_AT(15);
    STAMP_FLUSH(ws, 16);
}

extern "C" void kernel_launch(void* const* d_in, const int* in_sizes, int n_in, void* d_out, int out_size, void* d_ws, size_t ws_size, hipStream_t stream) {
    (void)in_sizes; (void)n_in; (void)out_size; (void)ws_size;
    const u16* x = (const u16*)d_in[0]; const u16* wqkvA = (const u16*)d_in[1]; const u16* woA = (const u16*)d_in[2];
    const u16* wqkvB = (const u16*)d_in[4]; const u16* woB = (const u16*)d_in[5]; const u16* gw = (const u16*)d_in[6];
    (void)hipFuncSetAttribute((const void*)k_fused, hipFuncAttributeMaxDynamicSharedMemorySize, LDS_BYTES);
    k_fused<<<256, 512, LDS_BYTES, stream>>>(x, wqkvA, wqkvB, woA, woB, gw, (char*)d_ws, (u16*)d_out);
}
```

```cpp
#define NO_PRIO
#include <hip/hip_runtime.h>
#include <hip/hip_bf16.h>
#include <stdint.h>
#pragma clang diagnostic ignored "-Winline-asm"

typedef unsigned short u16;
typedef short bf16x8 __attribute__((ext_vector_type(8)));
typedef short s16x4 __attribute__((ext_vector_type(4)));
typedef float f32x16 __attribute__((ext_vector_type(16)));
typedef float f32x4 __attribute__((ext_vector_type(4)));
typedef float f32x2 __attribute__((ext_vector_type(2)));
typedef __bf16 bf16x2_t __attribute__((ext_vector_type(2)));
typedef unsigned u32x4 __attribute__((ext_vector_type(4)));
typedef unsigned u32x2 __attribute__((ext_vector_type(2)));
#define LAS __attribute__((address_space(3)))
#define GAS __attribute__((address_space(1)))
#define DI __device__ __forceinline__
#define MFMA32(a, b, c) __builtin_amdgcn_mfma_f32_32x32x16_bf16((a), (b), (c), 0, 0, 0)
#define MFMA16(a, b, c) __builtin_amdgcn_mfma_f32_16x16x32_bf16((a), (b), (c), 0, 0, 0)
#define SBAR() __builtin_amdgcn_sched_barrier(0)
#define PIN(x) asm volatile("" : "+v"(x))
#define WAITCNT_VM_LGKM0(N) __builtin_amdgcn_s_waitcnt(((N) & 15) | (((N) >> 4) << 14) | 0x70)
#define WAIT_BAR(N) do { asm volatile("" ::: "memory"); WAITCNT_VM_LGKM0(N); __builtin_amdgcn_s_barrier(); asm volatile("" ::: "memory"); } while (0)
#define LBAR() WAIT_BAR(63)
#define NOP_ do {} while (0)

constexpr float QSCALE = 0.17677669529663687f * 1.4426950408889634f;
constexpr size_t WS_KV = 0;
constexpr size_t WS_O = 16u << 20;
constexpr size_t WS_G = 24u << 20;
constexpr size_t WS_F = (24u << 20) + 8192;
constexpr int LDS_BYTES = 163840;

DI unsigned cvtpk(float lo, float hi) { f32x2 v = {lo, hi}; bf16x2_t b = __builtin_convertvector(v, bf16x2_t); return __builtin_bit_cast(unsigned, b); }
DI bf16x8 pack8(const f32x16& x, int s, float sc) {
    u32x4 w = {cvtpk(x[8 * s] * sc, x[8 * s + 1] * sc), cvtpk(x[8 * s + 2] * sc, x[8 * s + 3] * sc), cvtpk(x[8 * s + 4] * sc, x[8 * s + 5] * sc), cvtpk(x[8 * s + 6] * sc, x[8 * s + 7] * sc)};
    return __builtin_bit_cast(bf16x8, w);
}
DI u32x4 pack44(const f32x4& a, const f32x4& b, float sc) { return u32x4{cvtpk(a[0] * sc, a[1] * sc), cvtpk(a[2] * sc, a[3] * sc), cvtpk(b[0] * sc, b[1] * sc), cvtpk(b[2] * sc, b[3] * sc)}; }
DI void glds16(unsigned voff, const void* sbase, unsigned lds_dst) {
    asm volatile("s_mov_b32 m0, %2\n\ts_nop 0\n\tglobal_load_lds_dwordx4 %0, %1" ::"v"(voff), "s"(sbase), "s"(lds_dst) : "memory", "m0");
}
DI void glds16c(unsigned voff, const void* sbase, unsigned lds_dst) {
    asm volatile("s_mov_b32 m0, %2\n\ts_nop 0\n\tglobal_load_lds_dwordx4 %0, %1 sc1" ::"v"(voff), "s"(sbase), "s"(lds_dst) : "memory", "m0");
}
DI void glds16m(unsigned voff, const void* sbase, unsigned lds_dst, unsigned mask) {
    asm volatile("s_mov_b32 m0, %2\n\ts_mov_b32 exec_lo, %3\n\ts_mov_b32 exec_hi, %3\n\tglobal_load_lds_dwordx4 %0, %1\n\ts_mov_b64 exec, -1" ::"v"(voff), "s"(sbase), "s"(lds_dst), "s"(mask) : "memory", "m0");
}
DI void st16_wt(unsigned voff, void* sbase, u32x4 v) { asm volatile("global_store_dwordx4 %0, %1, %2 sc1\n\ts_nop 1" ::"v"(voff), "v"(v), "s"(sbase) : "memory"); }
DI void st16_wtm(unsigned voff, void* sbase, u32x4 v, unsigned mask) {
    asm volatile("s_mov_b32 exec_lo, %3\n\ts_mov_b32 exec_hi, %3\n\tglobal_store_dwordx4 %0, %1, %2 sc1\n\ts_mov_b64 exec, -1\n\ts_nop 0" ::"v"(voff), "v"(v), "s"(sbase), "s"(mask) : "memory");
}
typedef short v4i16_t __attribute__((ext_vector_type(4)));
DI s16x4 ldtr(unsigned a) { return __builtin_bit_cast(s16x4, __builtin_amdgcn_ds_read_tr16_b64_v4i16((LAS v4i16_t*)(uintptr_t)a)); }
DI bf16x8 ld128(unsigned a) { return *(const LAS bf16x8*)(uintptr_t)a; }
DI void st128(unsigned a, u32x4 v) { *(LAS u32x4*)(uintptr_t)a = v; }
DI bf16x8 cat8(s16x4 lo, s16x4 hi) { return __builtin_shufflevector(lo, hi, 0, 1, 2, 3, 4, 5, 6, 7); }

#define STAMP_DECL do {} while (0)
#define STAMP_AT(i) do {} while (0)
#define STAMP_FLUSH(wsp, n) do {} while (0)

DI void flag_set(unsigned* f, unsigned v) { __hip_atomic_store((GAS unsigned*)f, v, __ATOMIC_RELAXED, __HIP_MEMORY_SCOPE_AGENT); }
DI void poll_spin(const unsigned* f, int n, unsigned want, int lane) {
    const unsigned long long t0 = __builtin_amdgcn_s_memrealtime();
    for (;;) {
        unsigned v = want;
        if (lane < n) v = __hip_atomic_load((GAS unsigned*)(f + lane), __ATOMIC_RELAXED, __HIP_MEMORY_SCOPE_AGENT);
        if (__all(v == want)) break;
        if (__builtin_amdgcn_s_memrealtime() - t0 > 400000ull) break;
        __builtin_amdgcn_s_sleep(1);
    }
    __builtin_amdgcn_fence(__ATOMIC_ACQUIRE, "wavefront");
}
#define POLL_ISSUE(pv, f, n) do { const unsigned* p_ = (f) + (lane < (n) ? lane : 0); asm volatile("global_load_dword %0, %1, off sc1" : "=v"(pv) : "v"(p_) : "memory"); } while (0)
#define POLL_CHECK(pv, f, n) do { asm volatile("s_waitcnt vmcnt(0)" : "+v"(pv)::"memory"); if (!__all((pv) == token)) poll_spin(f, n, token, lane); } while (0)

DI float xhalf_max(float m) { auto rr = __builtin_amdgcn_permlane32_swap(__float_as_uint(m), __float_as_uint(m), false, false); return fmaxf(__uint_as_float(rr[0]), __uint_as_float(rr[1])); }
DI float xhalf_sum(float m) { auto rr = __builtin_amdgcn_permlane32_swap(__float_as_uint(m), __float_as_uint(m), false, false); return __uint_as_float(rr[0]) + __uint_as_float(rr[1]); }
DI int keyof(int r, int h) { return (r & 3) + 8 * (r >> 2) + 4 * h; }
DI float rowmax16(const f32x16& s) {
    float a = fmaxf(fmaxf(s[0], s[1]), s[2]), b = fmaxf(fmaxf(s[3], s[4]), s[5]);
#pragma unroll
    for (int r = 6; r < 16; r += 2) a = fmaxf(fmaxf(a, s[r]), s[r + 1]);
    return xhalf_max(fmaxf(a, b));
}
DI bf16x8 packp(const f32x16& p, int s) {
    u32x4 w = {cvtpk(p[8 * s], p[8 * s + 1]), cvtpk(p[8 * s + 2], p[8 * s + 3]), cvtpk(p[8 * s + 4], p[8 * s + 5]), cvtpk(p[8 * s + 6], p[8 * s + 7])};
    return __builtin_bit_cast(bf16x8, w);
}
template <bool WIN>
DI void attn_safe(const char* kv, bf16x8 q0, bf16x8 q1, int rb, int lane, f32x16& Oout, float& lout) {
    const int ql = lane & 31, h = lane >> 5;
    __builtin_amdgcn_fence(__ATOMIC_ACQUIRE, "agent");
    asm volatile("s_waitcnt vmcnt(0)" ::: "memory");
    float m = -INFINITY, l = 0.f; f32x16 O = {};
    const int nb = WIN ? (rb < 8 ? rb + 1 : 9) : 64;
    for (int j = 0; j < nb; ++j) {
        const int kb = WIN ? rb - j : j;
        const char* kp = kv + (kb >> 3) * 32768 + (kb & 7) * 2048 + lane * 16;
        const bf16x8 k0 = *(const bf16x8*)kp, k1 = *(const bf16x8*)(kp + 1024), v0 = *(const bf16x8*)(kp + 16384), v1 = *(const bf16x8*)(kp + 16384 + 1024);
        f32x16 s = {}; s = MFMA32(k0, q0, s); s = MFMA32(k1, q1, s);
        if (WIN) {
#pragma unroll
            for (int r = 0; r < 16; ++r) { const int kl = keyof(r, h); if ((j == 0 && kl > ql) || (j == 8 && kl <= ql)) s[r] = -INFINITY; }
        }
        const float mn = fmaxf(m, rowmax16(s));
        const float al = __builtin_amdgcn_exp2f(m - mn); m = mn;
        float ps = 0.f;
#pragma unroll
        for (int r = 0; r < 16; ++r) { s[r] = __builtin_amdgcn_exp2f(s[r] - mn); }
        const bf16x8 p0 = packp(s, 0), p1 = packp(s, 1);
#pragma unroll
        for (int r = 0; r < 8; ++r) { ps += __uint_as_float((unsigned)(u16)p0[r] << 16) + __uint_as_float((unsigned)(u16)p1[r] << 16); }
        l = l * al + ps;
#pragma unroll
        for (int r = 0; r < 16; ++r) O[r] *= al;
        O = MFMA32(v0, p0, O); O = MFMA32(v1, p1, O);
    }
    Oout = O; lout = xhalf_sum(l);
}
DI bool unit_ok(const f32x16& O, float l) {
    bool ok = l <= 1.0995116e12f;
    float a = 0.f;
#pragma unroll
    for (int r = 0; r < 16; ++r) a = fmaxf(a, __builtin_fabsf(O[r]));
    bool okO = a <= 3.0e38f;
#pragma unroll
    for (int r = 0; r < 16; ++r) okO = okO && (O[r] == O[r]);
    return __all(ok && okO);
}

constexpr int P1_XSLOT = 32768, P1_WBASE = 3 * P1_XSLOT, P1_WSLOT = 12288;
constexpr int P2_AK = 0, P2_AV = 32768, P2_B = 65536, P2_SLOT = 32768;
constexpr int P2_Q = P2_B + 2 * P2_SLOT;
constexpr int P3_SLOT = 16384;

__global__ __launch_bounds__(512, 2) void k_fused(const u16* __restrict__ x, const u16* __restrict__ wA, const u16* __restrict__ wB, const u16* __restrict__ woA,
                                                  const u16* __restrict__ woB, const u16* __restrict__ gw, char* __restrict__ ws, u16* __restrict__ out) {
    extern __shared__ __attribute__((aligned(16))) char lds[];
    const unsigned lds0 = (unsigned)(uintptr_t)(LAS char*)lds;
    STAMP_DECL; STAMP_AT(0);
    const int tid = threadIdx.x, lane = tid & 63;
    const int w = __builtin_amdgcn_readfirstlane(tid >> 6);
    const int bid = blockIdx.x, xcd = bid & 7, idx = bid >> 3;
    const int head = xcd * 4 + (idx >> 3), rt = idx & 7;
    const int wm = (w & 1) | ((w >> 2) << 1), wn = (w >> 1) & 1, qw = 2 * wm + wn;
    unsigned* flags = (unsigned*)(ws + WS_F);
    const unsigned l16 = lane * 16;
    float gate;
    unsigned token;

    f32x16 acc[2][3];
    {
        const char* xg = (const char*)x + (size_t)rt * 256 * 2048;
        const bool w0 = w == 0, w7 = w == 7;
        const int whalf = w >= 4;
        unsigned vo[4], ve[4];
        const char* ebA; const char* ebB; unsigned estride, edst;
        {
            const int r8 = lane >> 3, c8 = lane & 7;
            const int sc = (lane & 3) ^ (((lane >> 5) & 1) << 1);
#pragma unroll
            for (int j = 0; j < 4; ++j) {
                const int p = 4 * (w7 ? 0 : w) + j;
                vo[j] = (unsigned)((8 * p + r8) * 2048 + ((c8 ^ ((4 * p + (lane >> 4)) & 7)) << 4));
                const int pe = 28 + j;
                const unsigned vxe = (unsigned)((8 * pe + r8) * 2048 + ((c8 ^ ((4 * pe + (lane >> 4)) & 7)) << 4));
                const int i = 4 * ((w7 || w0 ? 1 : w) - 1) + j, ii = i >= 12 ? i - 12 : i, seg = ii >> 1, rg = ii & 1, type = seg >= 3 ? seg - 3 : seg;
                const unsigned vwe = (unsigned)((whalf * 32 + rg * 16 + (lane >> 2)) * 6144 + type * 2048 + (sc << 4));
                ve[j] = w0 ? vxe : vwe;
            }
            const int i0 = 4 * ((w7 || w0 ? 1 : w) - 1), ii0 = i0 >= 12 ? i0 - 12 : i0;
            ebA = w0 ? xg : (const char*)(ii0 >= 6 ? wB : wA) + head * 64;
            ebB = w0 ? xg : (const char*)(ii0 + 2 >= 6 ? wB : wA) + head * 64;
            estride = w0 ? 128u : 64u * 6144u;
            edst = w0 ? 28 * 1024 : P1_WBASE + ii0 * 1024;
        }
        const unsigned odst = lds0 + (w7 ? 0 : w) * 4096;
        const unsigned mstage = __builtin_amdgcn_readfirstlane(w7 ? 0u : ~0u), mwarm = ~mstage;
        const char* pfw = (const char*)(bid < 128 ? wA : wB);
        unsigned pfo;
        { const int L = (bid & 127) * 24 + (lane < 24 ? lane : 0); pfo = (unsigned)((L / 48) * 6144 + (L % 48) * 128); }
#define P1_PF(base, off) asm volatile("s_mov_b32 m0, %3\n\ts_mov_b32 exec_lo, %2\n\ts_mov_b32 exec_hi, %2\n\tglobal_load_lds_dword %0, %1\n\ts_mov_b64 exec, -1" ::"v"(off), "s"(base), "s"(mwarm), "s"(lds0 + P1_WBASE + 5 * P1_WSLOT) : "memory", "m0")
#define P1_GO(og_, od_, j) glds16m(vo[j], og_, (od_) + (j) * 1024, mstage)
#define P1_GE(eA_, eB_, ed_, j) glds16m(ve[j], (j) < 2 ? (eA_) : (eB_), (ed_) + (j) * 1024, mstage)
#define P1_GO2 P1_GO
#define P1_GE2 P1_GE
#define P1_EDST(xo_, h0, h1) (lds0 + edst + (w0 ? (xo_) : (whalf ? (h1) : (h0))))
#pragma unroll
        for (int j = 0; j < 4; ++j) P1_GO(xg, odst, j);
        { const unsigned ed = P1_EDST(0, 0, P1_WSLOT);
#pragma unroll
          for (int j = 0; j < 4; ++j) P1_GE(ebA, ebB, ed, j); }
        u32x4 gx0, gx1, gw0, gw1;
        {
            const char* px = (const char*)x + (size_t)(rt * 256 + head * 8 + w) * 2048;
            const unsigned go = lane * 32, z = 0;
            asm volatile("global_load_dwordx4 %0, %5, %6\n\tglobal_load_dwordx4 %1, %5, %6 offset:16\n\tglobal_load_dwordx4 %2, %5, %7\n\tglobal_load_dwordx4 %3, %5, %7 offset:16\n\t"
                         "global_load_dword %4, %8, %9 sc1"
                         : "=&v"(gx0), "=&v"(gx1), "=&v"(gw0), "=&v"(gw1), "=&v"(token) : "v"(go), "s"(px), "s"(gw), "v"(z), "s"(flags) : "memory");
        }
#pragma unroll
        for (int j = 0; j < 4; ++j) P1_GO(xg + 128, odst + P1_XSLOT, j);
        { const unsigned ed = P1_EDST(P1_XSLOT, 2 * P1_WSLOT, 3 * P1_WSLOT);
#pragma unroll
          for (int j = 0; j < 4; ++j) P1_GE(ebA + estride, ebB + estride, ed, j); }
        ebA += 2 * (size_t)estride; ebB += 2 * (size_t)estride;
        const int hh1 = lane >> 5;
        unsigned xa[4], wa;
#pragma unroll
        for (int u = 0; u < 4; ++u) xa[u] = lds0 + (wm * 64 + (lane & 31)) * 128 + (((2 * u + hh1) ^ ((lane >> 1) & 7)) << 4);
        wa = lds0 + P1_WBASE + wn * 6144 + (8 * hh1 + ((lane & 15) >> 2)) * 64 + ((((lane >> 4) & 1) ^ hh1) * 32) + (lane & 3) * 8;
#pragma unroll
        for (int i = 0; i < 2; ++i)
#pragma unroll
            for (int j = 0; j < 3; ++j) acc[i][j] = f32x16{};
        bf16x8 xf[2][2][2]; s16x4 wl[2][2][3], wh[2][2][3];
#define P1_WF(b, k, t) cat8(wl[b][k][t], wh[b][k][t])
#define P1_MM(b, k, r, t) acc[r][t] = ((t) < 2) ? MFMA32(P1_WF(b, k, t), xf[b][k][r], acc[r][t]) : MFMA32(xf[b][k][r], P1_WF(b, k, t), acc[r][t])
#define P1_RW(b, wa_, k, t) do { wl[b][k][t] = ldtr((wa_) + (t) * 2048 + (k) * 1024); wh[b][k][t] = ldtr((wa_) + (t) * 2048 + (k) * 1024 + 256); } while (0)
#define P1_STEP(HF, c, n, xo, u, wo, RD, G0, G1, G2, G3) do { \
            const unsigned xa0_ = xa[2 * (u)] + (xo), xa1_ = xa[2 * (u) + 1] + (xo), wa_ = wa + (wo); \
            P1_MM(c, 0, 0, 0); if (RD) { xf[n][0][0] = ld128(xa0_); P1_RW(n, wa_, 0, 0); } if (HF == 0) { G0; } SBAR(); \
            P1_MM(c, 0, 0, 1); if (RD) { P1_RW(n, wa_, 0, 1); } SBAR(); \
            P1_MM(c, 0, 0, 2); if (RD) { P1_RW(n, wa_, 0, 2); } if (HF == 1) { G0; } SBAR(); \
            P1_MM(c, 0, 1, 0); if (RD) { xf[n][0][1] = ld128(xa0_ + 4096); } if (HF == 0) { G1; } SBAR(); \
            P1_MM(c, 0, 1, 1); if (RD) { xf[n][1][0] = ld128(xa1_); } SBAR(); \
            P1_MM(c, 0, 1, 2); if (RD) { P1_RW(n, wa_, 1, 0); } if (HF == 1) { G1; } SBAR(); \
            P1_MM(c, 1, 0, 0); if (RD) { P1_RW(n, wa_, 1, 1); } if (HF == 0) { G2; } SBAR(); \
            P1_MM(c, 1, 0, 1); if (RD) { P1_RW(n, wa_, 1, 2); } SBAR(); \
            P1_MM(c, 1, 0, 2); if (RD) { xf[n][1][1] = ld128(xa1_ + 4096); } if (HF == 1) { G2; } SBAR(); \
            P1_MM(c, 1, 1, 0); if (HF == 0) { G3; } SBAR(); \
            P1_MM(c, 1, 1, 1); if (HF == 1) { G3; } SBAR(); \
            P1_MM(c, 1, 1, 2); SBAR(); } while (0)
#define P1_GATE() do {     \
            asm volatile("" : "+v"(gx0), "+v"(gx1), "+v"(gw0), "+v"(gw1), "+v"(token)); \
            float s_ = 0.f; \
            _Pragma("unroll") for (int i = 0; i < 4; ++i) { \
                s_ += __uint_as_float(gx0[i] << 16) * __uint_as_float(gw0[i] << 16) + __uint_as_float(gx0[i] & 0xffff0000u) * __uint_as_float(gw0[i] & 0xffff0000u); \
                s_ += __uint_as_float(gx1[i] << 16) * __uint_as_float(gw1[i] << 16) + __uint_as_float(gx1[i] & 0xffff0000u) * __uint_as_float(gw1[i] & 0xffff0000u); } \
            gate = s_; PIN(gate); \
            token = __builtin_amdgcn_readfirstlane(token) + 1u; } while (0)
        STAMP_AT(1);
        if (w7) asm volatile("s_waitcnt vmcnt(0)" ::: "memory");
        WAIT_BAR(8);
        STAMP_AT(2);
#pragma unroll
        for (int j = 0; j < 4; ++j) P1_GO(xg + 256, odst + 2 * P1_XSLOT, j);
        P1_GATE();
        if (w7) {
            P1_PF(pfw + 2 * (64 * 6144), pfo); P1_PF(pfw + 3 * (64 * 6144), pfo);
            const char* xl = (const char*)x + (size_t)bid * 16384; const unsigned lo = lane * 128;
            P1_PF(xl, lo); P1_PF(xl + 8192, lo);
            P1_PF(pfw + 4 * (64 * 6144), pfo); P1_PF(pfw + 5 * (64 * 6144), pfo);
        }
        { xf[0][0][0] = ld128(xa[0]); xf[0][0][1] = ld128(xa[0] + 4096); xf[0][1][0] = ld128(xa[1]); xf[0][1][1] = ld128(xa[1] + 4096);
#pragma unroll
          for (int n = 0; n < 6; ++n) P1_RW(0, wa, n / 3, n % 3); }
        LBAR();
        unsigned xo0 = 0, xo1 = P1_XSLOT, xo2 = 2 * P1_XSLOT;
        unsigned wo0 = 0, wo1 = P1_WSLOT, wo2 = 2 * P1_WSLOT, wo3 = 3 * P1_WSLOT, wo4 = 4 * P1_WSLOT;
#define P1_TILE(HF, T, IE, IO, WA, WB, RDLAST, PFX) do { \
            const unsigned ed_ = P1_EDST(xo2, wo4, wo0), od_ = odst + xo0; const char* og_ = xg + ((T) + 3) * 128; \
            P1_STEP(HF, 0, 1, xo0, 1, wo1, P1_RD0, if (IE) P1_GE2(ebA, ebB, ed_, 0), if (IE) P1_GE2(ebA, ebB, ed_, 1), if (IE) P1_GE2(ebA, ebB, ed_, 2), if (IE) P1_GE2(ebA, ebB, ed_, 3)); \
            WA; \
            PFX; \
            P1_STEP(HF, 1, 0, xo1, 0, wo2, RDLAST, if (IO) P1_GO2(og_, od_, 0), if (IO) P1_GO2(og_, od_, 1), if (IO) P1_GO2(og_, od_, 2), if (IO) P1_GO2(og_, od_, 3)); \
            WB; \
            ebA += estride; ebB += estride; \
            { const unsigned t_ = xo0; xo0 = xo1; xo1 = xo2; xo2 = t_; const unsigned a_ = wo0, b_ = wo1; wo0 = wo2; wo1 = wo3; wo2 = wo4; wo3 = a_; wo4 = b_; } } while (0)
#define P1_WB(N) WAIT_BAR(N)
#define P1_RDL 1
#define P1_RD0 1
#define P1_LOOP(HF) do { \
            for (int T = 0; T < 13; ++T) { \
                  \
                const char* pb = T < 10 ? pfw + (size_t)(T + 6) * (64 * 6144) : (const char*)(bid < 128 ? woA : woB) + (bid & 127) * 16384 + (T & 1) * 8192; \
                const unsigned po = T < 10 ? pfo : lane * 128; \
                P1_TILE(HF, T, 1, 1, P1_WB(8), P1_WB(12), P1_RDL, if (HF == 1) P1_PF(pb, po)); \
            } \
            P1_TILE(HF, 13, 1, 0, P1_WB(8), P1_WB(8), 1, NOP_); \
            P1_TILE(HF, 14, 0, 0, P1_WB(0), NOP_, 1, NOP_); \
            P1_TILE(HF, 15, 0, 0, NOP_, NOP_, 0, NOP_); } while (0)
        if (w < 4) P1_LOOP(0); else P1_LOOP(1);
        asm volatile("s_waitcnt vmcnt(0)" ::: "memory");
#undef P1_LOOP
#undef P1_GATE
#undef P1_TILE
#undef P1_STEP
#undef P1_RW
#undef P1_MM
#undef P1_WF
#undef P1_GO
#undef P1_GE
#undef P1_EDST
#undef P1_PF
    }
    STAMP_AT(3);
#pragma unroll
    for (int o = 32; o >= 1; o >>= 1) gate += __shfl_xor(gate, o);
    gate = 1.0f / (1.0f + __expf(-gate));
    LBAR();
    u32x4 keep[8];
    bf16x8 qown0, qown1;
    const unsigned mB = __builtin_amdgcn_readfirstlane(wn ? ~0u : 0u), mA = ~mB;
    char* kvg = ws + WS_KV + (size_t)((wn * 32 + head) * 8 + rt) * 32768;
    const unsigned kvo = wm * 4096 + l16;
    {
#pragma unroll
        for (int r = 0; r < 2; ++r)
#pragma unroll
            for (int s = 0; s < 2; ++s) {
                const unsigned o = kvo + r * 2048 + s * 1024;
                keep[r * 2 + s] = __builtin_bit_cast(u32x4, pack8(acc[r][1], s, 1.0f)); keep[4 + r * 2 + s] = __builtin_bit_cast(u32x4, pack8(acc[r][2], s, 1.0f));
                st16_wtm(o, kvg, keep[r * 2 + s], mB); st16_wtm(16384 + o, kvg, keep[4 + r * 2 + s], mB);
            }
        const f32x16 qk = wn ? acc[1][0] : acc[0][0], qs = wn ? acc[0][0] : acc[1][0];
        qown0 = pack8(qk, 0, QSCALE); qown1 = pack8(qk, 1, QSCALE);
        st128(lds0 + P2_Q + w * 2048 + l16, __builtin_bit_cast(u32x4, pack8(qs, 0, QSCALE))); st128(lds0 + P2_Q + w * 2048 + 1024 + l16, __builtin_bit_cast(u32x4, pack8(qs, 1, QSCALE)));
        if (wn) {
#pragma unroll
            for (int i = 0; i < 4; ++i) { const unsigned o = kvo + (i >> 1) * 2048 + (i & 1) * 1024; st128(lds0 + P2_B + o, keep[i]); st128(lds0 + P2_B + 16384 + o, keep[4 + i]); }
        }
    }
    LBAR();
    STAMP_AT(4);

    const int rb = rt * 8 + qw;
    const int ql = lane & 31, hh = lane >> 5;
    const char* kvA = ws + WS_KV + (size_t)(head * 8) * 32768;
    const char* kvB = ws + WS_KV + (size_t)((32 + head) * 8) * 32768;
    unsigned* fKB = flags + 64 + head * 8; unsigned* fO = flags + 64 + 256; unsigned* fKA = flags + 64 + 512 + head * 8; unsigned* fOB = flags + 64 + 768;
    const int rtp = rt > 0 ? rt - 1 : 0;
    const unsigned* fKAp = fKA + rtp;
    bf16x8 sel;
    { const short o = (((lane >> 4) & 1) == ((lane >> 2) & 1)) ? (short)0x3F80 : (short)0;
#pragma unroll
      for (int i = 0; i < 8; ++i) sel[i] = o; }
    const bf16x8 qx0 = ld128(lds0 + P2_Q + (w ^ 2) * 2048 + l16), qx1 = ld128(lds0 + P2_Q + (w ^ 2) * 2048 + 1024 + l16);
    const bf16x8 qa0 = wn ? qx0 : qown0, qa1 = wn ? qx1 : qown1, qb0 = wn ? qown0 : qx0, qb1 = wn ? qown1 : qx1;
    char* og = ws + WS_O + ((size_t)rb * 64 + head) * 2048;
    const unsigned oo = (ql >> 4) * 1024 + (hh * 16 + (ql & 15)) * 16;
    const int rt3 = (xcd & 3) * 4 + (idx & 3), ct = (xcd >> 2) * 8 + (idx >> 2);
    const int p3 = w & 3, half = w >> 2;
    const char* wsrcB = (const char*)woB + ct * 128 + (size_t)half * 512 * 2048; const char* wsrcA = (const char*)woA + ct * 128 + (size_t)half * 512 * 2048;
    const unsigned vw0 = (unsigned)((16 * p3 + (lane >> 3)) * 2048 + (((lane & 7) ^ (((lane >> 4) & 3) << 1)) << 4)), vw1 = vw0 + 8 * 2048;
    const unsigned wdst3 = lds0 + half * 8192 + p3 * 2048;
#define P3_SOFF(s) ((s) < 6 ? 65536 + (s) * P3_SLOT : (s) < 10 ? ((s) - 6) * P3_SLOT : 65536 + ((s) - 10) * P3_SLOT)
#define P3_GW(st, i) glds16((i) ? vw1 : vw0, ((st) < 8 ? wsrcB : wsrcA) + (size_t)((st) & 7) * (64 * 2048), wdst3 + P3_SOFF(st) + (i) * 1024)

#define EX(x) __builtin_amdgcn_exp2f(x)
#define P8(x) __builtin_bit_cast(bf16x8, x)
#define MF(x) do { x; SBAR(); } while (0)
#define STEP(SC, SN, Q0, Q1, AK0, AK1, AV1, AV0, X) do { \
        MF(SN = MFMA32(k0, Q0, negm)); \
        t0 = EX(SC[0]); phi[2] = cvtpk(u0, u1); t1 = EX(SC[1]); phi[3] = cvtpk(u2, u3); k0 = ld128(AK0); PIN(phi); PIN(t0); PIN(t1); SBAR(); \
        t2 = EX(SC[2]); PIN(t2); SBAR(); \
        MF(O = MFMA32(v1, P8(phi), O)); \
        t3 = EX(SC[3]); u0 = EX(SC[4]); v1 = ld128(AV1); plo[0] = cvtpk(t0, t1); PIN(t3); PIN(u0); PIN(plo); SBAR(); \
        MF(L = MFMA16(sel, P8(phi), L)); \
        u1 = EX(SC[5]); plo[1] = cvtpk(t2, t3); PIN(plo); PIN(u1); SBAR(); \
        MF(SN = MFMA32(k1, Q1, SN)); \
        u2 = EX(SC[6]); u3 = EX(SC[7]); k1 = ld128(AK1); plo[2] = cvtpk(u0, u1); PIN(u2); PIN(u3); PIN(plo); SBAR(); \
        X; SBAR(); \
        t0 = EX(SC[8]); plo[3] = cvtpk(u2, u3); t1 = EX(SC[9]); t2 = EX(SC[10]); PIN(plo); PIN(t0); PIN(t1); PIN(t2); SBAR(); \
        MF(O = MFMA32(v0, P8(plo), O)); \
        t3 = EX(SC[11]); u0 = EX(SC[12]); v0 = ld128(AV0); phi[0] = cvtpk(t0, t1); PIN(t3); PIN(u0); PIN(phi); SBAR(); \
        MF(L = MFMA16(sel, P8(plo), L)); \
        u1 = EX(SC[13]); phi[1] = cvtpk(t2, t3); u2 = EX(SC[14]); u3 = EX(SC[15]); PIN(phi); PIN(u1); PIN(u2); PIN(u3); SBAR(); \
    } while (0)
#define SG_OFF(i) (((i) >> 1) * 16384 + ((i) & 1) * 1024)
#define SG_LD(i, src, dK, dV) glds16c(l16, (src) + w * 2048 + SG_OFF(i), ((i) >> 1 ? (dV) : (dK)) + w * 2048 + ((i) & 1) * 1024)
    {
        f32x16 O = {}, negm, SA, SB; f32x4 L = {};
        bf16x8 k0, k1, v0, v1; u32x4 plo, phi; float t0, t1, t2, t3, u0 = 0.f, u1 = 0.f, u2 = 0.f, u3 = 0.f;
        unsigned cur = lds0 + P2_B + l16, nxt = cur + P2_SLOT;
        {
            k0 = ld128(cur); k1 = ld128(cur + 1024);
            SA = f32x16{}; SA = MFMA32(k0, qb0, SA); SA = MFMA32(k1, qb1, SA);
            const float m = rowmax16(SA);
#pragma unroll
            for (int r = 0; r < 16; ++r) { negm[r] = -m; SA[r] -= m; }
            k0 = ld128(cur + 2048); k1 = ld128(cur + 2048 + 1024);
            v0 = ld128(cur + 16384);
            v1 = bf16x8{}; phi = u32x4{}; plo = u32x4{};
        }
#define KAD(j, s) (((j) < 8 ? cur + (j) * 2048 : nxt + ((j) - 8) * 2048) + (s) * 1024)
#define BSTEP(SC, SN, j, X) STEP(SC, SN, qb0, qb1, KAD((j) + 2, 0), KAD((j) + 2, 1), KAD(j, 1) + 16384, KAD((j) + 1, 0) + 16384, X)
#define KEEP_A(i) st16_wtm(kvo + ((i) >> 2) * 16384 + (((i) >> 1) & 1) * 2048 + ((i) & 1) * 1024, kvg, keep[i], mA)
        unsigned pv;
        BSTEP(SA, SB, 0, NOP_);
        STAMP_AT(5);
        WAIT_BAR(0);
        STAMP_AT(6);
        if (tid == 0) flag_set(fKB + rt, token);
        BSTEP(SB, SA, 1, POLL_ISSUE(pv, fKB, 8));
        { const char* s1 = kvB + ((rt + 1) & 7) * 32768; const unsigned d = lds0 + P2_B + P2_SLOT;
          BSTEP(SA, SB, 2, POLL_CHECK(pv, fKB, 8); SG_LD(0, s1, d, d + 16384); SG_LD(1, s1, d, d + 16384); SG_LD(2, s1, d, d + 16384); SG_LD(3, s1, d, d + 16384);
                           KEEP_A(0); KEEP_A(1); KEEP_A(2); KEEP_A(3)); }
        STAMP_AT(7);
        BSTEP(SB, SA, 3, KEEP_A(4); KEEP_A(5); KEEP_A(6); KEEP_A(7));
        BSTEP(SA, SB, 4, NOP_);
        STAMP_AT(8);
        BSTEP(SB, SA, 5, NOP_);
        STAMP_AT(9);
        WAIT_BAR(0);
        STAMP_AT(10);
        if (tid == 0) flag_set(fKA + rt, token);
        const char* aown = kvA + rt * 32768;
        int slot = 0;
        for (int i = 0; i < 7; ++i) {
            const int s1 = slot == 2 ? 0 : slot + 1, s2 = s1 == 2 ? 0 : s1 + 1;
            const char* src = i < 6 ? kvB + ((rt + i + 2) & 7) * 32768 : kvA + rtp * 32768;
            const unsigned dK = i < 6 ? lds0 + P2_B + s2 * P2_SLOT : lds0 + P2_AK, dV = i < 6 ? dK + 16384 : lds0 + P2_AV;
            if (i == 6) POLL_CHECK(pv, fKAp, 1);
            BSTEP(SA, SB, 6, SG_LD(0, src, dK, dV)); BSTEP(SB, SA, 7, SG_LD(1, src, dK, dV));
            cur = nxt; nxt = lds0 + P2_B + s2 * P2_SLOT + l16;
            BSTEP(SA, SB, 0, SG_LD(2, src, dK, dV)); BSTEP(SB, SA, 1, SG_LD(3, src, dK, dV));
            BSTEP(SA, SB, 2, if (i == 0) { SG_LD(0, aown, lds0 + P2_AK + 16384, lds0 + P2_AV + 16384); SG_LD(1, aown, lds0 + P2_AK + 16384, lds0 + P2_AV + 16384); });
            BSTEP(SB, SA, 3, if (i == 0) { SG_LD(2, aown, lds0 + P2_AK + 16384, lds0 + P2_AV + 16384); SG_LD(3, aown, lds0 + P2_AK + 16384, lds0 + P2_AV + 16384); });
            BSTEP(SA, SB, 4, if (i == 5) { POLL_ISSUE(pv, fKAp, 1); });
            BSTEP(SB, SA, 5, NOP_);
            WAIT_BAR(0);
            slot = s1;
        }
        BSTEP(SA, SB, 6, NOP_); BSTEP(SB, SA, 7, NOP_);
        phi[2] = cvtpk(u0, u1); phi[3] = cvtpk(u2, u3);
        O = MFMA32(v1, P8(phi), O); L = MFMA16(sel, P8(phi), L);
        float l = L[0];
        if (!unit_ok(O, l)) attn_safe<false>(kvB, qb0, qb1, rb, lane, O, l);
        const float f = 1.0f / l;
        st16_wt(oo, og + 32 * 2048, __builtin_bit_cast(u32x4, pack8(O, 0, f))); st16_wt(oo + 512, og + 32 * 2048, __builtin_bit_cast(u32x4, pack8(O, 1, f)));
#undef KEEP_A
#undef BSTEP
#undef KAD
    }
    STAMP_AT(11);
    LBAR();
    {
        f32x16 O = {}, negm, SA, SB; f32x4 L = {};
        bf16x8 k0, k1, v0, v1; u32x4 plo, phi; float t0, t1, t2, t3, u0 = 0.f, u1 = 0.f, u2 = 0.f, u3 = 0.f;
        const unsigned ab = lds0 + P2_AK + qw * 2048 + l16;
        const int jmax = rt > 0 ? 8 : qw;
#define AAD(j, s) (ab + (8 - ((j) > 8 ? 8 : (j))) * 2048 + (s) * 1024)
        {
            k0 = ld128(AAD(0, 0)); k1 = ld128(AAD(0, 1));
            SA = f32x16{}; SA = MFMA32(k0, qa0, SA); SA = MFMA32(k1, qa1, SA);
#pragma unroll
            for (int r = 0; r < 16; ++r) if (keyof(r, hh) > ql) SA[r] = -INFINITY;
            const float m = rowmax16(SA);
#pragma unroll
            for (int r = 0; r < 16; ++r) { negm[r] = -m; SA[r] -= m; }
            k0 = ld128(AAD(1, 0)); k1 = ld128(AAD(1, 1));
            v0 = ld128(AAD(0, 0) + 32768);
            v1 = bf16x8{}; phi = u32x4{}; plo = u32x4{};
        }
#define ASTEP(SC, SN, j, X) do { if ((j) + 1 > jmax) { _Pragma("unroll") for (int r_ = 0; r_ < 16; ++r_) negm[r_] = -INFINITY; } \
        STEP(SC, SN, qa0, qa1, AAD((j) + 2, 0), AAD((j) + 2, 1), AAD(j, 1) + 32768, AAD((j) + 1, 0) + 32768, X); } while (0)
        ASTEP(SA, SB, 0, P3_GW(0, 0); P3_GW(0, 1)); ASTEP(SB, SA, 1, P3_GW(1, 0); P3_GW(1, 1)); ASTEP(SA, SB, 2, P3_GW(2, 0); P3_GW(2, 1)); ASTEP(SB, SA, 3, P3_GW(3, 0); P3_GW(3, 1));
        ASTEP(SA, SB, 4, P3_GW(4, 0); P3_GW(4, 1));
        WAIT_BAR(10);
        if (tid == 0) flag_set(fOB + rt * 32 + head, token);
        ASTEP(SB, SA, 5, P3_GW(5, 0); P3_GW(5, 1)); ASTEP(SA, SB, 6, NOP_); ASTEP(SB, SA, 7, NOP_);
#pragma unroll
        for (int r = 0; r < 16; ++r) if (keyof(r, hh) <= ql) SA[r] = -INFINITY;
        ASTEP(SA, SB, 8, NOP_);
        phi[2] = cvtpk(u0, u1); phi[3] = cvtpk(u2, u3);
        O = MFMA32(v1, P8(phi), O); L = MFMA16(sel, P8(phi), L);
        float l = L[0];
        if (!unit_ok(O, l)) attn_safe<true>(kvA, qa0, qa1, rb, lane, O, l);
        const float f = 1.0f / l;
        st16_wt(oo, og, __builtin_bit_cast(u32x4, pack8(O, 0, f))); st16_wt(oo + 512, og, __builtin_bit_cast(u32x4, pack8(O, 1, f)));
        if (lane == 0) __hip_atomic_store((GAS float*)(ws + WS_G) + rt * 256 + head * 8 + w, gate, __ATOMIC_RELAXED, __HIP_MEMORY_SCOPE_AGENT);
#undef ASTEP
#undef AAD
    }
#undef STEP
#undef MF
#undef EX
#undef P8
#undef SG_LD
#undef SG_OFF
    STAMP_AT(12);
    WAIT_BAR(0);
    if (tid == 0) flag_set(fO + rt * 32 + head, token);
    STAMP_AT(13);

    {
#pragma unroll
        for (int st = 6; st < 10; ++st) { P3_GW(st, 0); P3_GW(st, 1); }
        poll_spin(fOB + (rt3 >> 1) * 32, 32, token, lane);
        STAMP_AT(14);
        const char* osrc = ws + WS_O + ((size_t)(rt3 * 4 + p3) * 64 + half * 16) * 2048;
        bf16x8 of[7][4];
#define P3_GO(st, i) asm volatile("global_load_dwordx4 %0, %1, %2 offset:" #i "*1024 sc1" : "=v"(of[(st) % 7][i]) : "v"(l16), "s"(osrc + ((st) < 8 ? 65536 : 0) + ((st) & 7) * 4096) : "memory")
#define P3_GO4(st) do { P3_GO(st, 0); P3_GO(st, 1); P3_GO(st, 2); P3_GO(st, 3); } while (0)
        P3_GO4(0); P3_GO4(1); P3_GO4(2);
        float gq[2];
#define P3_GG() do { const float* gp_ = (const float*)(ws + WS_G) + rt3 * 128 + p3 * 32; const unsigned go_ = (lane & 15) * 4; \
          asm volatile("global_load_dword %0, %2, %3 sc1\n\tglobal_load_dword %1, %2, %3 offset:64 sc1" : "=&v"(gq[0]), "=&v"(gq[1]) : "v"(go_), "s"(gp_) : "memory"); } while (0)
        const int g4 = lane >> 4, q4 = (lane & 15) >> 2;
        unsigned wa[4];
        { const unsigned a0 = (16 * (g4 >> 1) + 4 * (g4 & 1) + q4) * 128 + ((2 * (g4 & 1) + (q4 >> 1)) * 32) + (lane & 3) * 8;
#pragma unroll
          for (int n = 0; n < 4; ++n) wa[n] = lds0 + half * 8192 + (a0 ^ (n << 5)) + P3_SOFF(0); }
        f32x4 ac[2][2][4];
#pragma unroll
        for (int i = 0; i < 4; ++i)
#pragma unroll
            for (int j = 0; j < 4; ++j) ac[i >> 1][i & 1][j] = f32x4{};
        s16x4 wl[2][4], wh[2][4];
#define P3_RW(buf, k_, n_) do { wl[buf][n_] = ldtr(wa[n_] + (k_) * 4096); wh[buf][n_] = ldtr(wa[n_] + (k_) * 4096 + 1024); } while (0)
#define P3_MMA(st, k_, n_, wf_) do { ac[(st) >> 3][0][n_] = MFMA16(wf_, of[(st) % 7][2 * (k_)], ac[(st) >> 3][0][n_]); ac[(st) >> 3][1][n_] = MFMA16(wf_, of[(st) % 7][2 * (k_) + 1], ac[(st) >> 3][1][n_]); } while (0)
#define P3_K32(st, k_, X0, X1, X2, X3) do { \
            if ((k_) == 1 && (st) + 1 < 16) { _Pragma("unroll") for (int n_ = 0; n_ < 4; ++n_) { wa[n_] += (unsigned)(P3_SOFF((st) + 1) - P3_SOFF(st)); PIN(wa[n_]); } } \
            _Pragma("unroll") for (int n_ = 0; n_ < 4; ++n_) { \
                const bf16x8 wf_ = cat8(wl[k_][n_], wh[k_][n_]); \
                P3_MMA(st, k_, n_, wf_); \
                if ((k_) == 0) P3_RW(1, 1, n_); else if ((st) + 1 < 16) P3_RW(0, 0, n_); \
                if (n_ == 0) { X0; } if (n_ == 1) { X1; } if (n_ == 2) { X2; } if (n_ == 3) { X3; } \
                SBAR(); } } while (0)
#define P3_OW(st, NW) asm volatile("s_waitcnt vmcnt(" #NW ")" : "+v"(of[(st) % 7][0]), "+v"(of[(st) % 7][1]), "+v"(of[(st) % 7][2]), "+v"(of[(st) % 7][3])::"memory")
#define P3_STAGE(st, NW, BAR, nx) do { \
            P3_OW(st, NW); if (BAR) LBAR(); \
            P3_K32(st, 0, NOP_, NOP_, NOP_, NOP_); \
            if ((nx) < 16) { P3_K32(st, 1, P3_GO(nx, 0), P3_GO(nx, 1), P3_GO(nx, 2), P3_GO(nx, 3)); } \
            else { P3_K32(st, 1, NOP_, NOP_, NOP_, NOP_); } \
        } while (0)
        LBAR();
#pragma unroll
        for (int n_ = 0; n_ < 4; ++n_) P3_RW(0, 0, n_);
        unsigned pv;
        const unsigned* fOAq = fO + (rt3 >> 1) * 32;
        P3_OW(0, 8);
        P3_K32(0, 0, P3_GO(3, 0), P3_GO(3, 1), P3_GO(3, 2), P3_GO(3, 3)); P3_K32(0, 1, P3_GO(4, 0), P3_GO(4, 1), P3_GO(4, 2), P3_GO(4, 3));
        P3_OW(1, 12);
        P3_K32(1, 0, P3_GO(5, 0), P3_GO(5, 1), P3_GO(5, 2), P3_GO(5, 3)); P3_K32(1, 1, P3_GO(6, 0), P3_GO(6, 1), P3_GO(6, 2), P3_GO(6, 3));
        P3_STAGE(2, 16, 0, 7);
        P3_OW(3, 16); LBAR();
        P3_K32(3, 0, POLL_ISSUE(pv, fOAq, 32), P3_GW(10, 0), P3_GW(10, 1), P3_GW(11, 0)); P3_K32(3, 1, P3_GW(11, 1), P3_GW(12, 0), P3_GW(12, 1), NOP_);
        asm volatile("s_waitcnt vmcnt(6)" : "+v"(pv), "+v"(of[4][0]), "+v"(of[4][1]), "+v"(of[4][2]), "+v"(of[4][3])::"memory");
        if (!__all(pv == token)) poll_spin(fOAq, 32, token, lane);
        P3_K32(4, 0, P3_GG(), P3_GO(8, 0); P3_GO(8, 1), P3_GO(8, 2); P3_GO(8, 3), P3_GO(9, 0); P3_GO(9, 1)); P3_K32(4, 1, P3_GO(9, 2); P3_GO(9, 3), P3_GO(10, 0); P3_GO(10, 1), P3_GO(10, 2), P3_GO(10, 3));
        P3_STAGE(5, 29, 0, 11);
        P3_OW(6, 29); LBAR();
        P3_K32(6, 0, P3_GW(13, 0), P3_GW(13, 1), P3_GW(14, 0), P3_GW(14, 1)); P3_K32(6, 1, P3_GW(15, 0); P3_GW(15, 1); P3_GO(12, 0), P3_GO(12, 1), P3_GO(12, 2), P3_GO(12, 3));
        P3_STAGE(7, 35, 0, 13); P3_STAGE(8, 26, 0, 14);
        P3_STAGE(9, 26, 1, 15); P3_STAGE(10, 26, 0, 16); P3_STAGE(11, 22, 0, 16);
        P3_STAGE(12, 12, 1, 16); P3_STAGE(13, 8, 0, 16); P3_STAGE(14, 4, 0, 16); P3_STAGE(15, 0, 0, 16);
        asm volatile("" : "+v"(gq[0]), "+v"(gq[1]));
        f32x4 r2[2][4];
#pragma unroll
        for (int n = 0; n < 4; ++n) { r2[0][n] = ac[1][0][n] * gq[0] + ac[0][0][n] * (1.0f - gq[0]); r2[1][n] = ac[1][1][n] * gq[1] + ac[0][1][n] * (1.0f - gq[1]); }
        LBAR();
        LAS f32x4* mine = (LAS f32x4*)(uintptr_t)(lds0 + w * 4096 + l16);
        LAS f32x4* theirs = (LAS f32x4*)(uintptr_t)(lds0 + (w ^ 4) * 4096 + l16);
#pragma unroll
        for (int q = 0; q < 2; ++q) { mine[(q * 2) * 64] = half ? r2[q][0] : r2[q][2]; mine[(q * 2 + 1) * 64] = half ? r2[q][1] : r2[q][3]; }
        LBAR();
#pragma unroll
        for (int q = 0; q < 2; ++q)
#pragma unroll
            for (int e = 0; e < 2; ++e) {
                const f32x4 r = (half ? r2[q][2 + e] : r2[q][e]) + theirs[(q * 2 + e) * 64];
                *(LAS u32x2*)(uintptr_t)(lds0 + 32768 + (p3 * 32 + q * 16 + (lane & 15)) * 144 + half * 64 + e * 32 + g4 * 8) = u32x2{cvtpk(r[0], r[1]), cvtpk(r[2], r[3])};
            }
        LBAR();
#pragma unroll
        for (int i = 0; i < 2; ++i) {
            const int row = w * 16 + i * 8 + (lane >> 3);
            const u32x4 v = *(const LAS u32x4*)(uintptr_t)(lds0 + 32768 + row * 144 + (lane & 7) * 16);
            char* op = (char*)out + (size_t)(rt3 * 128) * 2048 + ct * 128;
            *(u32x4*)(op + row * 2048 + (lane & 7) * 16) = v;
        }
#undef P3_GO
#undef P3_GO4
#undef P3_MMA
#undef P3_GG
#undef P3_RW
#undef P3_K32
#undef P3_OW
#undef P3_STAGE
    }
#undef P3_GW
#undef P3_SOFF
    if (bid == 0 && w == 0) {
        const unsigned long long t0 = __builtin_amdgcn_s_memrealtime();
        for (;;) {
            bool ok = true;
#pragma unroll
            for (int i = 0; i < 4; ++i) ok = ok && (__hip_atomic_load((GAS unsigned*)(fO + i * 64 + lane), __ATOMIC_RELAXED, __HIP_MEMORY_SCOPE_AGENT) == token);
            if (__all(ok)) break;
            if (__builtin_amdgcn_s_memrealtime() - t0 > 400000ull) break;
            __builtin_amdgcn_s_sleep(1);
        }
        if (lane == 0) flag_set(flags, token);
    }
    STAMP_AT(15);
    STAMP_FLUSH(ws, 16);
}

extern "C" void kernel_launch(void* const* d_in, const int* in_sizes, int n_in, void* d_out, int out_size, void* d_ws, size_t ws_size, hipStream_t stream) {
    (void)in_sizes; (void)n_in; (void)out_size; (void)ws_size;
    const u16* x = (const u16*)d_in[0]; const u16* wqkvA = (const u16*)d_in[1]; const u16* woA = (const u16*)d_in[2];
    const u16* wqkvB = (const u16*)d_in[4]; const u16* woB = (const u16*)d_in[5]; const u16* gw = (const u16*)d_in[6];
    (void)hipFuncSetAttribute((const void*)k_fused, hipFuncAttributeMaxDynamicSharedMemorySize, LDS_BYTES);
    k_fused<<<256, 512, LDS_BYTES, stream>>>(x, wqkvA, wqkvB, woA, woB, gw, (char*)d_ws, (u16*)d_out);
}
```

```cpp
#include <hip/hip_runtime.h>
#include <hip/hip_bf16.h>
#include <stdint.h>
#pragma clang diagnostic ignored "-Winline-asm"

typedef unsigned short u16;
typedef short bf16x8 __attribute__((ext_vector_type(8)));
typedef short s16x4 __attribute__((ext_vector_type(4)));
typedef float f32x16 __attribute__((ext_vector_type(16)));
typedef float f32x4 __attribute__((ext_vector_type(4)));
typedef float f32x2 __attribute__((ext_vector_type(2)));
typedef __bf16 bf16x2_t __attribute__((ext_vector_type(2)));
typedef unsigned u32x4 __attribute__((ext_vector_type(4)));
typedef unsigned u32x2 __attribute__((ext_vector_type(2)));
#define LAS __attribute__((address_space(3)))
#define GAS __attribute__((address_space(1)))
#define DI __device__ __forceinline__
#define MFMA32(a, b, c) __builtin_amdgcn_mfma_f32_32x32x16_bf16((a), (b), (c), 0, 0, 0)
#define MFMA16(a, b, c) __builtin_amdgcn_mfma_f32_16x16x32_bf16((a), (b), (c), 0, 0, 0)
#define SBAR() __builtin_amdgcn_sched_barrier(0)
#define PIN(x) asm volatile("" : "+v"(x))
#define WAITCNT_VM_LGKM0(N) __builtin_amdgcn_s_waitcnt(((N) & 15) | (((N) >> 4) << 14) | 0x70)
#define WAIT_BAR(N) do { asm volatile("" ::: "memory"); WAITCNT_VM_LGKM0(N); __builtin_amdgcn_s_barrier(); asm volatile("" ::: "memory"); } while (0)
#define LBAR() WAIT_BAR(63)
#define NOP_ do {} while (0)

constexpr float QSCALE = 0.17677669529663687f * 1.4426950408889634f;
constexpr size_t WS_KV = 0;
constexpr size_t WS_O = 16u << 20;
constexpr size_t WS_G = 24u << 20;
constexpr size_t WS_F = (24u << 20) + 8192;
constexpr int LDS_BYTES = 163840;

DI unsigned cvtpk(float lo, float hi) { f32x2 v = {lo, hi}; bf16x2_t b = __builtin_convertvector(v, bf16x2_t); return __builtin_bit_cast(unsigned, b); }
DI bf16x8 pack8(const f32x16& x, int s, float sc) {
    u32x4 w = {cvtpk(x[8 * s] * sc, x[8 * s + 1] * sc), cvtpk(x[8 * s + 2] * sc, x[8 * s + 3] * sc), cvtpk(x[8 * s + 4] * sc, x[8 * s + 5] * sc), cvtpk(x[8 * s + 6] * sc, x[8 * s + 7] * sc)};
    return __builtin_bit_cast(bf16x8, w);
}
DI u32x4 pack44(const f32x4& a, const f32x4& b, float sc) { return u32x4{cvtpk(a[0] * sc, a[1] * sc), cvtpk(a[2] * sc, a[3] * sc), cvtpk(b[0] * sc, b[1] * sc), cvtpk(b[2] * sc, b[3] * sc)}; }
DI void glds16(unsigned voff, const void* sbase, unsigned lds_dst) {
    asm volatile("s_mov_b32 m0, %2\n\ts_nop 0\n\tglobal_load_lds_dwordx4 %0, %1" ::"v"(voff), "s"(sbase), "s"(lds_dst) : "memory", "m0");
}
DI void glds16c(unsigned voff, const void* sbase, unsigned lds_dst) {
    asm volatile("s_mov_b32 m0, %2\n\ts_nop 0\n\tglobal_load_lds_dwordx4 %0, %1 sc1" ::"v"(voff), "s"(sbase), "s"(lds_dst) : "memory", "m0");
}
DI void glds16m(unsigned voff, const void* sbase, unsigned lds_dst, unsigned mask) {
    asm volatile("s_mov_b32 m0, %2\n\ts_mov_b32 exec_lo, %3\n\ts_mov_b32 exec_hi, %3\n\tglobal_load_lds_dwordx4 %0, %1\n\ts_mov_b64 exec, -1" ::"v"(voff), "s"(sbase), "s"(lds_dst), "s"(mask) : "memory", "m0");
}
DI void st16_wt(unsigned voff, void* sbase, u32x4 v) { asm volatile("global_store_dwordx4 %0, %1, %2 sc1\n\ts_nop 1" ::"v"(voff), "v"(v), "s"(sbase) : "memory"); }
DI void st16_wtm(unsigned voff, void* sbase, u32x4 v, unsigned mask) {
    asm volatile("s_mov_b32 exec_lo, %3\n\ts_mov_b32 exec_hi, %3\n\tglobal_store_dwordx4 %0, %1, %2 sc1\n\ts_mov_b64 exec, -1\n\ts_nop 0" ::"v"(voff), "v"(v), "s"(sbase), "s"(mask) : "memory");
}
typedef short v4i16_t __attribute__((ext_vector_type(4)));
DI s16x4 ldtr(unsigned a) { return __builtin_bit_cast(s16x4, __builtin_amdgcn_ds_read_tr16_b64_v4i16((LAS v4i16_t*)(uintptr_t)a)); }
DI bf16x8 ld128(unsigned a) { return *(const LAS bf16x8*)(uintptr_t)a; }
DI void st128(unsigned a, u32x4 v) { *(LAS u32x4*)(uintptr_t)a = v; }
DI bf16x8 cat8(s16x4 lo, s16x4 hi) { return __builtin_shufflevector(lo, hi, 0, 1, 2, 3, 4, 5, 6, 7); }

#define STAMP_DECL do {} while (0)
#define STAMP_AT(i) do {} while (0)
#define STAMP_FLUSH(wsp, n) do {} while (0)

DI void flag_set(unsigned* f, unsigned v) { __hip_atomic_store((GAS unsigned*)f, v, __ATOMIC_RELAXED, __HIP_MEMORY_SCOPE_AGENT); }
DI void poll_spin(const unsigned* f, int n, unsigned want, int lane) {
    const unsigned long long t0 = __builtin_amdgcn_s_memrealtime();
    for (;;) {
        unsigned v = want;
        if (lane < n) v = __hip_atomic_load((GAS unsigned*)(f + lane), __ATOMIC_RELAXED, __HIP_MEMORY_SCOPE_AGENT);
        if (__all(v == want)) break;
        if (__builtin_amdgcn_s_memrealtime() - t0 > 400000ull) break;
        __builtin_amdgcn_s_sleep(1);
    }
    __builtin_amdgcn_fence(__ATOMIC_ACQUIRE, "wavefront");
}
#define POLL_ISSUE(pv, f, n) do { const unsigned* p_ = (f) + (lane < (n) ? lane : 0); asm volatile("global_load_dword %0, %1, off sc1" : "=v"(pv) : "v"(p_) : "memory"); } while (0)
#define POLL_CHECK(pv, f, n) do { asm volatile("s_waitcnt vmcnt(0)" : "+v"(pv)::"memory"); if (!__all((pv) == token)) poll_spin(f, n, token, lane); } while (0)

DI float xhalf_max(float m) { auto rr = __builtin_amdgcn_permlane32_swap(__float_as_uint(m), __float_as_uint(m), false, false); return fmaxf(__uint_as_float(rr[0]), __uint_as_float(rr[1])); }
DI float xhalf_sum(float m) { auto rr = __builtin_amdgcn_permlane32_swap(__float_as_uint(m), __float_as_uint(m), false, false); return __uint_as_float(rr[0]) + __uint_as_float(rr[1]); }
DI int keyof(int r, int h) { return (r & 3) + 8 * (r >> 2) + 4 * h; }
DI float rowmax16(const f32x16& s) {
    float a = fmaxf(fmaxf(s[0], s[1]), s[2]), b = fmaxf(fmaxf(s[3], s[4]), s[5]);
#pragma unroll
    for (int r = 6; r < 16; r += 2) a = fmaxf(fmaxf(a, s[r]), s[r + 1]);
    return xhalf_max(fmaxf(a, b));
}
DI bf16x8 packp(const f32x16& p, int s) {
    u32x4 w = {cvtpk(p[8 * s], p[8 * s + 1]), cvtpk(p[8 * s + 2], p[8 * s + 3]), cvtpk(p[8 * s + 4], p[8 * s + 5]), cvtpk(p[8 * s + 6], p[8 * s + 7])};
    return __builtin_bit_cast(bf16x8, w);
}
template <bool WIN>
DI void attn_safe(const char* kv, bf16x8 q0, bf16x8 q1, int rb, int lane, f32x16& Oout, float& lout) {
    const int ql = lane & 31, h = lane >> 5;
    __builtin_amdgcn_fence(__ATOMIC_ACQUIRE, "agent");
    asm volatile("s_waitcnt vmcnt(0)" ::: "memory");
    float m = -INFINITY, l = 0.f; f32x16 O = {};
    const int nb = WIN ? (rb < 8 ? rb + 1 : 9) : 64;
    for (int j = 0; j < nb; ++j) {
        const int kb = WIN ? rb - j : j;
        const char* kp = kv + (kb >> 3) * 32768 + (kb & 7) * 2048 + lane * 16;
        const bf16x8 k0 = *(const bf16x8*)kp, k1 = *(const bf16x8*)(kp + 1024), v0 = *(const bf16x8*)(kp + 16384), v1 = *(const bf16x8*)(kp + 16384 + 1024);
        f32x16 s = {}; s = MFMA32(k0, q0, s); s = MFMA32(k1, q1, s);
        if (WIN) {
#pragma unroll
            for (int r = 0; r < 16; ++r) { const int kl = keyof(r, h); if ((j == 0 && kl > ql) || (j == 8 && kl <= ql)) s[r] = -INFINITY; }
        }
        const float mn = fmaxf(m, rowmax16(s));
        const float al = __builtin_amdgcn_exp2f(m - mn); m = mn;
        float ps = 0.f;
#pragma unroll
        for (int r = 0; r < 16; ++r) { s[r] = __builtin_amdgcn_exp2f(s[r] - mn); }
        const bf16x8 p0 = packp(s, 0), p1 = packp(s, 1);
#pragma unroll
        for (int r = 0; r < 8; ++r) { ps += __uint_as_float((unsigned)(u16)p0[r] << 16) + __uint_as_float((unsigned)(u16)p1[r] << 16); }
        l = l * al + ps;
#pragma unroll
        for (int r = 0; r < 16; ++r) O[r] *= al;
        O = MFMA32(v0, p0, O); O = MFMA32(v1, p1, O);
    }
    Oout = O; lout = xhalf_sum(l);
}
DI bool unit_ok(const f32x16& O, float l) {
    bool ok = l <= 1.0995116e12f;
    float a = 0.f;
#pragma unroll
    for (int r = 0; r < 16; ++r) a = fmaxf(a, __builtin_fabsf(O[r]));
    bool okO = a <= 3.0e38f;
#pragma unroll
    for (int r = 0; r < 16; ++r) okO = okO && (O[r] == O[r]);
    return __all(ok && okO);
}

constexpr int P1_XSLOT = 32768, P1_WBASE = 3 * P1_XSLOT, P1_WSLOT = 12288;
constexpr int P2_AK = 0, P2_AV = 32768, P2_B = 65536, P2_SLOT = 32768;
constexpr int P2_Q = P2_B + 2 * P2_SLOT;
constexpr int P3_SLOT = 16384;

__global__ __launch_bounds__(512, 2) void k_fused(const u16* __restrict__ x, const u16* __restrict__ wA, const u16* __restrict__ wB, const u16* __restrict__ woA,
                                                  const u16* __restrict__ woB, const u16* __restrict__ gw, char* __restrict__ ws, u16* __restrict__ out) {
    extern __shared__ __attribute__((aligned(16))) char lds[];
    const unsigned lds0 = (unsigned)(uintptr_t)(LAS char*)lds;
    STAMP_DECL; STAMP_AT(0);
    const int tid = threadIdx.x, lane = tid & 63;
    const int w = __builtin_amdgcn_readfirstlane(tid >> 6);
    const int bid = blockIdx.x, xcd = bid & 7, idx = bid >> 3;
    const int head = xcd * 4 + (idx >> 3), rt = idx & 7;
    const int wm = (w & 1) | ((w >> 2) << 1), wn = (w >> 1) & 1, qw = 2 * wm + wn;
    unsigned* flags = (unsigned*)(ws + WS_F);
    const unsigned l16 = lane * 16;
    float gate;
    unsigned token;

    f32x16 acc[2][3];
    {
        const char* xg = (const char*)x + (size_t)rt * 256 * 2048;
        const bool w0 = w == 0, w7 = w == 7;
        const int whalf = w >= 4;
        unsigned vo[4], ve[4];
        const char* ebA; const char* ebB; unsigned estride, edst;
        {
            const int r8 = lane >> 3, c8 = lane & 7;
            const int sc = (lane & 3) ^ (((lane >> 5) & 1) << 1);
#pragma unroll
            for (int j = 0; j < 4; ++j) {
                const int p = 4 * (w7 ? 0 : w) + j;
                vo[j] = (unsigned)((8 * p + r8) * 2048 + ((c8 ^ ((4 * p + (lane >> 4)) & 7)) << 4));
                const int pe = 28 + j;
                const unsigned vxe = (unsigned)((8 * pe + r8) * 2048 + ((c8 ^ ((4 * pe + (lane >> 4)) & 7)) << 4));
                const int i = 4 * ((w7 || w0 ? 1 : w) - 1) + j, ii = i >= 12 ? i - 12 : i, seg = ii >> 1, rg = ii & 1, type = seg >= 3 ? seg - 3 : seg;
                const unsigned vwe = (unsigned)((whalf * 32 + rg * 16 + (lane >> 2)) * 6144 + type * 2048 + (sc << 4));
                ve[j] = w0 ? vxe : vwe;
            }
            const int i0 = 4 * ((w7 || w0 ? 1 : w) - 1), ii0 = i0 >= 12 ? i0 - 12 : i0;
            ebA = w0 ? xg : (const char*)(ii0 >= 6 ? wB : wA) + head * 64;
            ebB = w0 ? xg : (const char*)(ii0 + 2 >= 6 ? wB : wA) + head * 64;
            estride = w0 ? 128u : 64u * 6144u;
            edst = w0 ? 28 * 1024 : P1_WBASE + ii0 * 1024;
        }
        const unsigned odst = lds0 + (w7 ? 0 : w) * 4096;
        const unsigned mstage = __builtin_amdgcn_readfirstlane(w7 ? 0u : ~0u), mwarm = ~mstage;
        const char* pfw = (const char*)(bid < 128 ? wA : wB);
        unsigned pfo;
        { const int L = (bid & 127) * 24 + (lane < 24 ? lane : 0); pfo = (unsigned)((L / 48) * 6144 + (L % 48) * 128); }
#define P1_PF(base, off) asm volatile("s_mov_b32 m0, %3\n\ts_mov_b32 exec_lo, %2\n\ts_mov_b32 exec_hi, %2\n\tglobal_load_lds_dword %0, %1\n\ts_mov_b64 exec, -1" ::"v"(off), "s"(base), "s"(mwarm), "s"(lds0 + P1_WBASE + 5 * P1_WSLOT) : "memory", "m0")
#define P1_GO(og_, od_, j) glds16m(vo[j], og_, (od_) + (j) * 1024, mstage)
#define P1_GE(eA_, eB_, ed_, j) glds16m(ve[j], (j) < 2 ? (eA_) : (eB_), (ed_) + (j) * 1024, mstage)
#define P1_GO2(og_, od_, j) do { if (HF_ == 0) glds16(vo[j], og_, (od_) + (j) * 1024); else P1_GO(og_, od_, j); } while (0)
#define P1_GE2(eA_, eB_, ed_, j) do { if (HF_ == 0) glds16(ve[j], (j) < 2 ? (eA_) : (eB_), (ed_) + (j) * 1024); else P1_GE(eA_, eB_, ed_, j); } while (0)
#define P1_EDST(xo_, h0, h1) (lds0 + edst + (w0 ? (xo_) : (whalf ? (h1) : (h0))))
#pragma unroll
        for (int j = 0; j < 4; ++j) P1_GO(xg, odst, j);
        { const unsigned ed = P1_EDST(0, 0, P1_WSLOT);
#pragma unroll
          for (int j = 0; j < 4; ++j) P1_GE(ebA, ebB, ed, j); }
        u32x4 gx0, gx1, gw0, gw1;
        {
            const char* px = (const char*)x + (size_t)(rt * 256 + head * 8 + w) * 2048;
            const unsigned go = lane * 32, z = 0;
            asm volatile("global_load_dwordx4 %0, %5, %6\n\tglobal_load_dwordx4 %1, %5, %6 offset:16\n\tglobal_load_dwordx4 %2, %5, %7\n\tglobal_load_dwordx4 %3, %5, %7 offset:16\n\t"
                         "global_load_dword %4, %8, %9 sc1"
                         : "=&v"(gx0), "=&v"(gx1), "=&v"(gw0), "=&v"(gw1), "=&v"(token) : "v"(go), "s"(px), "s"(gw), "v"(z), "s"(flags) : "memory");
        }
#pragma unroll
        for (int j = 0; j < 4; ++j) P1_GO(xg + 128, odst + P1_XSLOT, j);
        { const unsigned ed = P1_EDST(P1_XSLOT, 2 * P1_WSLOT, 3 * P1_WSLOT);
#pragma unroll
          for (int j = 0; j < 4; ++j) P1_GE(ebA + estride, ebB + estride, ed, j); }
        ebA += 2 * (size_t)estride; ebB += 2 * (size_t)estride;
        const int hh1 = lane >> 5;
        unsigned xa[4], wa;
#pragma unroll
        for (int u = 0; u < 4; ++u) xa[u] = lds0 + (wm * 64 + (lane & 31)) * 128 + (((2 * u + hh1) ^ ((lane >> 1) & 7)) << 4);
        wa = lds0 + P1_WBASE + wn * 6144 + (8 * hh1 + ((lane & 15) >> 2)) * 64 + ((((lane >> 4) & 1) ^ hh1) * 32) + (lane & 3) * 8;
#pragma unroll
        for (int i = 0; i < 2; ++i)
#pragma unroll
            for (int j = 0; j < 3; ++j) acc[i][j] = f32x16{};
        bf16x8 xf[2][2][2]; s16x4 wl[2][2][3], wh[2][2][3];
#define P1_WF(b, k, t) cat8(wl[b][k][t], wh[b][k][t])
#define P1_MM(b, k, r, t) acc[r][t] = ((t) < 2) ? MFMA32(P1_WF(b, k, t), xf[b][k][r], acc[r][t]) : MFMA32(xf[b][k][r], P1_WF(b, k, t), acc[r][t])
#define P1_RW(b, wa_, k, t) do { wl[b][k][t] = ldtr((wa_) + (t) * 2048 + (k) * 1024); wh[b][k][t] = ldtr((wa_) + (t) * 2048 + (k) * 1024 + 256); } while (0)
#define P1_STEP(HF, c, n, xo, u, wo, RD, G0, G1, G2, G3) do { \
            const unsigned xa0_ = xa[2 * (u)] + (xo), xa1_ = xa[2 * (u) + 1] + (xo), wa_ = wa + (wo); \
            P1_MM(c, 0, 0, 0); if (RD) { xf[n][0][0] = ld128(xa0_); P1_RW(n, wa_, 0, 0); } if (HF == 0) { G0; } SBAR(); \
            P1_MM(c, 0, 0, 1); if (RD) { P1_RW(n, wa_, 0, 1); } SBAR(); \
            P1_MM(c, 0, 0, 2); if (RD) { P1_RW(n, wa_, 0, 2); } if (HF == 1) { G0; } SBAR(); \
            P1_MM(c, 0, 1, 0); if (RD) { xf[n][0][1] = ld128(xa0_ + 4096); } if (HF == 0) { G1; } SBAR(); \
            P1_MM(c, 0, 1, 1); if (RD) { xf[n][1][0] = ld128(xa1_); } SBAR(); \
            P1_MM(c, 0, 1, 2); if (RD) { P1_RW(n, wa_, 1, 0); } if (HF == 1) { G1; } SBAR(); \
            P1_MM(c, 1, 0, 0); if (RD) { P1_RW(n, wa_, 1, 1); } if (HF == 0) { G2; } SBAR(); \
            P1_MM(c, 1, 0, 1); if (RD) { P1_RW(n, wa_, 1, 2); } SBAR(); \
            P1_MM(c, 1, 0, 2); if (RD) { xf[n][1][1] = ld128(xa1_ + 4096); } if (HF == 1) { G2; } SBAR(); \
            P1_MM(c, 1, 1, 0); if (HF == 0) { G3; } SBAR(); \
            P1_MM(c, 1, 1, 1); if (HF == 1) { G3; } SBAR(); \
            P1_MM(c, 1, 1, 2); SBAR(); } while (0)
#define P1_GATE() do {     \
            asm volatile("" : "+v"(gx0), "+v"(gx1), "+v"(gw0), "+v"(gw1), "+v"(token)); \
            float s_ = 0.f; \
            _Pragma("unroll") for (int i = 0; i < 4; ++i) { \
                s_ += __uint_as_float(gx0[i] << 16) * __uint_as_float(gw0[i] << 16) + __uint_as_float(gx0[i] & 0xffff0000u) * __uint_as_float(gw0[i] & 0xffff0000u); \
                s_ += __uint_as_float(gx1[i] << 16) * __uint_as_float(gw1[i] << 16) + __uint_as_float(gx1[i] & 0xffff0000u) * __uint_as_float(gw1[i] & 0xffff0000u); } \
            gate = s_; PIN(gate); \
            token = __builtin_amdgcn_readfirstlane(token) + 1u; } while (0)
        STAMP_AT(1);
        if (w7) {
            P1_PF(pfw + 2 * (64 * 6144), pfo); P1_PF(pfw + 3 * (64 * 6144), pfo);
            const char* xl = (const char*)x + (size_t)bid * 16384; const unsigned lo = lane * 128;
            P1_PF(xl, lo); P1_PF(xl + 8192, lo);
            P1_PF(pfw + 4 * (64 * 6144), pfo); P1_PF(pfw + 5 * (64 * 6144), pfo);
        }
        if (w7) asm volatile("s_waitcnt vmcnt(6)" ::: "memory");
        if (w7) { LBAR(); } else
        WAIT_BAR(8);
        STAMP_AT(2);
#pragma unroll
        for (int j = 0; j < 4; ++j) P1_GO(xg + 256, odst + 2 * P1_XSLOT, j);
        P1_GATE();
        { xf[0][0][0] = ld128(xa[0]); xf[0][0][1] = ld128(xa[0] + 4096); xf[0][1][0] = ld128(xa[1]); xf[0][1][1] = ld128(xa[1] + 4096);
#pragma unroll
          for (int n = 0; n < 6; ++n) P1_RW(0, wa, n / 3, n % 3); }
        LBAR();
        unsigned xo0 = 0, xo1 = P1_XSLOT, xo2 = 2 * P1_XSLOT;
        unsigned wo0 = 0, wo1 = P1_WSLOT, wo2 = 2 * P1_WSLOT, wo3 = 3 * P1_WSLOT, wo4 = 4 * P1_WSLOT;
#define P1_TILE(HF, T, IE, IO, WA, WB, RDLAST, PFX) do { constexpr int HF_ = HF; \
            const unsigned ed_ = P1_EDST(xo2, wo4, wo0), od_ = odst + xo0; const char* og_ = xg + ((T) + 3) * 128; \
            P1_STEP(HF, 0, 1, xo0, 1, wo1, P1_RD0, if (IE) P1_GE2(ebA, ebB, ed_, 0), if (IE) P1_GE2(ebA, ebB, ed_, 1), if (IE) P1_GE2(ebA, ebB, ed_, 2), if (IE) P1_GE2(ebA, ebB, ed_, 3)); \
            WA; \
            PFX; \
            P1_STEP(HF, 1, 0, xo1, 0, wo2, RDLAST, if (IO) P1_GO2(og_, od_, 0), if (IO) P1_GO2(og_, od_, 1), if (IO) P1_GO2(og_, od_, 2), if (IO) P1_GO2(og_, od_, 3)); \
            WB; \
            ebA += estride; ebB += estride; \
            { const unsigned t_ = xo0; xo0 = xo1; xo1 = xo2; xo2 = t_; const unsigned a_ = wo0, b_ = wo1; wo0 = wo2; wo1 = wo3; wo2 = wo4; wo3 = a_; wo4 = b_; } } while (0)
#define P1_WB(N) WAIT_BAR(N)
#define P1_RDL 1
#define P1_RD0 1
#define P1_LOOP(HF) do { \
            for (int T = 0; T < 13; ++T) { \
                  \
                const char* pb = T < 10 ? pfw + (size_t)(T + 6) * (64 * 6144) : (const char*)(bid < 128 ? woA : woB) + (bid & 127) * 16384 + (T & 1) * 8192; \
                const unsigned po = T < 10 ? pfo : lane * 128; \
                P1_TILE(HF, T, 1, 1, P1_WB(8), P1_WB(12), P1_RDL, if (HF == 1) P1_PF(pb, po)); \
            } \
            P1_TILE(HF, 13, 1, 0, P1_WB(8), P1_WB(8), 1, NOP_); \
            P1_TILE(HF, 14, 0, 0, P1_WB(0), NOP_, 1, NOP_); \
            P1_TILE(HF, 15, 0, 0, NOP_, NOP_, 0, NOP_); } while (0)
        if (w < 4) P1_LOOP(0); else P1_LOOP(1);
        asm volatile("s_waitcnt vmcnt(0)" ::: "memory");
#undef P1_LOOP
#undef P1_GATE
#undef P1_TILE
#undef P1_STEP
#undef P1_RW
#undef P1_MM
#undef P1_WF
#undef P1_GO
#undef P1_GE
#undef P1_EDST
#undef P1_PF
    }
    STAMP_AT(3);
#pragma unroll
    for (int o = 32; o >= 1; o >>= 1) gate += __shfl_xor(gate, o);
    gate = 1.0f / (1.0f + __expf(-gate));
    u32x4 keep[8];
    bf16x8 qown0, qown1;
    const unsigned mB = __builtin_amdgcn_readfirstlane(wn ? ~0u : 0u), mA = ~mB;
    char* kvg = ws + WS_KV + (size_t)((wn * 32 + head) * 8 + rt) * 32768;
    const unsigned kvo = wm * 4096 + l16;
    {
#pragma unroll
        for (int r = 0; r < 2; ++r)
#pragma unroll
            for (int s = 0; s < 2; ++s) {
                const unsigned o = kvo + r * 2048 + s * 1024;
                keep[r * 2 + s] = __builtin_bit_cast(u32x4, pack8(acc[r][1], s, 1.0f)); keep[4 + r * 2 + s] = __builtin_bit_cast(u32x4, pack8(acc[r][2], s, 1.0f));
                st16_wtm(o, kvg, keep[r * 2 + s], mB); st16_wtm(16384 + o, kvg, keep[4 + r * 2 + s], mB);
            }
        const f32x16 qk = wn ? acc[1][0] : acc[0][0], qs = wn ? acc[0][0] : acc[1][0];
        qown0 = pack8(qk, 0, QSCALE); qown1 = pack8(qk, 1, QSCALE);
        st128(lds0 + P2_Q + w * 2048 + l16, __builtin_bit_cast(u32x4, pack8(qs, 0, QSCALE))); st128(lds0 + P2_Q + w * 2048 + 1024 + l16, __builtin_bit_cast(u32x4, pack8(qs, 1, QSCALE)));
        if (wn) {
#pragma unroll
            for (int i = 0; i < 4; ++i) { const unsigned o = kvo + (i >> 1) * 2048 + (i & 1) * 1024; st128(lds0 + P2_B + o, keep[i]); st128(lds0 + P2_B + 16384 + o, keep[4 + i]); }
        }
    }
    WAIT_BAR(0);
    STAMP_AT(4);

    const int rb = rt * 8 + qw;
    const int ql = lane & 31, hh = lane >> 5;
    const char* kvA = ws + WS_KV + (size_t)(head * 8) * 32768;
    const char* kvB = ws + WS_KV + (size_t)((32 + head) * 8) * 32768;
    unsigned* fKB = flags + 64 + head * 8; unsigned* fO = flags + 64 + 256; unsigned* fKA = flags + 64 + 512 + head * 8; unsigned* fOB = flags + 64 + 768;
    const int rtp = rt > 0 ? rt - 1 : 0;
    const unsigned* fKAp = fKA + rtp;
    if (tid == 0) flag_set(fKB + rt, token);
    bf16x8 sel;
    { const short o = (((lane >> 4) & 1) == ((lane >> 2) & 1)) ? (short)0x3F80 : (short)0;
#pragma unroll
      for (int i = 0; i < 8; ++i) sel[i] = o; }
    const bf16x8 qx0 = ld128(lds0 + P2_Q + (w ^ 2) * 2048 + l16), qx1 = ld128(lds0 + P2_Q + (w ^ 2) * 2048 + 1024 + l16);
    const bf16x8 qa0 = wn ? qx0 : qown0, qa1 = wn ? qx1 : qown1, qb0 = wn ? qown0 : qx0, qb1 = wn ? qown1 : qx1;
    char* og = ws + WS_O + ((size_t)rb * 64 + head) * 2048;
    const unsigned oo = (ql >> 4) * 1024 + (hh * 16 + (ql & 15)) * 16;
    const int rt3 = (xcd & 3) * 4 + (idx & 3), ct = (xcd >> 2) * 8 + (idx >> 2);
    const int p3 = w & 3, half = w >> 2;
    const char* wsrcB = (const char*)woB + ct * 128 + (size_t)half * 512 * 2048; const char* wsrcA = (const char*)woA + ct * 128 + (size_t)half * 512 * 2048;
    const unsigned vw0 = (unsigned)((16 * p3 + (lane >> 3)) * 2048 + (((lane & 7) ^ (((lane >> 4) & 3) << 1)) << 4)), vw1 = vw0 + 8 * 2048;
    const unsigned wdst3 = lds0 + half * 8192 + p3 * 2048;
#define P3_SOFF(s) ((s) < 6 ? 65536 + (s) * P3_SLOT : (s) < 10 ? ((s) - 6) * P3_SLOT : 65536 + ((s) - 10) * P3_SLOT)
#define P3_GW(st, i) glds16((i) ? vw1 : vw0, ((st) < 8 ? wsrcB : wsrcA) + (size_t)((st) & 7) * (64 * 2048), wdst3 + P3_SOFF(st) + (i) * 1024)

#define EX(x) __builtin_amdgcn_exp2f(x)
#define P8(x) __builtin_bit_cast(bf16x8, x)
#define MF(x) do { x; SBAR(); } while (0)
#define STEP(SC, SN, Q0, Q1, AK0, AK1, AV1, AV0, X) do { \
        MF(SN = MFMA32(k0, Q0, negm)); \
        t0 = EX(SC[0]); phi[2] = cvtpk(u0, u1); t1 = EX(SC[1]); phi[3] = cvtpk(u2, u3); k0 = ld128(AK0); PIN(phi); PIN(t0); PIN(t1); SBAR(); \
        t2 = EX(SC[2]); PIN(t2); SBAR(); \
        MF(O = MFMA32(v1, P8(phi), O)); \
        t3 = EX(SC[3]); u0 = EX(SC[4]); v1 = ld128(AV1); plo[0] = cvtpk(t0, t1); PIN(t3); PIN(u0); PIN(plo); SBAR(); \
        MF(L = MFMA16(sel, P8(phi), L)); \
        u1 = EX(SC[5]); plo[1] = cvtpk(t2, t3); PIN(plo); PIN(u1); SBAR(); \
        MF(SN = MFMA32(k1, Q1, SN)); \
        u2 = EX(SC[6]); u3 = EX(SC[7]); k1 = ld128(AK1); plo[2] = cvtpk(u0, u1); PIN(u2); PIN(u3); PIN(plo); SBAR(); \
        X; SBAR(); \
        t0 = EX(SC[8]); plo[3] = cvtpk(u2, u3); t1 = EX(SC[9]); t2 = EX(SC[10]); PIN(plo); PIN(t0); PIN(t1); PIN(t2); SBAR(); \
        MF(O = MFMA32(v0, P8(plo), O)); \
        t3 = EX(SC[11]); u0 = EX(SC[12]); v0 = ld128(AV0); phi[0] = cvtpk(t0, t1); PIN(t3); PIN(u0); PIN(phi); SBAR(); \
        MF(L = MFMA16(sel, P8(plo), L)); \
        u1 = EX(SC[13]); phi[1] = cvtpk(t2, t3); u2 = EX(SC[14]); u3 = EX(SC[15]); PIN(phi); PIN(u1); PIN(u2); PIN(u3); SBAR(); \
    } while (0)
#define SG_OFF(i) (((i) >> 1) * 16384 + ((i) & 1) * 1024)
#define SG_LD(i, src, dK, dV) glds16c(l16, (src) + w * 2048 + SG_OFF(i), ((i) >> 1 ? (dV) : (dK)) + w * 2048 + ((i) & 1) * 1024)
    {
        f32x16 O = {}, negm, SA, SB; f32x4 L = {};
        bf16x8 k0, k1, v0, v1; u32x4 plo, phi; float t0, t1, t2, t3, u0 = 0.f, u1 = 0.f, u2 = 0.f, u3 = 0.f;
        unsigned cur = lds0 + P2_B + l16, nxt = cur + P2_SLOT;
        {
            k0 = ld128(cur); k1 = ld128(cur + 1024);
            SA = f32x16{}; SA = MFMA32(k0, qb0, SA); SA = MFMA32(k1, qb1, SA);
            const float m = rowmax16(SA);
#pragma unroll
            for (int r = 0; r < 16; ++r) { negm[r] = -m; SA[r] -= m; }
            k0 = ld128(cur + 2048); k1 = ld128(cur + 2048 + 1024);
            v0 = ld128(cur + 16384);
            v1 = bf16x8{}; phi = u32x4{}; plo = u32x4{};
        }
#define KAD(j, s) (((j) < 8 ? cur + (j) * 2048 : nxt + ((j) - 8) * 2048) + (s) * 1024)
#define BSTEP(SC, SN, j, X) STEP(SC, SN, qb0, qb1, KAD((j) + 2, 0), KAD((j) + 2, 1), KAD(j, 1) + 16384, KAD((j) + 1, 0) + 16384, X)
#define KEEP_A(i) st16_wtm(kvo + ((i) >> 2) * 16384 + (((i) >> 1) & 1) * 2048 + ((i) & 1) * 1024, kvg, keep[i], mA)
        unsigned pv;
        BSTEP(SA, SB, 0, POLL_ISSUE(pv, fKB, 8));
        STAMP_AT(5);
        { const char* s1 = kvB + ((rt + 1) & 7) * 32768; const unsigned d = lds0 + P2_B + P2_SLOT;
          BSTEP(SB, SA, 1, POLL_CHECK(pv, fKB, 8); SG_LD(0, s1, d, d + 16384); SG_LD(1, s1, d, d + 16384); SG_LD(2, s1, d, d + 16384); SG_LD(3, s1, d, d + 16384);
                           KEEP_A(0); KEEP_A(1); KEEP_A(2); KEEP_A(3)); }
        STAMP_AT(6);
        BSTEP(SA, SB, 2, KEEP_A(4); KEEP_A(5); KEEP_A(6); KEEP_A(7));
        STAMP_AT(7);
        BSTEP(SB, SA, 3, NOP_);
        BSTEP(SA, SB, 4, NOP_);
        STAMP_AT(8);
        BSTEP(SB, SA, 5, NOP_);
        STAMP_AT(9);
        WAIT_BAR(0);
        STAMP_AT(10);
        if (tid == 0) flag_set(fKA + rt, token);
        const char* aown = kvA + rt * 32768;
        int slot = 0;
        for (int i = 0; i < 7; ++i) {
            const int s1 = slot == 2 ? 0 : slot + 1, s2 = s1 == 2 ? 0 : s1 + 1;
            const char* src = i < 6 ? kvB + ((rt + i + 2) & 7) * 32768 : kvA + rtp * 32768;
            const unsigned dK = i < 6 ? lds0 + P2_B + s2 * P2_SLOT : lds0 + P2_AK, dV = i < 6 ? dK + 16384 : lds0 + P2_AV;
            if (i == 6) POLL_CHECK(pv, fKAp, 1);
            BSTEP(SA, SB, 6, SG_LD(0, src, dK, dV)); BSTEP(SB, SA, 7, SG_LD(1, src, dK, dV));
            cur = nxt; nxt = lds0 + P2_B + s2 * P2_SLOT + l16;
            BSTEP(SA, SB, 0, SG_LD(2, src, dK, dV)); BSTEP(SB, SA, 1, SG_LD(3, src, dK, dV));
            BSTEP(SA, SB, 2, if (i == 0) { SG_LD(0, aown, lds0 + P2_AK + 16384, lds0 + P2_AV + 16384); SG_LD(1, aown, lds0 + P2_AK + 16384, lds0 + P2_AV + 16384); });
            BSTEP(SB, SA, 3, if (i == 0) { SG_LD(2, aown, lds0 + P2_AK + 16384, lds0 + P2_AV + 16384); SG_LD(3, aown, lds0 + P2_AK + 16384, lds0 + P2_AV + 16384); });
            BSTEP(SA, SB, 4, if (i == 5) { POLL_ISSUE(pv, fKAp, 1); });
            BSTEP(SB, SA, 5, NOP_);
            WAIT_BAR(0);
            slot = s1;
        }
        BSTEP(SA, SB, 6, NOP_); BSTEP(SB, SA, 7, NOP_);
        phi[2] = cvtpk(u0, u1); phi[3] = cvtpk(u2, u3);
        O = MFMA32(v1, P8(phi), O); L = MFMA16(sel, P8(phi), L);
        float l = L[0];
        if (!unit_ok(O, l)) attn_safe<false>(kvB, qb0, qb1, rb, lane, O, l);
        const float f = 1.0f / l;
        st16_wt(oo, og + 32 * 2048, __builtin_bit_cast(u32x4, pack8(O, 0, f))); st16_wt(oo + 512, og + 32 * 2048, __builtin_bit_cast(u32x4, pack8(O, 1, f)));
#undef KEEP_A
#undef BSTEP
#undef KAD
    }
    STAMP_AT(11);
    LBAR();
    {
        f32x16 O = {}, negm, SA, SB; f32x4 L = {};
        bf16x8 k0, k1, v0, v1; u32x4 plo, phi; float t0, t1, t2, t3, u0 = 0.f, u1 = 0.f, u2 = 0.f, u3 = 0.f;
        const unsigned ab = lds0 + P2_AK + qw * 2048 + l16;
        const int jmax = rt > 0 ? 8 : qw;
#define AAD(j, s) (ab + (8 - ((j) > 8 ? 8 : (j))) * 2048 + (s) * 1024)
        {
            k0 = ld128(AAD(0, 0)); k1 = ld128(AAD(0, 1));
            SA = f32x16{}; SA = MFMA32(k0, qa0, SA); SA = MFMA32(k1, qa1, SA);
#pragma unroll
            for (int r = 0; r < 16; ++r) if (keyof(r, hh) > ql) SA[r] = -INFINITY;
            const float m = rowmax16(SA);
#pragma unroll
            for (int r = 0; r < 16; ++r) { negm[r] = -m; SA[r] -= m; }
            k0 = ld128(AAD(1, 0)); k1 = ld128(AAD(1, 1));
            v0 = ld128(AAD(0, 0) + 32768);
            v1 = bf16x8{}; phi = u32x4{}; plo = u32x4{};
        }
#define ASTEP(SC, SN, j, X) do { if ((j) + 1 > jmax) { _Pragma("unroll") for (int r_ = 0; r_ < 16; ++r_) negm[r_] = -INFINITY; } \
        STEP(SC, SN, qa0, qa1, AAD((j) + 2, 0), AAD((j) + 2, 1), AAD(j, 1) + 32768, AAD((j) + 1, 0) + 32768, X); } while (0)
        ASTEP(SA, SB, 0, P3_GW(0, 0); P3_GW(0, 1)); ASTEP(SB, SA, 1, P3_GW(1, 0); P3_GW(1, 1)); ASTEP(SA, SB, 2, P3_GW(2, 0); P3_GW(2, 1)); ASTEP(SB, SA, 3, P3_GW(3, 0); P3_GW(3, 1));
        ASTEP(SA, SB, 4, P3_GW(4, 0); P3_GW(4, 1));
        WAIT_BAR(10);
        if (tid == 0) flag_set(fOB + rt * 32 + head, token);
        ASTEP(SB, SA, 5, P3_GW(5, 0); P3_GW(5, 1)); ASTEP(SA, SB, 6, NOP_); ASTEP(SB, SA, 7, NOP_);
#pragma unroll
        for (int r = 0; r < 16; ++r) if (keyof(r, hh) <= ql) SA[r] = -INFINITY;
        ASTEP(SA, SB, 8, NOP_);
        phi[2] = cvtpk(u0, u1); phi[3] = cvtpk(u2, u3);
        O = MFMA32(v1, P8(phi), O); L = MFMA16(sel, P8(phi), L);
        float l = L[0];
        if (!unit_ok(O, l)) attn_safe<true>(kvA, qa0, qa1, rb, lane, O, l);
        const float f = 1.0f / l;
        st16_wt(oo, og, __builtin_bit_cast(u32x4, pack8(O, 0, f))); st16_wt(oo + 512, og, __builtin_bit_cast(u32x4, pack8(O, 1, f)));
        if (lane == 0) __hip_atomic_store((GAS float*)(ws + WS_G) + rt * 256 + head * 8 + w, gate, __ATOMIC_RELAXED, __HIP_MEMORY_SCOPE_AGENT);
#undef ASTEP
#undef AAD
    }
#undef STEP
#undef MF
#undef EX
#undef P8
#undef SG_LD
#undef SG_OFF
    STAMP_AT(12);
    LBAR();

    {
        {
            unsigned pv0; const unsigned* fOBq = fOB + (rt3 >> 1) * 32;
            POLL_ISSUE(pv0, fOBq, 32);
#pragma unroll
            for (int st = 6; st < 10; ++st) { P3_GW(st, 0); P3_GW(st, 1); }
            asm volatile("s_waitcnt vmcnt(8)" : "+v"(pv0)::"memory");
            if (!__all(pv0 == token)) poll_spin(fOBq, 32, token, lane);
        }
        LBAR();
        if (tid == 0) flag_set(fO + rt * 32 + head, token);
        STAMP_AT(13);
        STAMP_AT(14);
        const char* osrc = ws + WS_O + ((size_t)(rt3 * 4 + p3) * 64 + half * 16) * 2048;
        bf16x8 of[7][4];
#define P3_GO(st, i) asm volatile("global_load_dwordx4 %0, %1, %2 offset:" #i "*1024 sc1" : "=v"(of[(st) % 7][i]) : "v"(l16), "s"(osrc + ((st) < 8 ? 65536 : 0) + ((st) & 7) * 4096) : "memory")
#define P3_GO4(st) do { P3_GO(st, 0); P3_GO(st, 1); P3_GO(st, 2); P3_GO(st, 3); } while (0)
        P3_GO4(0); P3_GO4(1); P3_GO4(2);
        float gq[2];
#define P3_GG() do { const float* gp_ = (const float*)(ws + WS_G) + rt3 * 128 + p3 * 32; const unsigned go_ = (lane & 15) * 4; \
          asm volatile("global_load_dword %0, %2, %3 sc1\n\tglobal_load_dword %1, %2, %3 offset:64 sc1" : "=&v"(gq[0]), "=&v"(gq[1]) : "v"(go_), "s"(gp_) : "memory"); } while (0)
        const int g4 = lane >> 4, q4 = (lane & 15) >> 2;
        unsigned wa[4];
        { const unsigned a0 = (16 * (g4 >> 1) + 4 * (g4 & 1) + q4) * 128 + ((2 * (g4 & 1) + (q4 >> 1)) * 32) + (lane & 3) * 8;
#pragma unroll
          for (int n = 0; n < 4; ++n) wa[n] = lds0 + half * 8192 + (a0 ^ (n << 5)) + P3_SOFF(0); }
        f32x4 ac[2][2][4];
#pragma unroll
        for (int i = 0; i < 4; ++i)
#pragma unroll
            for (int j = 0; j < 4; ++j) ac[i >> 1][i & 1][j] = f32x4{};
        s16x4 wl[2][4], wh[2][4];
#define P3_RW(buf, k_, n_) do { wl[buf][n_] = ldtr(wa[n_] + (k_) * 4096); wh[buf][n_] = ldtr(wa[n_] + (k_) * 4096 + 1024); } while (0)
#define P3_MMA(st, k_, n_, wf_) do { ac[(st) >> 3][0][n_] = MFMA16(wf_, of[(st) % 7][2 * (k_)], ac[(st) >> 3][0][n_]); ac[(st) >> 3][1][n_] = MFMA16(wf_, of[(st) % 7][2 * (k_) + 1], ac[(st) >> 3][1][n_]); } while (0)
#define P3_K32(st, k_, X0, X1, X2, X3) do { \
            if ((k_) == 1 && (st) + 1 < 16) { _Pragma("unroll") for (int n_ = 0; n_ < 4; ++n_) { wa[n_] += (unsigned)(P3_SOFF((st) + 1) - P3_SOFF(st)); PIN(wa[n_]); } } \
            _Pragma("unroll") for (int n_ = 0; n_ < 4; ++n_) { \
                const bf16x8 wf_ = cat8(wl[k_][n_], wh[k_][n_]); \
                P3_MMA(st, k_, n_, wf_); \
                if ((k_) == 0) P3_RW(1, 1, n_); else if ((st) + 1 < 16) P3_RW(0, 0, n_); \
                if (n_ == 0) { X0; } if (n_ == 1) { X1; } if (n_ == 2) { X2; } if (n_ == 3) { X3; } \
                SBAR(); } } while (0)
#define P3_OW(st, NW) asm volatile("s_waitcnt vmcnt(" #NW ")" : "+v"(of[(st) % 7][0]), "+v"(of[(st) % 7][1]), "+v"(of[(st) % 7][2]), "+v"(of[(st) % 7][3])::"memory")
#define P3_STAGE(st, NW, BAR, nx) do { \
            P3_OW(st, NW); if (BAR) LBAR(); \
            P3_K32(st, 0, NOP_, NOP_, NOP_, NOP_); \
            if ((nx) < 16) { P3_K32(st, 1, P3_GO(nx, 0), P3_GO(nx, 1), P3_GO(nx, 2), P3_GO(nx, 3)); } \
            else { P3_K32(st, 1, NOP_, NOP_, NOP_, NOP_); } \
        } while (0)
#pragma unroll
        for (int n_ = 0; n_ < 4; ++n_) P3_RW(0, 0, n_);
        unsigned pv;
        const unsigned* fOAq = fO + (rt3 >> 1) * 32;
        P3_OW(0, 8);
        P3_K32(0, 0, P3_GO(3, 0), P3_GO(3, 1), P3_GO(3, 2), P3_GO(3, 3)); P3_K32(0, 1, P3_GO(4, 0), P3_GO(4, 1), P3_GO(4, 2), P3_GO(4, 3));
        P3_OW(1, 12);
        P3_K32(1, 0, P3_GO(5, 0), P3_GO(5, 1), P3_GO(5, 2), P3_GO(5, 3)); P3_K32(1, 1, P3_GO(6, 0), P3_GO(6, 1), P3_GO(6, 2), P3_GO(6, 3));
        P3_STAGE(2, 16, 0, 7);
        P3_OW(3, 16); LBAR();
        P3_K32(3, 0, POLL_ISSUE(pv, fOAq, 32), P3_GW(10, 0), P3_GW(10, 1), P3_GW(11, 0)); P3_K32(3, 1, P3_GW(11, 1), P3_GW(12, 0), P3_GW(12, 1), NOP_);
        asm volatile("s_waitcnt vmcnt(6)" : "+v"(pv), "+v"(of[4][0]), "+v"(of[4][1]), "+v"(of[4][2]), "+v"(of[4][3])::"memory");
        if (!__all(pv == token)) poll_spin(fOAq, 32, token, lane);
        P3_K32(4, 0, P3_GG(), P3_GO(8, 0); P3_GO(8, 1), P3_GO(8, 2); P3_GO(8, 3), P3_GO(9, 0); P3_GO(9, 1)); P3_K32(4, 1, P3_GO(9, 2); P3_GO(9, 3), P3_GO(10, 0); P3_GO(10, 1), P3_GO(10, 2), P3_GO(10, 3));
        P3_STAGE(5, 29, 0, 11);
        P3_OW(6, 29); LBAR();
        P3_K32(6, 0, P3_GW(13, 0), P3_GW(13, 1), P3_GW(14, 0), P3_GW(14, 1)); P3_K32(6, 1, P3_GW(15, 0); P3_GW(15, 1); P3_GO(12, 0), P3_GO(12, 1), P3_GO(12, 2), P3_GO(12, 3));
        P3_STAGE(7, 35, 0, 13); P3_STAGE(8, 26, 0, 14);
        P3_STAGE(9, 26, 1, 15); P3_STAGE(10, 26, 0, 16); P3_STAGE(11, 22, 0, 16);
        P3_STAGE(12, 12, 1, 16); P3_STAGE(13, 8, 0, 16); P3_STAGE(14, 4, 0, 16); P3_STAGE(15, 0, 0, 16);
        asm volatile("" : "+v"(gq[0]), "+v"(gq[1]));
        f32x4 r2[2][4];
#pragma unroll
        for (int n = 0; n < 4; ++n) { r2[0][n] = ac[1][0][n] * gq[0] + ac[0][0][n] * (1.0f - gq[0]); r2[1][n] = ac[1][1][n] * gq[1] + ac[0][1][n] * (1.0f - gq[1]); }
        LAS f32x4* mine = (LAS f32x4*)(uintptr_t)(lds0 + w * 4096 + l16);
        LAS f32x4* theirs = (LAS f32x4*)(uintptr_t)(lds0 + (w ^ 4) * 4096 + l16);
#pragma unroll
        for (int q = 0; q < 2; ++q) { mine[(q * 2) * 64] = half ? r2[q][0] : r2[q][2]; mine[(q * 2 + 1) * 64] = half ? r2[q][1] : r2[q][3]; }
        LBAR();
#pragma unroll
        for (int q = 0; q < 2; ++q)
#pragma unroll
            for (int e = 0; e < 2; ++e) {
                const f32x4 r = (half ? r2[q][2 + e] : r2[q][e]) + theirs[(q * 2 + e) * 64];
                *(LAS u32x2*)(uintptr_t)(lds0 + 32768 + (p3 * 32 + q * 16 + (lane & 15)) * 144 + half * 64 + e * 32 + g4 * 8) = u32x2{cvtpk(r[0], r[1]), cvtpk(r[2], r[3])};
            }
        LBAR();
#pragma unroll
        for (int i = 0; i < 2; ++i) {
            const int row = w * 16 + i * 8 + (lane >> 3);
            const u32x4 v = *(const LAS u32x4*)(uintptr_t)(lds0 + 32768 + row * 144 + (lane & 7) * 16);
            char* op = (char*)out + (size_t)(rt3 * 128) * 2048 + ct * 128;
            *(u32x4*)(op + row * 2048 + (lane & 7) * 16) = v;
        }
#undef P3_GO
#undef P3_GO4
#undef P3_MMA
#undef P3_GG
#undef P3_RW
#undef P3_K32
#undef P3_OW
#undef P3_STAGE
    }
#undef P3_GW
#undef P3_SOFF
    if (bid == 0 && w == 0) {
        const unsigned long long t0 = __builtin_amdgcn_s_memrealtime();
        for (;;) {
            bool ok = true;
#pragma unroll
            for (int i = 0; i < 4; ++i) ok = ok && (__hip_atomic_load((GAS unsigned*)(fO + i * 64 + lane), __ATOMIC_RELAXED, __HIP_MEMORY_SCOPE_AGENT) == token);
            if (__all(ok)) break;
            if (__builtin_amdgcn_s_memrealtime() - t0 > 400000ull) break;
            __builtin_amdgcn_s_sleep(1);
        }
        if (lane == 0) flag_set(flags, token);
    }
    STAMP_AT(15);
    STAMP_FLUSH(ws, 16);
}

extern "C" void kernel_launch(void* const* d_in, const int* in_sizes, int n_in, void* d_out, int out_size, void* d_ws, size_t ws_size, hipStream_t stream) {
    (void)in_sizes; (void)n_in; (void)out_size; (void)ws_size;
    const u16* x = (const u16*)d_in[0]; const u16* wqkvA = (const u16*)d_in[1]; const u16* woA = (const u16*)d_in[2];
    const u16* wqkvB = (const u16*)d_in[4]; const u16* woB = (const u16*)d_in[5]; const u16* gw = (const u16*)d_in[6];
    (void)hipFuncSetAttribute((const void*)k_fused, hipFuncAttributeMaxDynamicSharedMemorySize, LDS_BYTES);
    k_fused<<<256, 512, LDS_BYTES, stream>>>(x, wqkvA, wqkvB, woA, woB, gw, (char*)d_ws, (u16*)d_out);
}
```

```cpp
#define STAMP
#include <hip/hip_runtime.h>
#include <hip/hip_bf16.h>
#include <stdint.h>
#include <stdio.h>
#pragma clang diagnostic ignored "-Winline-asm"

typedef unsigned short u16;
typedef short bf16x8 __attribute__((ext_vector_type(8)));
typedef short s16x4 __attribute__((ext_vector_type(4)));
typedef float f32x16 __attribute__((ext_vector_type(16)));
typedef float f32x4 __attribute__((ext_vector_type(4)));
typedef float f32x2 __attribute__((ext_vector_type(2)));
typedef __bf16 bf16x2_t __attribute__((ext_vector_type(2)));
typedef unsigned u32x4 __attribute__((ext_vector_type(4)));
typedef unsigned u32x2 __attribute__((ext_vector_type(2)));
#define LAS __attribute__((address_space(3)))
#define GAS __attribute__((address_space(1)))
#define DI __device__ __forceinline__
#define MFMA32(a, b, c) __builtin_amdgcn_mfma_f32_32x32x16_bf16((a), (b), (c), 0, 0, 0)
#define MFMA16(a, b, c) __builtin_amdgcn_mfma_f32_16x16x32_bf16((a), (b), (c), 0, 0, 0)
#define SBAR() __builtin_amdgcn_sched_barrier(0)
#define PIN(x) asm volatile("" : "+v"(x))
#define WAITCNT_VM_LGKM0(N) __builtin_amdgcn_s_waitcnt(((N) & 15) | (((N) >> 4) << 14) | 0x70)
#define WAIT_BAR(N) do { asm volatile("" ::: "memory"); WAITCNT_VM_LGKM0(N); __builtin_amdgcn_s_barrier(); asm volatile("" ::: "memory"); } while (0)
#define LBAR() WAIT_BAR(63)
#define NOP_ do {} while (0)

constexpr float QSCALE = 0.17677669529663687f * 1.4426950408889634f;
constexpr size_t WS_KV = 0;
constexpr size_t WS_O = 16u << 20;
constexpr size_t WS_G = 24u << 20;
constexpr size_t WS_F = (24u << 20) + 8192;
constexpr int LDS_BYTES = 163840;

DI unsigned cvtpk(float lo, float hi) { f32x2 v = {lo, hi}; bf16x2_t b = __builtin_convertvector(v, bf16x2_t); return __builtin_bit_cast(unsigned, b); }
DI bf16x8 pack8(const f32x16& x, int s, float sc) {
    u32x4 w = {cvtpk(x[8 * s] * sc, x[8 * s + 1] * sc), cvtpk(x[8 * s + 2] * sc, x[8 * s + 3] * sc), cvtpk(x[8 * s + 4] * sc, x[8 * s + 5] * sc), cvtpk(x[8 * s + 6] * sc, x[8 * s + 7] * sc)};
    return __builtin_bit_cast(bf16x8, w);
}
DI u32x4 pack44(const f32x4& a, const f32x4& b, float sc) { return u32x4{cvtpk(a[0] * sc, a[1] * sc), cvtpk(a[2] * sc, a[3] * sc), cvtpk(b[0] * sc, b[1] * sc), cvtpk(b[2] * sc, b[3] * sc)}; }
DI void glds16(unsigned voff, const void* sbase, unsigned lds_dst) {
    asm volatile("s_mov_b32 m0, %2\n\ts_nop 0\n\tglobal_load_lds_dwordx4 %0, %1" ::"v"(voff), "s"(sbase), "s"(lds_dst) : "memory", "m0");
}
DI void glds16c(unsigned voff, const void* sbase, unsigned lds_dst) {
    asm volatile("s_mov_b32 m0, %2\n\ts_nop 0\n\tglobal_load_lds_dwordx4 %0, %1 sc1" ::"v"(voff), "s"(sbase), "s"(lds_dst) : "memory", "m0");
}
DI void glds16m(unsigned voff, const void* sbase, unsigned lds_dst, unsigned mask) {
    asm volatile("s_mov_b32 m0, %2\n\ts_mov_b32 exec_lo, %3\n\ts_mov_b32 exec_hi, %3\n\tglobal_load_lds_dwordx4 %0, %1\n\ts_mov_b64 exec, -1" ::"v"(voff), "s"(sbase), "s"(lds_dst), "s"(mask) : "memory", "m0");
}
DI void st16_wt(unsigned voff, void* sbase, u32x4 v) { asm volatile("global_store_dwordx4 %0, %1, %2 sc1\n\ts_nop 1" ::"v"(voff), "v"(v), "s"(sbase) : "memory"); }
DI void st16_wtm(unsigned voff, void* sbase, u32x4 v, unsigned mask) {
    asm volatile("s_mov_b32 exec_lo, %3\n\ts_mov_b32 exec_hi, %3\n\tglobal_store_dwordx4 %0, %1, %2 sc1\n\ts_mov_b64 exec, -1\n\ts_nop 0" ::"v"(voff), "v"(v), "s"(sbase), "s"(mask) : "memory");
}
typedef short v4i16_t __attribute__((ext_vector_type(4)));
DI s16x4 ldtr(unsigned a) { return __builtin_bit_cast(s16x4, __builtin_amdgcn_ds_read_tr16_b64_v4i16((LAS v4i16_t*)(uintptr_t)a)); }
DI bf16x8 ld128(unsigned a) { return *(const LAS bf16x8*)(uintptr_t)a; }
DI void st128(unsigned a, u32x4 v) { *(LAS u32x4*)(uintptr_t)a = v; }
DI bf16x8 cat8(s16x4 lo, s16x4 hi) { return __builtin_shufflevector(lo, hi, 0, 1, 2, 3, 4, 5, 6, 7); }

#define STAMP_DECL unsigned long long st_r[16], st_c[16]
#define STAMP_AT(i) do { __builtin_amdgcn_sched_barrier(0); st_r[i] = __builtin_amdgcn_s_memrealtime(); st_c[i] = __builtin_amdgcn_s_memtime(); __builtin_amdgcn_s_waitcnt(0xC07F); __builtin_amdgcn_sched_barrier(0); } while (0)
#define STAMP_FLUSH(wsp, n) do { if (threadIdx.x == 0) { unsigned long long* sp_ = (unsigned long long*)((char*)(wsp) + (64u << 20)) + blockIdx.x * 32; \
    for (int i_ = 0; i_ < (n); ++i_) { sp_[i_ * 2] = st_r[i_]; sp_[i_ * 2 + 1] = st_c[i_]; } } } while (0)

DI void flag_set(unsigned* f, unsigned v) { __hip_atomic_store((GAS unsigned*)f, v, __ATOMIC_RELAXED, __HIP_MEMORY_SCOPE_AGENT); }
DI void poll_spin(const unsigned* f, int n, unsigned want, int lane) {
    const unsigned long long t0 = __builtin_amdgcn_s_memrealtime();
    for (;;) {
        unsigned v = want;
        if (lane < n) v = __hip_atomic_load((GAS unsigned*)(f + lane), __ATOMIC_RELAXED, __HIP_MEMORY_SCOPE_AGENT);
        if (__all(v == want)) break;
        if (__builtin_amdgcn_s_memrealtime() - t0 > 400000ull) break;
        __builtin_amdgcn_s_sleep(1);
    }
    __builtin_amdgcn_fence(__ATOMIC_ACQUIRE, "wavefront");
}
#define POLL_ISSUE(pv, f, n) do { const unsigned* p_ = (f) + (lane < (n) ? lane : 0); asm volatile("global_load_dword %0, %1, off sc1" : "=v"(pv) : "v"(p_) : "memory"); } while (0)
#define POLL_CHECK(pv, f, n) do { asm volatile("s_waitcnt vmcnt(0)" : "+v"(pv)::"memory"); if (!__all((pv) == token)) poll_spin(f, n, token, lane); } while (0)

DI float xhalf_max(float m) { auto rr = __builtin_amdgcn_permlane32_swap(__float_as_uint(m), __float_as_uint(m), false, false); return fmaxf(__uint_as_float(rr[0]), __uint_as_float(rr[1])); }
DI float xhalf_sum(float m) { auto rr = __builtin_amdgcn_permlane32_swap(__float_as_uint(m), __float_as_uint(m), false, false); return __uint_as_float(rr[0]) + __uint_as_float(rr[1]); }
DI int keyof(int r, int h) { return (r & 3) + 8 * (r >> 2) + 4 * h; }
DI float rowmax16(const f32x16& s) {
    float a = fmaxf(fmaxf(s[0], s[1]), s[2]), b = fmaxf(fmaxf(s[3], s[4]), s[5]);
#pragma unroll
    for (int r = 6; r < 16; r += 2) a = fmaxf(fmaxf(a, s[r]), s[r + 1]);
    return xhalf_max(fmaxf(a, b));
}
DI bf16x8 packp(const f32x16& p, int s) {
    u32x4 w = {cvtpk(p[8 * s], p[8 * s + 1]), cvtpk(p[8 * s + 2], p[8 * s + 3]), cvtpk(p[8 * s + 4], p[8 * s + 5]), cvtpk(p[8 * s + 6], p[8 * s + 7])};
    return __builtin_bit_cast(bf16x8, w);
}
template <bool WIN>
DI void attn_safe(const char* kv, bf16x8 q0, bf16x8 q1, int rb, int lane, f32x16& Oout, float& lout) {
    const int ql = lane & 31, h = lane >> 5;
    __builtin_amdgcn_fence(__ATOMIC_ACQUIRE, "agent");
    asm volatile("s_waitcnt vmcnt(0)" ::: "memory");
    float m = -INFINITY, l = 0.f; f32x16 O = {};
    const int nb = WIN ? (rb < 8 ? rb + 1 : 9) : 64;
    for (int j = 0; j < nb; ++j) {
        const int kb = WIN ? rb - j : j;
        const char* kp = kv + (kb >> 3) * 32768 + (kb & 7) * 2048 + lane * 16;
        const bf16x8 k0 = *(const bf16x8*)kp, k1 = *(const bf16x8*)(kp + 1024), v0 = *(const bf16x8*)(kp + 16384), v1 = *(const bf16x8*)(kp + 16384 + 1024);
        f32x16 s = {}; s = MFMA32(k0, q0, s); s = MFMA32(k1, q1, s);
        if (WIN) {
#pragma unroll
            for (int r = 0; r < 16; ++r) { const int kl = keyof(r, h); if ((j == 0 && kl > ql) || (j == 8 && kl <= ql)) s[r] = -INFINITY; }
        }
        const float mn = fmaxf(m, rowmax16(s));
        const float al = __builtin_amdgcn_exp2f(m - mn); m = mn;
        float ps = 0.f;
#pragma unroll
        for (int r = 0; r < 16; ++r) { s[r] = __builtin_amdgcn_exp2f(s[r] - mn); }
        const bf16x8 p0 = packp(s, 0), p1 = packp(s, 1);
#pragma unroll
        for (int r = 0; r < 8; ++r) { ps += __uint_as_float((unsigned)(u16)p0[r] << 16) + __uint_as_float((unsigned)(u16)p1[r] << 16); }
        l = l * al + ps;
#pragma unroll
        for (int r = 0; r < 16; ++r) O[r] *= al;
        O = MFMA32(v0, p0, O); O = MFMA32(v1, p1, O);
    }
    Oout = O; lout = xhalf_sum(l);
}
DI bool unit_ok(const f32x16& O, float l) {
    bool ok = l <= 1.0995116e12f;
    float a = 0.f;
#pragma unroll
    for (int r = 0; r < 16; ++r) a = fmaxf(a, __builtin_fabsf(O[r]));
    bool okO = a <= 3.0e38f;
#pragma unroll
    for (int r = 0; r < 16; ++r) okO = okO && (O[r] == O[r]);
    return __all(ok && okO);
}

constexpr int P1_XSLOT = 32768, P1_WBASE = 3 * P1_XSLOT, P1_WSLOT = 12288;
constexpr int P2_AK = 0, P2_AV = 32768, P2_B = 65536, P2_SLOT = 32768;
constexpr int P2_Q = P2_B + 2 * P2_SLOT;
constexpr int P3_SLOT = 16384;

__global__ __launch_bounds__(512, 2) void k_fused(const u16* __restrict__ x, const u16* __restrict__ wA, const u16* __restrict__ wB, const u16* __restrict__ woA,
                                                  const u16* __restrict__ woB, const u16* __restrict__ gw, char* __restrict__ ws, u16* __restrict__ out) {
    extern __shared__ __attribute__((aligned(16))) char lds[];
    const unsigned lds0 = (unsigned)(uintptr_t)(LAS char*)lds;
    STAMP_DECL; STAMP_AT(0);
    const int tid = threadIdx.x, lane = tid & 63;
    const int w = __builtin_amdgcn_readfirstlane(tid >> 6);
    const int bid = blockIdx.x, xcd = bid & 7, idx = bid >> 3;
    const int head = xcd * 4 + (idx >> 3), rt = idx & 7;
    const int wm = (w & 1) | ((w >> 2) << 1), wn = (w >> 1) & 1, qw = 2 * wm + wn;
    unsigned* flags = (unsigned*)(ws + WS_F);
    const unsigned l16 = lane * 16;
    float gate;
    unsigned token;

    f32x16 acc[2][3];
    {
        const char* xg = (const char*)x + (size_t)rt * 256 * 2048;
        const bool w0 = w == 0, w7 = w == 7;
        const int whalf = w >= 4;
        unsigned vo[4], ve[4];
        const char* ebA; const char* ebB; unsigned estride, edst;
        {
            const int r8 = lane >> 3, c8 = lane & 7;
            const int sc = (lane & 3) ^ (((lane >> 5) & 1) << 1);
#pragma unroll
            for (int j = 0; j < 4; ++j) {
                const int p = 4 * (w7 ? 0 : w) + j;
                vo[j] = (unsigned)((8 * p + r8) * 2048 + ((c8 ^ ((4 * p + (lane >> 4)) & 7)) << 4));
                const int pe = 28 + j;
                const unsigned vxe = (unsigned)((8 * pe + r8) * 2048 + ((c8 ^ ((4 * pe + (lane >> 4)) & 7)) << 4));
                const int i = 4 * ((w7 || w0 ? 1 : w) - 1) + j, ii = i >= 12 ? i - 12 : i, seg = ii >> 1, rg = ii & 1, type = seg >= 3 ? seg - 3 : seg;
                const unsigned vwe = (unsigned)((whalf * 32 + rg * 16 + (lane >> 2)) * 6144 + type * 2048 + (sc << 4));
                ve[j] = w0 ? vxe : vwe;
            }
            const int i0 = 4 * ((w7 || w0 ? 1 : w) - 1), ii0 = i0 >= 12 ? i0 - 12 : i0;
            ebA = w0 ? xg : (const char*)(ii0 >= 6 ? wB : wA) + head * 64;
            ebB = w0 ? xg : (const char*)(ii0 + 2 >= 6 ? wB : wA) + head * 64;
            estride = w0 ? 128u : 64u * 6144u;
            edst = w0 ? 28 * 1024 : P1_WBASE + ii0 * 1024;
        }
        const unsigned odst = lds0 + (w7 ? 0 : w) * 4096;
        const unsigned mstage = __builtin_amdgcn_readfirstlane(w7 ? 0u : ~0u), mwarm = ~mstage;
        const char* pfw = (const char*)(bid < 128 ? wA : wB);
        unsigned pfo;
        { const int L = (bid & 127) * 24 + (lane < 24 ? lane : 0); pfo = (unsigned)((L / 48) * 6144 + (L % 48) * 128); }
#define P1_PF(base, off) asm volatile("s_mov_b32 m0, %3\n\ts_mov_b32 exec_lo, %2\n\ts_mov_b32 exec_hi, %2\n\tglobal_load_lds_dword %0, %1\n\ts_mov_b64 exec, -1" ::"v"(off), "s"(base), "s"(mwarm), "s"(lds0 + P1_WBASE + 5 * P1_WSLOT) : "memory", "m0")
#define P1_GO(og_, od_, j) glds16m(vo[j], og_, (od_) + (j) * 1024, mstage)
#define P1_GE(eA_, eB_, ed_, j) glds16m(ve[j], (j) < 2 ? (eA_) : (eB_), (ed_) + (j) * 1024, mstage)
#define P1_GO2(og_, od_, j) do { if (HF_ == 0) glds16(vo[j], og_, (od_) + (j) * 1024); else P1_GO(og_, od_, j); } while (0)
#define P1_GE2(eA_, eB_, ed_, j) do { if (HF_ == 0) glds16(ve[j], (j) < 2 ? (eA_) : (eB_), (ed_) + (j) * 1024); else P1_GE(eA_, eB_, ed_, j); } while (0)
#define P1_EDST(xo_, h0, h1) (lds0 + edst + (w0 ? (xo_) : (whalf ? (h1) : (h0))))
#pragma unroll
        for (int j = 0; j < 4; ++j) P1_GO(xg, odst, j);
        { const unsigned ed = P1_EDST(0, 0, P1_WSLOT);
#pragma unroll
          for (int j = 0; j < 4; ++j) P1_GE(ebA, ebB, ed, j); }
        u32x4 gx0, gx1, gw0, gw1;
        {
            const char* px = (const char*)x + (size_t)(rt * 256 + head * 8 + w) * 2048;
            const unsigned go = lane * 32, z = 0;
            asm volatile("global_load_dwordx4 %0, %5, %6\n\tglobal_load_dwordx4 %1, %5, %6 offset:16\n\tglobal_load_dwordx4 %2, %5, %7\n\tglobal_load_dwordx4 %3, %5, %7 offset:16\n\t"
                         "global_load_dword %4, %8, %9 sc1"
                         : "=&v"(gx0), "=&v"(gx1), "=&v"(gw0), "=&v"(gw1), "=&v"(token) : "v"(go), "s"(px), "s"(gw), "v"(z), "s"(flags) : "memory");
        }
#pragma unroll
        for (int j = 0; j < 4; ++j) P1_GO(xg + 128, odst + P1_XSLOT, j);
        { const unsigned ed = P1_EDST(P1_XSLOT, 2 * P1_WSLOT, 3 * P1_WSLOT);
#pragma unroll
          for (int j = 0; j < 4; ++j) P1_GE(ebA + estride, ebB + estride, ed, j); }
        ebA += 2 * (size_t)estride; ebB += 2 * (size_t)estride;
        const int hh1 = lane >> 5;
        unsigned xa[4], wa;
#pragma unroll
        for (int u = 0; u < 4; ++u) xa[u] = lds0 + (wm * 64 + (lane & 31)) * 128 + (((2 * u + hh1) ^ ((lane >> 1) & 7)) << 4);
        wa = lds0 + P1_WBASE + wn * 6144 + (8 * hh1 + ((lane & 15) >> 2)) * 64 + ((((lane >> 4) & 1) ^ hh1) * 32) + (lane & 3) * 8;
#pragma unroll
        for (int i = 0; i < 2; ++i)
#pragma unroll
            for (int j = 0; j < 3; ++j) acc[i][j] = f32x16{};
        bf16x8 xf[2][2][2]; s16x4 wl[2][2][3], wh[2][2][3];
#define P1_WF(b, k, t) cat8(wl[b][k][t], wh[b][k][t])
#define P1_MM(b, k, r, t) acc[r][t] = ((t) < 2) ? MFMA32(P1_WF(b, k, t), xf[b][k][r], acc[r][t]) : MFMA32(xf[b][k][r], P1_WF(b, k, t), acc[r][t])
#define P1_RW(b, wa_, k, t) do { wl[b][k][t] = ldtr((wa_) + (t) * 2048 + (k) * 1024); wh[b][k][t] = ldtr((wa_) + (t) * 2048 + (k) * 1024 + 256); } while (0)
#define P1_STEP(HF, c, n, xo, u, wo, RD, G0, G1, G2, G3) do { \
            const unsigned xa0_ = xa[2 * (u)] + (xo), xa1_ = xa[2 * (u) + 1] + (xo), wa_ = wa + (wo); \
            P1_MM(c, 0, 0, 0); if (RD) { xf[n][0][0] = ld128(xa0_); P1_RW(n, wa_, 0, 0); } if (HF == 0) { G0; } SBAR(); \
            P1_MM(c, 0, 0, 1); if (RD) { P1_RW(n, wa_, 0, 1); } SBAR(); \
            P1_MM(c, 0, 0, 2); if (RD) { P1_RW(n, wa_, 0, 2); } if (HF == 1) { G0; } SBAR(); \
            P1_MM(c, 0, 1, 0); if (RD) { xf[n][0][1] = ld128(xa0_ + 4096); } if (HF == 0) { G1; } SBAR(); \
            P1_MM(c, 0, 1, 1); if (RD) { xf[n][1][0] = ld128(xa1_); } SBAR(); \
            P1_MM(c, 0, 1, 2); if (RD) { P1_RW(n, wa_, 1, 0); } if (HF == 1) { G1; } SBAR(); \
            P1_MM(c, 1, 0, 0); if (RD) { P1_RW(n, wa_, 1, 1); } if (HF == 0) { G2; } SBAR(); \
            P1_MM(c, 1, 0, 1); if (RD) { P1_RW(n, wa_, 1, 2); } SBAR(); \
            P1_MM(c, 1, 0, 2); if (RD) { xf[n][1][1] = ld128(xa1_ + 4096); } if (HF == 1) { G2; } SBAR(); \
            P1_MM(c, 1, 1, 0); if (HF == 0) { G3; } SBAR(); \
            P1_MM(c, 1, 1, 1); if (HF == 1) { G3; } SBAR(); \
            P1_MM(c, 1, 1, 2); SBAR(); } while (0)
#define P1_GATE() do {     \
            asm volatile("" : "+v"(gx0), "+v"(gx1), "+v"(gw0), "+v"(gw1), "+v"(token)); \
            float s_ = 0.f; \
            _Pragma("unroll") for (int i = 0; i < 4; ++i) { \
                s_ += __uint_as_float(gx0[i] << 16) * __uint_as_float(gw0[i] << 16) + __uint_as_float(gx0[i] & 0xffff0000u) * __uint_as_float(gw0[i] & 0xffff0000u); \
                s_ += __uint_as_float(gx1[i] << 16) * __uint_as_float(gw1[i] << 16) + __uint_as_float(gx1[i] & 0xffff0000u) * __uint_as_float(gw1[i] & 0xffff0000u); } \
            gate = s_; PIN(gate); \
            token = __builtin_amdgcn_readfirstlane(token) + 1u; } while (0)
        STAMP_AT(1);
        if (w7) asm volatile("s_waitcnt vmcnt(0)" ::: "memory");
        WAIT_BAR(8);
        STAMP_AT(2);
#pragma unroll
        for (int j = 0; j < 4; ++j) P1_GO(xg + 256, odst + 2 * P1_XSLOT, j);
        P1_GATE();
        if (w7) {
            P1_PF(pfw + 2 * (64 * 6144), pfo); P1_PF(pfw + 3 * (64 * 6144), pfo);
            const char* xl = (const char*)x + (size_t)bid * 16384; const unsigned lo = lane * 128;
            P1_PF(xl, lo); P1_PF(xl + 8192, lo);
            P1_PF(pfw + 4 * (64 * 6144), pfo); P1_PF(pfw + 5 * (64 * 6144), pfo);
        }
        { xf[0][0][0] = ld128(xa[0]); xf[0][0][1] = ld128(xa[0] + 4096); xf[0][1][0] = ld128(xa[1]); xf[0][1][1] = ld128(xa[1] + 4096);
#pragma unroll
          for (int n = 0; n < 6; ++n) P1_RW(0, wa, n / 3, n % 3); }
        LBAR();
        unsigned xo0 = 0, xo1 = P1_XSLOT, xo2 = 2 * P1_XSLOT;
        unsigned wo0 = 0, wo1 = P1_WSLOT, wo2 = 2 * P1_WSLOT, wo3 = 3 * P1_WSLOT, wo4 = 4 * P1_WSLOT;
#define P1_TILE(HF, T, IE, IO, WA, WB, RDLAST, PFX) do { constexpr int HF_ = HF; \
            const unsigned ed_ = P1_EDST(xo2, wo4, wo0), od_ = odst + xo0; const char* og_ = xg + ((T) + 3) * 128; \
            P1_STEP(HF, 0, 1, xo0, 1, wo1, P1_RD0, if (IE) P1_GE2(ebA, ebB, ed_, 0), if (IE) P1_GE2(ebA, ebB, ed_, 1), if (IE) P1_GE2(ebA, ebB, ed_, 2), if (IE) P1_GE2(ebA, ebB, ed_, 3)); \
            WA; \
            PFX; \
            P1_STEP(HF, 1, 0, xo1, 0, wo2, RDLAST, if (IO) P1_GO2(og_, od_, 0), if (IO) P1_GO2(og_, od_, 1), if (IO) P1_GO2(og_, od_, 2), if (IO) P1_GO2(og_, od_, 3)); \
            WB; \
            ebA += estride; ebB += estride; \
            { const unsigned t_ = xo0; xo0 = xo1; xo1 = xo2; xo2 = t_; const unsigned a_ = wo0, b_ = wo1; wo0 = wo2; wo1 = wo3; wo2 = wo4; wo3 = a_; wo4 = b_; } } while (0)
#define P1_WB(N) WAIT_BAR(N)
#define P1_RDL 1
#define P1_RD0 1
#define P1_LOOP(HF) do { \
            for (int T = 0; T < 13; ++T) { \
                  \
                const char* pb = T < 10 ? pfw + (size_t)(T + 6) * (64 * 6144) : (const char*)(bid < 128 ? woA : woB) + (bid & 127) * 16384 + (T & 1) * 8192; \
                const unsigned po = T < 10 ? pfo : lane * 128; \
                P1_TILE(HF, T, 1, 1, P1_WB(8), P1_WB(12), P1_RDL, if (HF == 1) P1_PF(pb, po)); \
            } \
            P1_TILE(HF, 13, 1, 0, P1_WB(8), P1_WB(8), 1, NOP_); \
            P1_TILE(HF, 14, 0, 0, P1_WB(0), NOP_, 1, NOP_); \
            P1_TILE(HF, 15, 0, 0, NOP_, NOP_, 0, NOP_); } while (0)
        if (w < 4) P1_LOOP(0); else P1_LOOP(1);
        asm volatile("s_waitcnt vmcnt(0)" ::: "memory");
#undef P1_LOOP
#undef P1_GATE
#undef P1_TILE
#undef P1_STEP
#undef P1_RW
#undef P1_MM
#undef P1_WF
#undef P1_GO
#undef P1_GE
#undef P1_EDST
#undef P1_PF
    }
    STAMP_AT(3);
#pragma unroll
    for (int o = 32; o >= 1; o >>= 1) gate += __shfl_xor(gate, o);
    gate = 1.0f / (1.0f + __expf(-gate));
    u32x4 keep[8];
    bf16x8 qown0, qown1;
    const unsigned mB = __builtin_amdgcn_readfirstlane(wn ? ~0u : 0u), mA = ~mB;
    char* kvg = ws + WS_KV + (size_t)((wn * 32 + head) * 8 + rt) * 32768;
    const unsigned kvo = wm * 4096 + l16;
    {
#pragma unroll
        for (int r = 0; r < 2; ++r)
#pragma unroll
            for (int s = 0; s < 2; ++s) {
                const unsigned o = kvo + r * 2048 + s * 1024;
                keep[r * 2 + s] = __builtin_bit_cast(u32x4, pack8(acc[r][1], s, 1.0f)); keep[4 + r * 2 + s] = __builtin_bit_cast(u32x4, pack8(acc[r][2], s, 1.0f));
                st16_wtm(o, kvg, keep[r * 2 + s], mB); st16_wtm(16384 + o, kvg, keep[4 + r * 2 + s], mB);
            }
        const f32x16 qk = wn ? acc[1][0] : acc[0][0], qs = wn ? acc[0][0] : acc[1][0];
        qown0 = pack8(qk, 0, QSCALE); qown1 = pack8(qk, 1, QSCALE);
        st128(lds0 + P2_Q + w * 2048 + l16, __builtin_bit_cast(u32x4, pack8(qs, 0, QSCALE))); st128(lds0 + P2_Q + w * 2048 + 1024 + l16, __builtin_bit_cast(u32x4, pack8(qs, 1, QSCALE)));
        if (wn) {
#pragma unroll
            for (int i = 0; i < 4; ++i) { const unsigned o = kvo + (i >> 1) * 2048 + (i & 1) * 1024; st128(lds0 + P2_B + o, keep[i]); st128(lds0 + P2_B + 16384 + o, keep[4 + i]); }
        }
    }
    WAIT_BAR(0);
    STAMP_AT(4);

    const int rb = rt * 8 + qw;
    const int ql = lane & 31, hh = lane >> 5;
    const char* kvA = ws + WS_KV + (size_t)(head * 8) * 32768;
    const char* kvB = ws + WS_KV + (size_t)((32 + head) * 8) * 32768;
    unsigned* fKB = flags + 64 + head * 8; unsigned* fO = flags + 64 + 256; unsigned* fKA = flags + 64 + 512 + head * 8; unsigned* fOB = flags + 64 + 768;
    const int rtp = rt > 0 ? rt - 1 : 0;
    const unsigned* fKAp = fKA + rtp;
    if (tid == 0) flag_set(fKB + rt, token);
    bf16x8 sel;
    { const short o = (((lane >> 4) & 1) == ((lane >> 2) & 1)) ? (short)0x3F80 : (short)0;
#pragma unroll
      for (int i = 0; i < 8; ++i) sel[i] = o; }
    const bf16x8 qx0 = ld128(lds0 + P2_Q + (w ^ 2) * 2048 + l16), qx1 = ld128(lds0 + P2_Q + (w ^ 2) * 2048 + 1024 + l16);
    const bf16x8 qa0 = wn ? qx0 : qown0, qa1 = wn ? qx1 : qown1, qb0 = wn ? qown0 : qx0, qb1 = wn ? qown1 : qx1;
    char* og = ws + WS_O + ((size_t)rb * 64 + head) * 2048;
    const unsigned oo = (ql >> 4) * 1024 + (hh * 16 + (ql & 15)) * 16;
    const int rt3 = (xcd & 3) * 4 + (idx & 3), ct = (xcd >> 2) * 8 + (idx >> 2);
    const int p3 = w & 3, half = w >> 2;
    const char* wsrcB = (const char*)woB + ct * 128 + (size_t)half * 512 * 2048; const char* wsrcA = (const char*)woA + ct * 128 + (size_t)half * 512 * 2048;
    const unsigned vw0 = (unsigned)((16 * p3 + (lane >> 3)) * 2048 + (((lane & 7) ^ (((lane >> 4) & 3) << 1)) << 4)), vw1 = vw0 + 8 * 2048;
    const unsigned wdst3 = lds0 + half * 8192 + p3 * 2048;
#define P3_SOFF(s) ((s) < 6 ? 65536 + (s) * P3_SLOT : (s) < 10 ? ((s) - 6) * P3_SLOT : 65536 + ((s) - 10) * P3_SLOT)
#define P3_GW(st, i) glds16((i) ? vw1 : vw0, ((st) < 8 ? wsrcB : wsrcA) + (size_t)((st) & 7) * (64 * 2048), wdst3 + P3_SOFF(st) + (i) * 1024)

#define EX(x) __builtin_amdgcn_exp2f(x)
#define P8(x) __builtin_bit_cast(bf16x8, x)
#define MF(x) do { x; SBAR(); } while (0)
#define STEP(SC, SN, Q0, Q1, AK0, AK1, AV1, AV0, X) do { \
        MF(SN = MFMA32(k0, Q0, negm)); \
        t0 = EX(SC[0]); phi[2] = cvtpk(u0, u1); t1 = EX(SC[1]); phi[3] = cvtpk(u2, u3); k0 = ld128(AK0); PIN(phi); PIN(t0); PIN(t1); SBAR(); \
        t2 = EX(SC[2]); PIN(t2); SBAR(); \
        MF(O = MFMA32(v1, P8(phi), O)); \
        t3 = EX(SC[3]); u0 = EX(SC[4]); v1 = ld128(AV1); plo[0] = cvtpk(t0, t1); PIN(t3); PIN(u0); PIN(plo); SBAR(); \
        MF(L = MFMA16(sel, P8(phi), L)); \
        u1 = EX(SC[5]); plo[1] = cvtpk(t2, t3); PIN(plo); PIN(u1); SBAR(); \
        MF(SN = MFMA32(k1, Q1, SN)); \
        u2 = EX(SC[6]); u3 = EX(SC[7]); k1 = ld128(AK1); plo[2] = cvtpk(u0, u1); PIN(u2); PIN(u3); PIN(plo); SBAR(); \
        X; SBAR(); \
        t0 = EX(SC[8]); plo[3] = cvtpk(u2, u3); t1 = EX(SC[9]); t2 = EX(SC[10]); PIN(plo); PIN(t0); PIN(t1); PIN(t2); SBAR(); \
        MF(O = MFMA32(v0, P8(plo), O)); \
        t3 = EX(SC[11]); u0 = EX(SC[12]); v0 = ld128(AV0); phi[0] = cvtpk(t0, t1); PIN(t3); PIN(u0); PIN(phi); SBAR(); \
        MF(L = MFMA16(sel, P8(plo), L)); \
        u1 = EX(SC[13]); phi[1] = cvtpk(t2, t3); u2 = EX(SC[14]); u3 = EX(SC[15]); PIN(phi); PIN(u1); PIN(u2); PIN(u3); SBAR(); \
    } while (0)
#define SG_OFF(i) (((i) >> 1) * 16384 + ((i) & 1) * 1024)
#define SG_LD(i, src, dK, dV) glds16c(l16, (src) + w * 2048 + SG_OFF(i), ((i) >> 1 ? (dV) : (dK)) + w * 2048 + ((i) & 1) * 1024)
    {
        f32x16 O = {}, negm, SA, SB; f32x4 L = {};
        bf16x8 k0, k1, v0, v1; u32x4 plo, phi; float t0, t1, t2, t3, u0 = 0.f, u1 = 0.f, u2 = 0.f, u3 = 0.f;
        unsigned cur = lds0 + P2_B + l16, nxt = cur + P2_SLOT;
        {
            k0 = ld128(cur); k1 = ld128(cur + 1024);
            SA = f32x16{}; SA = MFMA32(k0, qb0, SA); SA = MFMA32(k1, qb1, SA);
            const float m = rowmax16(SA);
#pragma unroll
            for (int r = 0; r < 16; ++r) { negm[r] = -m; SA[r] -= m; }
            k0 = ld128(cur + 2048); k1 = ld128(cur + 2048 + 1024);
            v0 = ld128(cur + 16384);
            v1 = bf16x8{}; phi = u32x4{}; plo = u32x4{};
        }
#define KAD(j, s) (((j) < 8 ? cur + (j) * 2048 : nxt + ((j) - 8) * 2048) + (s) * 1024)
#define BSTEP(SC, SN, j, X) STEP(SC, SN, qb0, qb1, KAD((j) + 2, 0), KAD((j) + 2, 1), KAD(j, 1) + 16384, KAD((j) + 1, 0) + 16384, X)
#define KEEP_A(i) st16_wtm(kvo + ((i) >> 2) * 16384 + (((i) >> 1) & 1) * 2048 + ((i) & 1) * 1024, kvg, keep[i], mA)
        unsigned pv;
        BSTEP(SA, SB, 0, POLL_ISSUE(pv, fKB, 8));
        STAMP_AT(5);
        { const char* s1 = kvB + ((rt + 1) & 7) * 32768; const unsigned d = lds0 + P2_B + P2_SLOT;
          BSTEP(SB, SA, 1, POLL_CHECK(pv, fKB, 8); SG_LD(0, s1, d, d + 16384); SG_LD(1, s1, d, d + 16384); SG_LD(2, s1, d, d + 16384); SG_LD(3, s1, d, d + 16384);
                           KEEP_A(0); KEEP_A(1); KEEP_A(2); KEEP_A(3)); }
        STAMP_AT(6);
        BSTEP(SA, SB, 2, KEEP_A(4); KEEP_A(5); KEEP_A(6); KEEP_A(7));
        STAMP_AT(7);
        BSTEP(SB, SA, 3, NOP_);
        BSTEP(SA, SB, 4, NOP_);
        STAMP_AT(8);
        BSTEP(SB, SA, 5, NOP_);
        STAMP_AT(9);
        WAIT_BAR(0);
        STAMP_AT(10);
        if (tid == 0) flag_set(fKA + rt, token);
        const char* aown = kvA + rt * 32768;
        int slot = 0;
        for (int i = 0; i < 7; ++i) {
            const int s1 = slot == 2 ? 0 : slot + 1, s2 = s1 == 2 ? 0 : s1 + 1;
            const char* src = i < 6 ? kvB + ((rt + i + 2) & 7) * 32768 : kvA + rtp * 32768;
            const unsigned dK = i < 6 ? lds0 + P2_B + s2 * P2_SLOT : lds0 + P2_AK, dV = i < 6 ? dK + 16384 : lds0 + P2_AV;
            if (i == 6) POLL_CHECK(pv, fKAp, 1);
            BSTEP(SA, SB, 6, SG_LD(0, src, dK, dV)); BSTEP(SB, SA, 7, SG_LD(1, src, dK, dV));
            cur = nxt; nxt = lds0 + P2_B + s2 * P2_SLOT + l16;
            BSTEP(SA, SB, 0, SG_LD(2, src, dK, dV)); BSTEP(SB, SA, 1, SG_LD(3, src, dK, dV));
            BSTEP(SA, SB, 2, if (i == 0) { SG_LD(0, aown, lds0 + P2_AK + 16384, lds0 + P2_AV + 16384); SG_LD(1, aown, lds0 + P2_AK + 16384, lds0 + P2_AV + 16384); });
            BSTEP(SB, SA, 3, if (i == 0) { SG_LD(2, aown, lds0 + P2_AK + 16384, lds0 + P2_AV + 16384); SG_LD(3, aown, lds0 + P2_AK + 16384, lds0 + P2_AV + 16384); });
            BSTEP(SA, SB, 4, if (i == 5) { POLL_ISSUE(pv, fKAp, 1); });
            BSTEP(SB, SA, 5, NOP_);
            WAIT_BAR(0);
            slot = s1;
        }
        BSTEP(SA, SB, 6, NOP_); BSTEP(SB, SA, 7, NOP_);
        phi[2] = cvtpk(u0, u1); phi[3] = cvtpk(u2, u3);
        O = MFMA32(v1, P8(phi), O); L = MFMA16(sel, P8(phi), L);
        float l = L[0];
        if (!unit_ok(O, l)) attn_safe<false>(kvB, qb0, qb1, rb, lane, O, l);
        const float f = 1.0f / l;
        st16_wt(oo, og + 32 * 2048, __builtin_bit_cast(u32x4, pack8(O, 0, f))); st16_wt(oo + 512, og + 32 * 2048, __builtin_bit_cast(u32x4, pack8(O, 1, f)));
#undef KEEP_A
#undef BSTEP
#undef KAD
    }
    STAMP_AT(11);
    LBAR();
    {
        f32x16 O = {}, negm, SA, SB; f32x4 L = {};
        bf16x8 k0, k1, v0, v1; u32x4 plo, phi; float t0, t1, t2, t3, u0 = 0.f, u1 = 0.f, u2 = 0.f, u3 = 0.f;
        const unsigned ab = lds0 + P2_AK + qw * 2048 + l16;
        const int jmax = rt > 0 ? 8 : qw;
#define AAD(j, s) (ab + (8 - ((j) > 8 ? 8 : (j))) * 2048 + (s) * 1024)
        {
            k0 = ld128(AAD(0, 0)); k1 = ld128(AAD(0, 1));
            SA = f32x16{}; SA = MFMA32(k0, qa0, SA); SA = MFMA32(k1, qa1, SA);
#pragma unroll
            for (int r = 0; r < 16; ++r) if (keyof(r, hh) > ql) SA[r] = -INFINITY;
            const float m = rowmax16(SA);
#pragma unroll
            for (int r = 0; r < 16; ++r) { negm[r] = -m; SA[r] -= m; }
            k0 = ld128(AAD(1, 0)); k1 = ld128(AAD(1, 1));
            v0 = ld128(AAD(0, 0) + 32768);
            v1 = bf16x8{}; phi = u32x4{}; plo = u32x4{};
        }
#define ASTEP(SC, SN, j, X) do { if ((j) + 1 > jmax) { _Pragma("unroll") for (int r_ = 0; r_ < 16; ++r_) negm[r_] = -INFINITY; } \
        STEP(SC, SN, qa0, qa1, AAD((j) + 2, 0), AAD((j) + 2, 1), AAD(j, 1) + 32768, AAD((j) + 1, 0) + 32768, X); } while (0)
        ASTEP(SA, SB, 0, P3_GW(0, 0); P3_GW(0, 1)); ASTEP(SB, SA, 1, P3_GW(1, 0); P3_GW(1, 1)); ASTEP(SA, SB, 2, P3_GW(2, 0); P3_GW(2, 1)); ASTEP(SB, SA, 3, P3_GW(3, 0); P3_GW(3, 1));
        ASTEP(SA, SB, 4, P3_GW(4, 0); P3_GW(4, 1));
        WAIT_BAR(10);
        if (tid == 0) flag_set(fOB + rt * 32 + head, token);
        ASTEP(SB, SA, 5, P3_GW(5, 0); P3_GW(5, 1)); ASTEP(SA, SB, 6, NOP_); ASTEP(SB, SA, 7, NOP_);
#pragma unroll
        for (int r = 0; r < 16; ++r) if (keyof(r, hh) <= ql) SA[r] = -INFINITY;
        ASTEP(SA, SB, 8, NOP_);
        phi[2] = cvtpk(u0, u1); phi[3] = cvtpk(u2, u3);
        O = MFMA32(v1, P8(phi), O); L = MFMA16(sel, P8(phi), L);
        float l = L[0];
        if (!unit_ok(O, l)) attn_safe<true>(kvA, qa0, qa1, rb, lane, O, l);
        const float f = 1.0f / l;
        st16_wt(oo, og, __builtin_bit_cast(u32x4, pack8(O, 0, f))); st16_wt(oo + 512, og, __builtin_bit_cast(u32x4, pack8(O, 1, f)));
        if (lane == 0) __hip_atomic_store((GAS float*)(ws + WS_G) + rt * 256 + head * 8 + w, gate, __ATOMIC_RELAXED, __HIP_MEMORY_SCOPE_AGENT);
#undef ASTEP
#undef AAD
    }
#undef STEP
#undef MF
#undef EX
#undef P8
#undef SG_LD
#undef SG_OFF
    STAMP_AT(12);
    LBAR();

    {
        {
            unsigned pv0; const unsigned* fOBq = fOB + (rt3 >> 1) * 32;
            POLL_ISSUE(pv0, fOBq, 32);
#pragma unroll
            for (int st = 6; st < 10; ++st) { P3_GW(st, 0); P3_GW(st, 1); }
            asm volatile("s_waitcnt vmcnt(8)" : "+v"(pv0)::"memory");
            if (!__all(pv0 == token)) poll_spin(fOBq, 32, token, lane);
        }
        LBAR();
        if (tid == 0) flag_set(fO + rt * 32 + head, token);
        STAMP_AT(13);
        STAMP_AT(14);
        const char* osrc = ws + WS_O + ((size_t)(rt3 * 4 + p3) * 64 + half * 16) * 2048;
        bf16x8 of[7][4];
#define P3_GO(st, i) asm volatile("global_load_dwordx4 %0, %1, %2 offset:" #i "*1024 sc1" : "=v"(of[(st) % 7][i]) : "v"(l16), "s"(osrc + ((st) < 8 ? 65536 : 0) + ((st) & 7) * 4096) : "memory")
#define P3_GO4(st) do { P3_GO(st, 0); P3_GO(st, 1); P3_GO(st, 2); P3_GO(st, 3); } while (0)
        P3_GO4(0); P3_GO4(1); P3_GO4(2);
        float gq[2];
#define P3_GG() do { const float* gp_ = (const float*)(ws + WS_G) + rt3 * 128 + p3 * 32; const unsigned go_ = (lane & 15) * 4; \
          asm volatile("global_load_dword %0, %2, %3 sc1\n\tglobal_load_dword %1, %2, %3 offset:64 sc1" : "=&v"(gq[0]), "=&v"(gq[1]) : "v"(go_), "s"(gp_) : "memory"); } while (0)
        const int g4 = lane >> 4, q4 = (lane & 15) >> 2;
        unsigned wa[4];
        { const unsigned a0 = (16 * (g4 >> 1) + 4 * (g4 & 1) + q4) * 128 + ((2 * (g4 & 1) + (q4 >> 1)) * 32) + (lane & 3) * 8;
#pragma unroll
          for (int n = 0; n < 4; ++n) wa[n] = lds0 + half * 8192 + (a0 ^ (n << 5)) + P3_SOFF(0); }
        f32x4 ac[2][2][4];
#pragma unroll
        for (int i = 0; i < 4; ++i)
#pragma unroll
            for (int j = 0; j < 4; ++j) ac[i >> 1][i & 1][j] = f32x4{};
        s16x4 wl[2][4], wh[2][4];
#define P3_RW(buf, k_, n_) do { wl[buf][n_] = ldtr(wa[n_] + (k_) * 4096); wh[buf][n_] = ldtr(wa[n_] + (k_) * 4096 + 1024); } while (0)
#define P3_MMA(st, k_, n_, wf_) do { ac[(st) >> 3][0][n_] = MFMA16(wf_, of[(st) % 7][2 * (k_)], ac[(st) >> 3][0][n_]); ac[(st) >> 3][1][n_] = MFMA16(wf_, of[(st) % 7][2 * (k_) + 1], ac[(st) >> 3][1][n_]); } while (0)
#define P3_K32(st, k_, X0, X1, X2, X3) do { \
            if ((k_) == 1 && (st) + 1 < 16) { _Pragma("unroll") for (int n_ = 0; n_ < 4; ++n_) { wa[n_] += (unsigned)(P3_SOFF((st) + 1) - P3_SOFF(st)); PIN(wa[n_]); } } \
            _Pragma("unroll") for (int n_ = 0; n_ < 4; ++n_) { \
                const bf16x8 wf_ = cat8(wl[k_][n_], wh[k_][n_]); \
                P3_MMA(st, k_, n_, wf_); \
                if ((k_) == 0) P3_RW(1, 1, n_); else if ((st) + 1 < 16) P3_RW(0, 0, n_); \
                if (n_ == 0) { X0; } if (n_ == 1) { X1; } if (n_ == 2) { X2; } if (n_ == 3) { X3; } \
                SBAR(); } } while (0)
#define P3_OW(st, NW) asm volatile("s_waitcnt vmcnt(" #NW ")" : "+v"(of[(st) % 7][0]), "+v"(of[(st) % 7][1]), "+v"(of[(st) % 7][2]), "+v"(of[(st) % 7][3])::"memory")
#define P3_STAGE(st, NW, BAR, nx) do { \
            P3_OW(st, NW); if (BAR) LBAR(); \
            P3_K32(st, 0, NOP_, NOP_, NOP_, NOP_); \
            if ((nx) < 16) { P3_K32(st, 1, P3_GO(nx, 0), P3_GO(nx, 1), P3_GO(nx, 2), P3_GO(nx, 3)); } \
            else { P3_K32(st, 1, NOP_, NOP_, NOP_, NOP_); } \
        } while (0)
#pragma unroll
        for (int n_ = 0; n_ < 4; ++n_) P3_RW(0, 0, n_);
        unsigned pv;
        const unsigned* fOAq = fO + (rt3 >> 1) * 32;
        P3_OW(0, 8);
        P3_K32(0, 0, P3_GO(3, 0), P3_GO(3, 1), P3_GO(3, 2), P3_GO(3, 3)); P3_K32(0, 1, P3_GO(4, 0), P3_GO(4, 1), P3_GO(4, 2), P3_GO(4, 3));
        P3_OW(1, 12);
        P3_K32(1, 0, P3_GO(5, 0), P3_GO(5, 1), P3_GO(5, 2), P3_GO(5, 3)); P3_K32(1, 1, P3_GO(6, 0), P3_GO(6, 1), P3_GO(6, 2), P3_GO(6, 3));
        P3_STAGE(2, 16, 0, 7);
        P3_OW(3, 16); LBAR();
        P3_K32(3, 0, POLL_ISSUE(pv, fOAq, 32), P3_GW(10, 0), P3_GW(10, 1), P3_GW(11, 0)); P3_K32(3, 1, P3_GW(11, 1), P3_GW(12, 0), P3_GW(12, 1), NOP_);
        asm volatile("s_waitcnt vmcnt(6)" : "+v"(pv), "+v"(of[4][0]), "+v"(of[4][1]), "+v"(of[4][2]), "+v"(of[4][3])::"memory");
        if (!__all(pv == token)) poll_spin(fOAq, 32, token, lane);
        P3_K32(4, 0, P3_GG(), P3_GO(8, 0); P3_GO(8, 1), P3_GO(8, 2); P3_GO(8, 3), P3_GO(9, 0); P3_GO(9, 1)); P3_K32(4, 1, P3_GO(9, 2); P3_GO(9, 3), P3_GO(10, 0); P3_GO(10, 1), P3_GO(10, 2), P3_GO(10, 3));
        P3_STAGE(5, 29, 0, 11);
        P3_OW(6, 29); LBAR();
        P3_K32(6, 0, P3_GW(13, 0), P3_GW(13, 1), P3_GW(14, 0), P3_GW(14, 1)); P3_K32(6, 1, P3_GW(15, 0); P3_GW(15, 1); P3_GO(12, 0), P3_GO(12, 1), P3_GO(12, 2), P3_GO(12, 3));
        P3_STAGE(7, 35, 0, 13); P3_STAGE(8, 26, 0, 14);
        P3_STAGE(9, 26, 1, 15); P3_STAGE(10, 26, 0, 16); P3_STAGE(11, 22, 0, 16);
        P3_STAGE(12, 12, 1, 16); P3_STAGE(13, 8, 0, 16); P3_STAGE(14, 4, 0, 16); P3_STAGE(15, 0, 0, 16);
        asm volatile("" : "+v"(gq[0]), "+v"(gq[1]));
        f32x4 r2[2][4];
#pragma unroll
        for (int n = 0; n < 4; ++n) { r2[0][n] = ac[1][0][n] * gq[0] + ac[0][0][n] * (1.0f - gq[0]); r2[1][n] = ac[1][1][n] * gq[1] + ac[0][1][n] * (1.0f - gq[1]); }
        LAS f32x4* mine = (LAS f32x4*)(uintptr_t)(lds0 + w * 4096 + l16);
        LAS f32x4* theirs = (LAS f32x4*)(uintptr_t)(lds0 + (w ^ 4) * 4096 + l16);
#pragma unroll
        for (int q = 0; q < 2; ++q) { mine[(q * 2) * 64] = half ? r2[q][0] : r2[q][2]; mine[(q * 2 + 1) * 64] = half ? r2[q][1] : r2[q][3]; }
        LBAR();
#pragma unroll
        for (int q = 0; q < 2; ++q)
#pragma unroll
            for (int e = 0; e < 2; ++e) {
                const f32x4 r = (half ? r2[q][2 + e] : r2[q][e]) + theirs[(q * 2 + e) * 64];
                *(LAS u32x2*)(uintptr_t)(lds0 + 32768 + (p3 * 32 + q * 16 + (lane & 15)) * 144 + half * 64 + e * 32 + g4 * 8) = u32x2{cvtpk(r[0], r[1]), cvtpk(r[2], r[3])};
            }
        LBAR();
#pragma unroll
        for (int i = 0; i < 2; ++i) {
            const int row = w * 16 + i * 8 + (lane >> 3);
            const u32x4 v = *(const LAS u32x4*)(uintptr_t)(lds0 + 32768 + row * 144 + (lane & 7) * 16);
            char* op = (char*)out + (size_t)(rt3 * 128) * 2048 + ct * 128;
            *(u32x4*)(op + row * 2048 + (lane & 7) * 16) = v;
        }
#undef P3_GO
#undef P3_GO4
#undef P3_MMA
#undef P3_GG
#undef P3_RW
#undef P3_K32
#undef P3_OW
#undef P3_STAGE
    }
#undef P3_GW
#undef P3_SOFF
    if (bid == 0 && w == 0) {
        const unsigned long long t0 = __builtin_amdgcn_s_memrealtime();
        for (;;) {
            bool ok = true;
#pragma unroll
            for (int i = 0; i < 4; ++i) ok = ok && (__hip_atomic_load((GAS unsigned*)(fO + i * 64 + lane), __ATOMIC_RELAXED, __HIP_MEMORY_SCOPE_AGENT) == token);
            if (__all(ok)) break;
            if (__builtin_amdgcn_s_memrealtime() - t0 > 400000ull) break;
            __builtin_amdgcn_s_sleep(1);
        }
        if (lane == 0) flag_set(flags, token);
    }
    STAMP_AT(15);
    STAMP_FLUSH(ws, 16);
    if (threadIdx.x == 0 && (blockIdx.x % 37) == 0) {
        printf("WG %3d t0 %llu rel(us*100):", blockIdx.x, st_r[0] % 100000000ull);
        printf(" %lld %lld %lld %lld %lld %lld %lld %lld %lld %lld %lld | cyc %lld %lld %lld\n", st_r[1] - st_r[0], st_r[2] - st_r[0], st_r[3] - st_r[0], st_r[4] - st_r[0], st_r[5] - st_r[0], st_r[9] - st_r[0], st_r[10] - st_r[0], st_r[11] - st_r[0], st_r[12] - st_r[0], st_r[14] - st_r[0], st_r[15] - st_r[0],
               st_c[3] - st_c[2], st_c[11] - st_c[10], st_c[15] - st_c[14]);
    }
}

extern "C" void kernel_launch(void* const* d_in, const int* in_sizes, int n_in, void* d_out, int out_size, void* d_ws, size_t ws_size, hipStream_t stream) {
    (void)in_sizes; (void)n_in; (void)out_size; (void)ws_size;
    const u16* x = (const u16*)d_in[0]; const u16* wqkvA = (const u16*)d_in[1]; const u16* woA = (const u16*)d_in[2];
    const u16* wqkvB = (const u16*)d_in[4]; const u16* woB = (const u16*)d_in[5]; const u16* gw = (const u16*)d_in[6];
    (void)hipFuncSetAttribute((const void*)k_fused, hipFuncAttributeMaxDynamicSharedMemorySize, LDS_BYTES);
    k_fused<<<256, 512, LDS_BYTES, stream>>>(x, wqkvA, wqkvB, woA, woB, gw, (char*)d_ws, (u16*)d_out);
}
```

```cpp
#include <hip/hip_runtime.h>
#include <hip/hip_bf16.h>
#include <stdint.h>
#pragma clang diagnostic ignored "-Winline-asm"

typedef unsigned short u16;
typedef short bf16x8 __attribute__((ext_vector_type(8)));
typedef short s16x4 __attribute__((ext_vector_type(4)));
typedef float f32x16 __attribute__((ext_vector_type(16)));
typedef float f32x4 __attribute__((ext_vector_type(4)));
typedef float f32x2 __attribute__((ext_vector_type(2)));
typedef __bf16 bf16x2_t __attribute__((ext_vector_type(2)));
typedef unsigned u32x4 __attribute__((ext_vector_type(4)));
typedef unsigned u32x2 __attribute__((ext_vector_type(2)));
#define LAS __attribute__((address_space(3)))
#define GAS __attribute__((address_space(1)))
#define DI __device__ __forceinline__
#define MFMA32(a, b, c) __builtin_amdgcn_mfma_f32_32x32x16_bf16((a), (b), (c), 0, 0, 0)
#define MFMA16(a, b, c) __builtin_amdgcn_mfma_f32_16x16x32_bf16((a), (b), (c), 0, 0, 0)
#define SBAR() __builtin_amdgcn_sched_barrier(0)
#define PIN(x) asm volatile("" : "+v"(x))
#define WAITCNT_VM_LGKM0(N) __builtin_amdgcn_s_waitcnt(((N) & 15) | (((N) >> 4) << 14) | 0x70)
#define WAIT_BAR(N) do { asm volatile("" ::: "memory"); WAITCNT_VM_LGKM0(N); __builtin_amdgcn_s_barrier(); asm volatile("" ::: "memory"); } while (0)
#define LBAR() WAIT_BAR(63)
#define NOP_ do {} while (0)

constexpr float QSCALE = 0.17677669529663687f * 1.4426950408889634f;
constexpr size_t WS_KV = 0;
constexpr size_t WS_O = 16u << 20;
constexpr size_t WS_G = 24u << 20;
constexpr size_t WS_F = (24u << 20) + 8192;
constexpr int LDS_BYTES = 163840;

DI unsigned cvtpk(float lo, float hi) { f32x2 v = {lo, hi}; bf16x2_t b = __builtin_convertvector(v, bf16x2_t); return __builtin_bit_cast(unsigned, b); }
DI bf16x8 pack8(const f32x16& x, int s, float sc) {
    u32x4 w = {cvtpk(x[8 * s] * sc, x[8 * s + 1] * sc), cvtpk(x[8 * s + 2] * sc, x[8 * s + 3] * sc), cvtpk(x[8 * s + 4] * sc, x[8 * s + 5] * sc), cvtpk(x[8 * s + 6] * sc, x[8 * s + 7] * sc)};
    return __builtin_bit_cast(bf16x8, w);
}
DI void glds16(unsigned voff, const void* sbase, unsigned lds_dst) {
    asm volatile("s_mov_b32 m0, %2\n\ts_nop 0\n\tglobal_load_lds_dwordx4 %0, %1" ::"v"(voff), "s"(sbase), "s"(lds_dst) : "memory", "m0");
}
DI void glds16c(unsigned voff, const void* sbase, unsigned lds_dst) {
    asm volatile("s_mov_b32 m0, %2\n\ts_nop 0\n\tglobal_load_lds_dwordx4 %0, %1 sc1" ::"v"(voff), "s"(sbase), "s"(lds_dst) : "memory", "m0");
}
DI void glds16m(unsigned voff, const void* sbase, unsigned lds_dst, unsigned mask) {
    asm volatile("s_mov_b32 m0, %2\n\ts_mov_b32 exec_lo, %3\n\ts_mov_b32 exec_hi, %3\n\tglobal_load_lds_dwordx4 %0, %1\n\ts_mov_b64 exec, -1" ::"v"(voff), "s"(sbase), "s"(lds_dst), "s"(mask) : "memory", "m0");
}
DI void st16_wt(unsigned voff, void* sbase, u32x4 v) { asm volatile("global_store_dwordx4 %0, %1, %2 sc1\n\ts_nop 1" ::"v"(voff), "v"(v), "s"(sbase) : "memory"); }
DI void st16_wtm(unsigned voff, void* sbase, u32x4 v, unsigned mask) {
    asm volatile("s_mov_b32 exec_lo, %3\n\ts_mov_b32 exec_hi, %3\n\tglobal_store_dwordx4 %0, %1, %2 sc1\n\ts_mov_b64 exec, -1\n\ts_nop 0" ::"v"(voff), "v"(v), "s"(sbase), "s"(mask) : "memory");
}
typedef short v4i16_t __attribute__((ext_vector_type(4)));
DI s16x4 ldtr(unsigned a) { return __builtin_bit_cast(s16x4, __builtin_amdgcn_ds_read_tr16_b64_v4i16((LAS v4i16_t*)(uintptr_t)a)); }
DI bf16x8 ld128(unsigned a) { return *(const LAS bf16x8*)(uintptr_t)a; }
DI void st128(unsigned a, u32x4 v) { *(LAS u32x4*)(uintptr_t)a = v; }
DI bf16x8 cat8(s16x4 lo, s16x4 hi) { return __builtin_shufflevector(lo, hi, 0, 1, 2, 3, 4, 5, 6, 7); }

DI void flag_set(unsigned* f, unsigned v) { __hip_atomic_store((GAS unsigned*)f, v, __ATOMIC_RELAXED, __HIP_MEMORY_SCOPE_AGENT); }
DI void poll_spin(const unsigned* f, int n, unsigned want, int lane) {
    const unsigned long long t0 = __builtin_amdgcn_s_memrealtime();
    for (;;) {
        unsigned v = want;
        if (lane < n) v = __hip_atomic_load((GAS unsigned*)(f + lane), __ATOMIC_RELAXED, __HIP_MEMORY_SCOPE_AGENT);
        if (__all(v == want)) break;
        if (__builtin_amdgcn_s_memrealtime() - t0 > 400000ull) break;
        __builtin_amdgcn_s_sleep(1);
    }
    __builtin_amdgcn_fence(__ATOMIC_ACQUIRE, "wavefront");
}
#define POLL_ISSUE(pv, f, n) do { const unsigned* p_ = (f) + (lane < (n) ? lane : 0); asm volatile("global_load_dword %0, %1, off sc1" : "=v"(pv) : "v"(p_) : "memory"); } while (0)
#define POLL_CHECK(pv, f, n) do { asm volatile("s_waitcnt vmcnt(0)" : "+v"(pv)::"memory"); if (!__all((pv) == token)) poll_spin(f, n, token, lane); } while (0)

DI float xhalf_max(float m) { auto rr = __builtin_amdgcn_permlane32_swap(__float_as_uint(m), __float_as_uint(m), false, false); return fmaxf(__uint_as_float(rr[0]), __uint_as_float(rr[1])); }
DI float xhalf_sum(float m) { auto rr = __builtin_amdgcn_permlane32_swap(__float_as_uint(m), __float_as_uint(m), false, false); return __uint_as_float(rr[0]) + __uint_as_float(rr[1]); }
DI int keyof(int r, int h) { return (r & 3) + 8 * (r >> 2) + 4 * h; }
DI float rowmax16(const f32x16& s) {
    float a = fmaxf(fmaxf(s[0], s[1]), s[2]), b = fmaxf(fmaxf(s[3], s[4]), s[5]);
#pragma unroll
    for (int r = 6; r < 16; r += 2) a = fmaxf(fmaxf(a, s[r]), s[r + 1]);
    return xhalf_max(fmaxf(a, b));
}
DI bf16x8 packp(const f32x16& p, int s) {
    u32x4 w = {cvtpk(p[8 * s], p[8 * s + 1]), cvtpk(p[8 * s + 2], p[8 * s + 3]), cvtpk(p[8 * s + 4], p[8 * s + 5]), cvtpk(p[8 * s + 6], p[8 * s + 7])};
    return __builtin_bit_cast(bf16x8, w);
}
template <bool WIN>
DI void attn_safe(const char* kv, bf16x8 q0, bf16x8 q1, int rb, int lane, f32x16& Oout, float& lout) {
    const int ql = lane & 31, h = lane >> 5;
    __builtin_amdgcn_fence(__ATOMIC_ACQUIRE, "agent");
    asm volatile("s_waitcnt vmcnt(0)" ::: "memory");
    float m = -INFINITY, l = 0.f; f32x16 O = {};
    const int nb = WIN ? (rb < 8 ? rb + 1 : 9) : 64;
    for (int j = 0; j < nb; ++j) {
        const int kb = WIN ? rb - j : j;
        const char* kp = kv + (kb >> 3) * 32768 + (kb & 7) * 2048 + lane * 16;
        const bf16x8 k0 = *(const bf16x8*)kp, k1 = *(const bf16x8*)(kp + 1024), v0 = *(const bf16x8*)(kp + 16384), v1 = *(const bf16x8*)(kp + 16384 + 1024);
        f32x16 s = {}; s = MFMA32(k0, q0, s); s = MFMA32(k1, q1, s);
        if (WIN) {
#pragma unroll
            for (int r = 0; r < 16; ++r) { const int kl = keyof(r, h); if ((j == 0 && kl > ql) || (j == 8 && kl <= ql)) s[r] = -INFINITY; }
        }
        const float mn = fmaxf(m, rowmax16(s));
        const float al = __builtin_amdgcn_exp2f(m - mn); m = mn;
        float ps = 0.f;
#pragma unroll
        for (int r = 0; r < 16; ++r) { s[r] = __builtin_amdgcn_exp2f(s[r] - mn); }
        const bf16x8 p0 = packp(s, 0), p1 = packp(s, 1);
#pragma unroll
        for (int r = 0; r < 8; ++r) { ps += __uint_as_float((unsigned)(u16)p0[r] << 16) + __uint_as_float((unsigned)(u16)p1[r] << 16); }
        l = l * al + ps;
#pragma unroll
        for (int r = 0; r < 16; ++r) O[r] *= al;
        O = MFMA32(v0, p0, O); O = MFMA32(v1, p1, O);
    }
    Oout = O; lout = xhalf_sum(l);
}
DI bool unit_ok(const f32x16& O, float l) {
    bool ok = l <= 1.0995116e12f;
    float a = 0.f;
#pragma unroll
    for (int r = 0; r < 16; ++r) a = fmaxf(a, __builtin_fabsf(O[r]));
    bool okO = a <= 3.0e38f;
#pragma unroll
    for (int r = 0; r < 16; ++r) okO = okO && (O[r] == O[r]);
    return __all(ok && okO);
}

constexpr int P1_XSLOT = 32768, P1_WBASE = 3 * P1_XSLOT, P1_WSLOT = 12288;
constexpr int P2_AK = 0, P2_AV = 32768, P2_B = 65536, P2_SLOT = 32768;
constexpr int P2_Q = P2_B + 2 * P2_SLOT;
constexpr int P3_SLOT = 16384;

__global__ __launch_bounds__(512, 2) void k_fused(const u16* __restrict__ x, const u16* __restrict__ wA, const u16* __restrict__ wB, const u16* __restrict__ woA,
                                                  const u16* __restrict__ woB, const u16* __restrict__ gw, char* __restrict__ ws, u16* __restrict__ out) {
    extern __shared__ __attribute__((aligned(16))) char lds[];
    const unsigned lds0 = (unsigned)(uintptr_t)(LAS char*)lds;
    const int tid = threadIdx.x, lane = tid & 63;
    const int w = __builtin_amdgcn_readfirstlane(tid >> 6);
    const int bid = blockIdx.x, xcd = bid & 7, idx = bid >> 3;
    const int head = xcd * 4 + (idx >> 3), rt = idx & 7;
    const int wm = (w & 1) | ((w >> 2) << 1), wn = (w >> 1) & 1, qw = 2 * wm + wn;
    unsigned* flags = (unsigned*)(ws + WS_F);
    const unsigned l16 = lane * 16;
    float gate;
    unsigned token;

    f32x16 acc[2][3];
    {
        const char* xg = (const char*)x + (size_t)rt * 256 * 2048;
        const bool w0 = w == 0, w7 = w == 7;
        const int whalf = w >= 4;
        unsigned vo[4], ve[4];
        const char* ebA; const char* ebB; unsigned estride, edst;
        {
            const int r8 = lane >> 3, c8 = lane & 7;
            const int sc = (lane & 3) ^ (((lane >> 5) & 1) << 1);
#pragma unroll
            for (int j = 0; j < 4; ++j) {
                const int p = 4 * (w7 ? 0 : w) + j;
                vo[j] = (unsigned)((8 * p + r8) * 2048 + ((c8 ^ ((4 * p + (lane >> 4)) & 7)) << 4));
                const int pe = 28 + j;
                const unsigned vxe = (unsigned)((8 * pe + r8) * 2048 + ((c8 ^ ((4 * pe + (lane >> 4)) & 7)) << 4));
                const int i = 4 * ((w7 || w0 ? 1 : w) - 1) + j, ii = i >= 12 ? i - 12 : i, seg = ii >> 1, rg = ii & 1, type = seg >= 3 ? seg - 3 : seg;
                const unsigned vwe = (unsigned)((whalf * 32 + rg * 16 + (lane >> 2)) * 6144 + type * 2048 + (sc << 4));
                ve[j] = w0 ? vxe : vwe;
            }
            const int i0 = 4 * ((w7 || w0 ? 1 : w) - 1), ii0 = i0 >= 12 ? i0 - 12 : i0;
            ebA = w0 ? xg : (const char*)(ii0 >= 6 ? wB : wA) + head * 64;
            ebB = w0 ? xg : (const char*)(ii0 + 2 >= 6 ? wB : wA) + head * 64;
            estride = w0 ? 128u : 64u * 6144u;
            edst = w0 ? 28 * 1024 : P1_WBASE + ii0 * 1024;
        }
        const unsigned odst = lds0 + (w7 ? 0 : w) * 4096;
        const unsigned mstage = __builtin_amdgcn_readfirstlane(w7 ? 0u : ~0u), mwarm = ~mstage;
        const char* pfw = (const char*)(bid < 128 ? wA : wB);
        unsigned pfo;
        { const int L = (bid & 127) * 24 + (lane < 24 ? lane : 0); pfo = (unsigned)((L / 48) * 6144 + (L % 48) * 128); }
#define P1_PF(base, off) asm volatile("s_mov_b32 m0, %3\n\ts_mov_b32 exec_lo, %2\n\ts_mov_b32 exec_hi, %2\n\tglobal_load_lds_dword %0, %1\n\ts_mov_b64 exec, -1" ::"v"(off), "s"(base), "s"(mwarm), "s"(lds0 + P1_WBASE + 5 * P1_WSLOT) : "memory", "m0")
#define P1_GO(og_, od_, j) glds16m(vo[j], og_, (od_) + (j) * 1024, mstage)
#define P1_GE(eA_, eB_, ed_, j) glds16m(ve[j], (j) < 2 ? (eA_) : (eB_), (ed_) + (j) * 1024, mstage)
#define P1_GO2(og_, od_, j) do { if (HF_ == 0) glds16(vo[j], og_, (od_) + (j) * 1024); else P1_GO(og_, od_, j); } while (0)
#define P1_GE2(eA_, eB_, ed_, j) do { if (HF_ == 0) glds16(ve[j], (j) < 2 ? (eA_) : (eB_), (ed_) + (j) * 1024); else P1_GE(eA_, eB_, ed_, j); } while (0)
#define P1_EDST(xo_, h0, h1) (lds0 + edst + (w0 ? (xo_) : (whalf ? (h1) : (h0))))
#pragma unroll
        for (int j = 0; j < 4; ++j) P1_GO(xg, odst, j);
        { const unsigned ed = P1_EDST(0, 0, P1_WSLOT);
#pragma unroll
          for (int j = 0; j < 4; ++j) P1_GE(ebA, ebB, ed, j); }
        u32x4 gx0, gx1, gw0, gw1;
        {
            const char* px = (const char*)x + (size_t)(rt * 256 + head * 8 + w) * 2048;
            const unsigned go = lane * 32, z = 0;
            asm volatile("global_load_dwordx4 %0, %5, %6\n\tglobal_load_dwordx4 %1, %5, %6 offset:16\n\tglobal_load_dwordx4 %2, %5, %7\n\tglobal_load_dwordx4 %3, %5, %7 offset:16\n\t"
                         "global_load_dword %4, %8, %9 sc1"
                         : "=&v"(gx0), "=&v"(gx1), "=&v"(gw0), "=&v"(gw1), "=&v"(token) : "v"(go), "s"(px), "s"(gw), "v"(z), "s"(flags) : "memory");
        }
#pragma unroll
        for (int j = 0; j < 4; ++j) P1_GO(xg + 128, odst + P1_XSLOT, j);
        { const unsigned ed = P1_EDST(P1_XSLOT, 2 * P1_WSLOT, 3 * P1_WSLOT);
#pragma unroll
          for (int j = 0; j < 4; ++j) P1_GE(ebA + estride, ebB + estride, ed, j); }
        ebA += 2 * (size_t)estride; ebB += 2 * (size_t)estride;
        const int hh1 = lane >> 5;
        unsigned xa[4], wa;
#pragma unroll
        for (int u = 0; u < 4; ++u) xa[u] = lds0 + (wm * 64 + (lane & 31)) * 128 + (((2 * u + hh1) ^ ((lane >> 1) & 7)) << 4);
        wa = lds0 + P1_WBASE + wn * 6144 + (8 * hh1 + ((lane & 15) >> 2)) * 64 + ((((lane >> 4) & 1) ^ hh1) * 32) + (lane & 3) * 8;
#pragma unroll
        for (int i = 0; i < 2; ++i)
#pragma unroll
            for (int j = 0; j < 3; ++j) acc[i][j] = f32x16{};
        bf16x8 xf[2][2][2]; s16x4 wl[2][2][3], wh[2][2][3];
#define P1_WF(b, k, t) cat8(wl[b][k][t], wh[b][k][t])
#define P1_MM(b, k, r, t) acc[r][t] = ((t) < 2) ? MFMA32(P1_WF(b, k, t), xf[b][k][r], acc[r][t]) : MFMA32(xf[b][k][r], P1_WF(b, k, t), acc[r][t])
#define P1_RW(b, wa_, k, t) do { wl[b][k][t] = ldtr((wa_) + (t) * 2048 + (k) * 1024); wh[b][k][t] = ldtr((wa_) + (t) * 2048 + (k) * 1024 + 256); } while (0)
#define P1_STEP(HF, c, n, xo, u, wo, RD, G0, G1, G2, G3) do { \
            const unsigned xa0_ = xa[2 * (u)] + (xo), xa1_ = xa[2 * (u) + 1] + (xo), wa_ = wa + (wo); \
            P1_MM(c, 0, 0, 0); if (RD) { xf[n][0][0] = ld128(xa0_); P1_RW(n, wa_, 0, 0); } if (HF == 0) { G0; } SBAR(); \
            P1_MM(c, 0, 0, 1); if (RD) { P1_RW(n, wa_, 0, 1); } SBAR(); \
            P1_MM(c, 0, 0, 2); if (RD) { P1_RW(n, wa_, 0, 2); } if (HF == 1) { G0; } SBAR(); \
            P1_MM(c, 0, 1, 0); if (RD) { xf[n][0][1] = ld128(xa0_ + 4096); } if (HF == 0) { G1; } SBAR(); \
            P1_MM(c, 0, 1, 1); if (RD) { xf[n][1][0] = ld128(xa1_); } SBAR(); \
            P1_MM(c, 0, 1, 2); if (RD) { P1_RW(n, wa_, 1, 0); } if (HF == 1) { G1; } SBAR(); \
            P1_MM(c, 1, 0, 0); if (RD) { P1_RW(n, wa_, 1, 1); } if (HF == 0) { G2; } SBAR(); \
            P1_MM(c, 1, 0, 1); if (RD) { P1_RW(n, wa_, 1, 2); } SBAR(); \
            P1_MM(c, 1, 0, 2); if (RD) { xf[n][1][1] = ld128(xa1_ + 4096); } if (HF == 1) { G2; } SBAR(); \
            P1_MM(c, 1, 1, 0); if (HF == 0) { G3; } SBAR(); \
            P1_MM(c, 1, 1, 1); if (HF == 1) { G3; } SBAR(); \
            P1_MM(c, 1, 1, 2); SBAR(); } while (0)
#define P1_GATE() do {     \
            asm volatile("" : "+v"(gx0), "+v"(gx1), "+v"(gw0), "+v"(gw1), "+v"(token)); \
            float s_ = 0.f; \
            _Pragma("unroll") for (int i = 0; i < 4; ++i) { \
                s_ += __uint_as_float(gx0[i] << 16) * __uint_as_float(gw0[i] << 16) + __uint_as_float(gx0[i] & 0xffff0000u) * __uint_as_float(gw0[i] & 0xffff0000u); \
                s_ += __uint_as_float(gx1[i] << 16) * __uint_as_float(gw1[i] << 16) + __uint_as_float(gx1[i] & 0xffff0000u) * __uint_as_float(gw1[i] & 0xffff0000u); } \
            gate = s_; PIN(gate); \
            token = __builtin_amdgcn_readfirstlane(token) + 1u; } while (0)
        if (w7) asm volatile("s_waitcnt vmcnt(0)" ::: "memory");
        WAIT_BAR(8);
#pragma unroll
        for (int j = 0; j < 4; ++j) P1_GO(xg + 256, odst + 2 * P1_XSLOT, j);
        P1_GATE();
        if (w7) {
            P1_PF(pfw + 2 * (64 * 6144), pfo); P1_PF(pfw + 3 * (64 * 6144), pfo);
            const char* xl = (const char*)x + (size_t)bid * 16384; const unsigned lo = lane * 128;
            P1_PF(xl, lo); P1_PF(xl + 8192, lo);
            P1_PF(pfw + 4 * (64 * 6144), pfo); P1_PF(pfw + 5 * (64 * 6144), pfo);
        }
        { xf[0][0][0] = ld128(xa[0]); xf[0][0][1] = ld128(xa[0] + 4096); xf[0][1][0] = ld128(xa[1]); xf[0][1][1] = ld128(xa[1] + 4096);
#pragma unroll
          for (int n = 0; n < 6; ++n) P1_RW(0, wa, n / 3, n % 3); }
        LBAR();
        unsigned xo0 = 0, xo1 = P1_XSLOT, xo2 = 2 * P1_XSLOT;
        unsigned wo0 = 0, wo1 = P1_WSLOT, wo2 = 2 * P1_WSLOT, wo3 = 3 * P1_WSLOT, wo4 = 4 * P1_WSLOT;
#define P1_TILE(HF, T, IE, IO, WA, WB, RDLAST, PFX) do { constexpr int HF_ = HF; \
            const unsigned ed_ = P1_EDST(xo2, wo4, wo0), od_ = odst + xo0; const char* og_ = xg + ((T) + 3) * 128; \
            P1_STEP(HF, 0, 1, xo0, 1, wo1, 1, if (IE) P1_GE2(ebA, ebB, ed_, 0), if (IE) P1_GE2(ebA, ebB, ed_, 1), if (IE) P1_GE2(ebA, ebB, ed_, 2), if (IE) P1_GE2(ebA, ebB, ed_, 3)); \
            WA; \
            PFX; \
            P1_STEP(HF, 1, 0, xo1, 0, wo2, RDLAST, if (IO) P1_GO2(og_, od_, 0), if (IO) P1_GO2(og_, od_, 1), if (IO) P1_GO2(og_, od_, 2), if (IO) P1_GO2(og_, od_, 3)); \
            WB; \
            ebA += estride; ebB += estride; \
            { const unsigned t_ = xo0; xo0 = xo1; xo1 = xo2; xo2 = t_; const unsigned a_ = wo0, b_ = wo1; wo0 = wo2; wo1 = wo3; wo2 = wo4; wo3 = a_; wo4 = b_; } } while (0)
#define P1_LOOP(HF) do { \
            for (int T = 0; T < 13; ++T) { \
                  \
                const char* pb = T < 10 ? pfw + (size_t)(T + 6) * (64 * 6144) : (const char*)(bid < 128 ? woA : woB) + (bid & 127) * 16384 + (T & 1) * 8192; \
                const unsigned po = T < 10 ? pfo : lane * 128; \
                P1_TILE(HF, T, 1, 1, WAIT_BAR(8), WAIT_BAR(12), 1, if (HF == 1) P1_PF(pb, po)); \
            } \
            P1_TILE(HF, 13, 1, 0, WAIT_BAR(8), WAIT_BAR(8), 1, NOP_); \
            P1_TILE(HF, 14, 0, 0, WAIT_BAR(0), NOP_, 1, NOP_); \
            P1_TILE(HF, 15, 0, 0, NOP_, NOP_, 0, NOP_); } while (0)
        if (w < 4) P1_LOOP(0); else P1_LOOP(1);
        asm volatile("s_waitcnt vmcnt(0)" ::: "memory");
#undef P1_LOOP
#undef P1_GATE
#undef P1_TILE
#undef P1_STEP
#undef P1_RW
#undef P1_MM
#undef P1_WF
#undef P1_GO
#undef P1_GE
#undef P1_EDST
#undef P1_PF
    }
#pragma unroll
    for (int o = 32; o >= 1; o >>= 1) gate += __shfl_xor(gate, o);
    gate = 1.0f / (1.0f + __expf(-gate));
    u32x4 keep[8];
    bf16x8 qown0, qown1;
    const unsigned mB = __builtin_amdgcn_readfirstlane(wn ? ~0u : 0u), mA = ~mB;
    char* kvg = ws + WS_KV + (size_t)((wn * 32 + head) * 8 + rt) * 32768;
    const unsigned kvo = wm * 4096 + l16;
    {
#pragma unroll
        for (int r = 0; r < 2; ++r)
#pragma unroll
            for (int s = 0; s < 2; ++s) {
                const unsigned o = kvo + r * 2048 + s * 1024;
                keep[r * 2 + s] = __builtin_bit_cast(u32x4, pack8(acc[r][1], s, 1.0f)); keep[4 + r * 2 + s] = __builtin_bit_cast(u32x4, pack8(acc[r][2], s, 1.0f));
                st16_wtm(o, kvg, keep[r * 2 + s], mB); st16_wtm(16384 + o, kvg, keep[4 + r * 2 + s], mB);
            }
        const f32x16 qk = wn ? acc[1][0] : acc[0][0], qs = wn ? acc[0][0] : acc[1][0];
        qown0 = pack8(qk, 0, QSCALE); qown1 = pack8(qk, 1, QSCALE);
        st128(lds0 + P2_Q + w * 2048 + l16, __builtin_bit_cast(u32x4, pack8(qs, 0, QSCALE))); st128(lds0 + P2_Q + w * 2048 + 1024 + l16, __builtin_bit_cast(u32x4, pack8(qs, 1, QSCALE)));
        if (wn) {
#pragma unroll
            for (int i = 0; i < 4; ++i) { const unsigned o = kvo + (i >> 1) * 2048 + (i & 1) * 1024; st128(lds0 + P2_B + o, keep[i]); st128(lds0 + P2_B + 16384 + o, keep[4 + i]); }
        }
    }
    WAIT_BAR(0);

    const int rb = rt * 8 + qw;
    const int ql = lane & 31, hh = lane >> 5;
    const char* kvA = ws + WS_KV + (size_t)(head * 8) * 32768;
    const char* kvB = ws + WS_KV + (size_t)((32 + head) * 8) * 32768;
    unsigned* fKB = flags + 64 + head * 8; unsigned* fO = flags + 64 + 256; unsigned* fKA = flags + 64 + 512 + head * 8; unsigned* fOB = flags + 64 + 768;
    const int rtp = rt > 0 ? rt - 1 : 0;
    const unsigned* fKAp = fKA + rtp;
    if (tid == 0) flag_set(fKB + rt, token);
    bf16x8 sel;
    { const short o = (((lane >> 4) & 1) == ((lane >> 2) & 1)) ? (short)0x3F80 : (short)0;
#pragma unroll
      for (int i = 0; i < 8; ++i) sel[i] = o; }
    const bf16x8 qx0 = ld128(lds0 + P2_Q + (w ^ 2) * 2048 + l16), qx1 = ld128(lds0 + P2_Q + (w ^ 2) * 2048 + 1024 + l16);
    const bf16x8 qa0 = wn ? qx0 : qown0, qa1 = wn ? qx1 : qown1, qb0 = wn ? qown0 : qx0, qb1 = wn ? qown1 : qx1;
    char* og = ws + WS_O + ((size_t)rb * 64 + head) * 2048;
    const unsigned oo = (ql >> 4) * 1024 + (hh * 16 + (ql & 15)) * 16;
    const int rt3 = (xcd & 3) * 4 + (idx & 3), ct = (xcd >> 2) * 8 + (idx >> 2);
    const int p3 = w & 3, half = w >> 2;
    const char* wsrcB = (const char*)woB + ct * 128 + (size_t)half * 512 * 2048; const char* wsrcA = (const char*)woA + ct * 128 + (size_t)half * 512 * 2048;
    const unsigned vw0 = (unsigned)((16 * p3 + (lane >> 3)) * 2048 + (((lane & 7) ^ (((lane >> 4) & 3) << 1)) << 4)), vw1 = vw0 + 8 * 2048;
    const unsigned wdst3 = lds0 + half * 8192 + p3 * 2048;
#define P3_SOFF(s) ((s) < 6 ? 65536 + (s) * P3_SLOT : (s) < 10 ? ((s) - 6) * P3_SLOT : 65536 + ((s) - 10) * P3_SLOT)
#define P3_GW(st, i) glds16((i) ? vw1 : vw0, ((st) < 8 ? wsrcB : wsrcA) + (size_t)((st) & 7) * (64 * 2048), wdst3 + P3_SOFF(st) + (i) * 1024)

#define EX(x) __builtin_amdgcn_exp2f(x)
#define P8(x) __builtin_bit_cast(bf16x8, x)
#define MF(x) do { x; SBAR(); } while (0)
#define STEP(SC, SN, Q0, Q1, AK0, AK1, AV1, AV0, X) do { \
        MF(SN = MFMA32(k0, Q0, negm)); \
        t0 = EX(SC[0]); phi[2] = cvtpk(u0, u1); t1 = EX(SC[1]); phi[3] = cvtpk(u2, u3); k0 = ld128(AK0); PIN(phi); PIN(t0); PIN(t1); SBAR(); \
        t2 = EX(SC[2]); PIN(t2); SBAR(); \
        MF(O = MFMA32(v1, P8(phi), O)); \
        t3 = EX(SC[3]); u0 = EX(SC[4]); v1 = ld128(AV1); plo[0] = cvtpk(t0, t1); PIN(t3); PIN(u0); PIN(plo); SBAR(); \
        MF(L = MFMA16(sel, P8(phi), L)); \
        u1 = EX(SC[5]); plo[1] = cvtpk(t2, t3); PIN(plo); PIN(u1); SBAR(); \
        MF(SN = MFMA32(k1, Q1, SN)); \
        u2 = EX(SC[6]); u3 = EX(SC[7]); k1 = ld128(AK1); plo[2] = cvtpk(u0, u1); PIN(u2); PIN(u3); PIN(plo); SBAR(); \
        X; SBAR(); \
        t0 = EX(SC[8]); plo[3] = cvtpk(u2, u3); t1 = EX(SC[9]); t2 = EX(SC[10]); PIN(plo); PIN(t0); PIN(t1); PIN(t2); SBAR(); \
        MF(O = MFMA32(v0, P8(plo), O)); \
        t3 = EX(SC[11]); u0 = EX(SC[12]); v0 = ld128(AV0); phi[0] = cvtpk(t0, t1); PIN(t3); PIN(u0); PIN(phi); SBAR(); \
        MF(L = MFMA16(sel, P8(plo), L)); \
        u1 = EX(SC[13]); phi[1] = cvtpk(t2, t3); u2 = EX(SC[14]); u3 = EX(SC[15]); PIN(phi); PIN(u1); PIN(u2); PIN(u3); SBAR(); \
    } while (0)
#define SG_OFF(i) (((i) >> 1) * 16384 + ((i) & 1) * 1024)
#define SG_LD(i, src, dK, dV) glds16c(l16, (src) + w * 2048 + SG_OFF(i), ((i) >> 1 ? (dV) : (dK)) + w * 2048 + ((i) & 1) * 1024)
    {
        f32x16 O = {}, negm, SA, SB; f32x4 L = {};
        bf16x8 k0, k1, v0, v1; u32x4 plo, phi; float t0, t1, t2, t3, u0 = 0.f, u1 = 0.f, u2 = 0.f, u3 = 0.f;
        unsigned cur = lds0 + P2_B + l16, nxt = cur + P2_SLOT;
        {
            k0 = ld128(cur); k1 = ld128(cur + 1024);
            SA = f32x16{}; SA = MFMA32(k0, qb0, SA); SA = MFMA32(k1, qb1, SA);
            const float m = rowmax16(SA);
#pragma unroll
            for (int r = 0; r < 16; ++r) { negm[r] = -m; SA[r] -= m; }
            k0 = ld128(cur + 2048); k1 = ld128(cur + 2048 + 1024);
            v0 = ld128(cur + 16384);
            v1 = bf16x8{}; phi = u32x4{}; plo = u32x4{};
        }
#define KAD(j, s) (((j) < 8 ? cur + (j) * 2048 : nxt + ((j) - 8) * 2048) + (s) * 1024)
#define BSTEP(SC, SN, j, X) STEP(SC, SN, qb0, qb1, KAD((j) + 2, 0), KAD((j) + 2, 1), KAD(j, 1) + 16384, KAD((j) + 1, 0) + 16384, X)
#define KEEP_A(i) st16_wtm(kvo + ((i) >> 2) * 16384 + (((i) >> 1) & 1) * 2048 + ((i) & 1) * 1024, kvg, keep[i], mA)
        unsigned pv;
        BSTEP(SA, SB, 0, POLL_ISSUE(pv, fKB, 8));
        { const char* s1 = kvB + ((rt + 1) & 7) * 32768; const unsigned d = lds0 + P2_B + P2_SLOT;
          BSTEP(SB, SA, 1, POLL_CHECK(pv, fKB, 8); SG_LD(0, s1, d, d + 16384); SG_LD(1, s1, d, d + 16384); SG_LD(2, s1, d, d + 16384); SG_LD(3, s1, d, d + 16384);
                           KEEP_A(0); KEEP_A(1); KEEP_A(2); KEEP_A(3)); }
        BSTEP(SA, SB, 2, KEEP_A(4); KEEP_A(5); KEEP_A(6); KEEP_A(7));
        BSTEP(SB, SA, 3, NOP_);
        BSTEP(SA, SB, 4, NOP_);
        BSTEP(SB, SA, 5, NOP_);
        WAIT_BAR(0);
        if (tid == 0) flag_set(fKA + rt, token);
        const char* aown = kvA + rt * 32768;
        int slot = 0;
        for (int i = 0; i < 7; ++i) {
            const int s1 = slot == 2 ? 0 : slot + 1, s2 = s1 == 2 ? 0 : s1 + 1;
            const char* src = i < 6 ? kvB + ((rt + i + 2) & 7) * 32768 : kvA + rtp * 32768;
            const unsigned dK = i < 6 ? lds0 + P2_B + s2 * P2_SLOT : lds0 + P2_AK, dV = i < 6 ? dK + 16384 : lds0 + P2_AV;
            if (i == 6) POLL_CHECK(pv, fKAp, 1);
            BSTEP(SA, SB, 6, SG_LD(0, src, dK, dV)); BSTEP(SB, SA, 7, SG_LD(1, src, dK, dV));
            cur = nxt; nxt = lds0 + P2_B + s2 * P2_SLOT + l16;
            BSTEP(SA, SB, 0, SG_LD(2, src, dK, dV)); BSTEP(SB, SA, 1, SG_LD(3, src, dK, dV));
            BSTEP(SA, SB, 2, if (i == 0) { SG_LD(0, aown, lds0 + P2_AK + 16384, lds0 + P2_AV + 16384); SG_LD(1, aown, lds0 + P2_AK + 16384, lds0 + P2_AV + 16384); });
            BSTEP(SB, SA, 3, if (i == 0) { SG_LD(2, aown, lds0 + P2_AK + 16384, lds0 + P2_AV + 16384); SG_LD(3, aown, lds0 + P2_AK + 16384, lds0 + P2_AV + 16384); });
            BSTEP(SA, SB, 4, if (i == 5) { POLL_ISSUE(pv, fKAp, 1); });
            BSTEP(SB, SA, 5, NOP_);
            WAIT_BAR(0);
            slot = s1;
        }
        BSTEP(SA, SB, 6, NOP_); BSTEP(SB, SA, 7, NOP_);
        phi[2] = cvtpk(u0, u1); phi[3] = cvtpk(u2, u3);
        O = MFMA32(v1, P8(phi), O); L = MFMA16(sel, P8(phi), L);
        float l = L[0];
        if (!unit_ok(O, l)) attn_safe<false>(kvB, qb0, qb1, rb, lane, O, l);
        const float f = 1.0f / l;
        st16_wt(oo, og + 32 * 2048, __builtin_bit_cast(u32x4, pack8(O, 0, f))); st16_wt(oo + 512, og + 32 * 2048, __builtin_bit_cast(u32x4, pack8(O, 1, f)));
#undef KEEP_A
#undef BSTEP
#undef KAD
    }
    LBAR();
    {
        f32x16 O = {}, negm, SA, SB; f32x4 L = {};
        bf16x8 k0, k1, v0, v1; u32x4 plo, phi; float t0, t1, t2, t3, u0 = 0.f, u1 = 0.f, u2 = 0.f, u3 = 0.f;
        const unsigned ab = lds0 + P2_AK + qw * 2048 + l16;
        const int jmax = rt > 0 ? 8 : qw;
#define AAD(j, s) (ab + (8 - ((j) > 8 ? 8 : (j))) * 2048 + (s) * 1024)
        {
            k0 = ld128(AAD(0, 0)); k1 = ld128(AAD(0, 1));
            SA = f32x16{}; SA = MFMA32(k0, qa0, SA); SA = MFMA32(k1, qa1, SA);
#pragma unroll
            for (int r = 0; r < 16; ++r) if (keyof(r, hh) > ql) SA[r] = -INFINITY;
            const float m = rowmax16(SA);
#pragma unroll
            for (int r = 0; r < 16; ++r) { negm[r] = -m; SA[r] -= m; }
            k0 = ld128(AAD(1, 0)); k1 = ld128(AAD(1, 1));
            v0 = ld128(AAD(0, 0) + 32768);
            v1 = bf16x8{}; phi = u32x4{}; plo = u32x4{};
        }
#define ASTEP(SC, SN, j, X) do { if ((j) + 1 > jmax) { _Pragma("unroll") for (int r_ = 0; r_ < 16; ++r_) negm[r_] = -INFINITY; } \
        STEP(SC, SN, qa0, qa1, AAD((j) + 2, 0), AAD((j) + 2, 1), AAD(j, 1) + 32768, AAD((j) + 1, 0) + 32768, X); } while (0)
        ASTEP(SA, SB, 0, P3_GW(0, 0); P3_GW(0, 1)); ASTEP(SB, SA, 1, P3_GW(1, 0); P3_GW(1, 1)); ASTEP(SA, SB, 2, P3_GW(2, 0); P3_GW(2, 1)); ASTEP(SB, SA, 3, P3_GW(3, 0); P3_GW(3, 1));
        ASTEP(SA, SB, 4, P3_GW(4, 0); P3_GW(4, 1));
        WAIT_BAR(10);
        if (tid == 0) flag_set(fOB + rt * 32 + head, token);
        ASTEP(SB, SA, 5, P3_GW(5, 0); P3_GW(5, 1)); ASTEP(SA, SB, 6, NOP_); ASTEP(SB, SA, 7, NOP_);
#pragma unroll
        for (int r = 0; r < 16; ++r) if (keyof(r, hh) <= ql) SA[r] = -INFINITY;
        ASTEP(SA, SB, 8, NOP_);
        phi[2] = cvtpk(u0, u1); phi[3] = cvtpk(u2, u3);
        O = MFMA32(v1, P8(phi), O); L = MFMA16(sel, P8(phi), L);
        float l = L[0];
        if (!unit_ok(O, l)) attn_safe<true>(kvA, qa0, qa1, rb, lane, O, l);
        const float f = 1.0f / l;
        st16_wt(oo, og, __builtin_bit_cast(u32x4, pack8(O, 0, f))); st16_wt(oo + 512, og, __builtin_bit_cast(u32x4, pack8(O, 1, f)));
        if (lane == 0) __hip_atomic_store((GAS float*)(ws + WS_G) + rt * 256 + head * 8 + w, gate, __ATOMIC_RELAXED, __HIP_MEMORY_SCOPE_AGENT);
#undef ASTEP
#undef AAD
    }
#undef STEP
#undef MF
#undef EX
#undef P8
#undef SG_LD
#undef SG_OFF
    LBAR();

    {
        {
            unsigned pv0; const unsigned* fOBq = fOB + (rt3 >> 1) * 32;
            POLL_ISSUE(pv0, fOBq, 32);
#pragma unroll
            for (int st = 6; st < 10; ++st) { P3_GW(st, 0); P3_GW(st, 1); }
            asm volatile("s_waitcnt vmcnt(8)" : "+v"(pv0)::"memory");
            if (!__all(pv0 == token)) poll_spin(fOBq, 32, token, lane);
        }
        LBAR();
        if (tid == 0) flag_set(fO + rt * 32 + head, token);
        const char* osrc = ws + WS_O + ((size_t)(rt3 * 4 + p3) * 64 + half * 16) * 2048;
        bf16x8 of[7][4];
#define P3_GO(st, i) asm volatile("global_load_dwordx4 %0, %1, %2 offset:" #i "*1024 sc1" : "=v"(of[(st) % 7][i]) : "v"(l16), "s"(osrc + ((st) < 8 ? 65536 : 0) + ((st) & 7) * 4096) : "memory")
#define P3_GO4(st) do { P3_GO(st, 0); P3_GO(st, 1); P3_GO(st, 2); P3_GO(st, 3); } while (0)
        P3_GO4(0); P3_GO4(1); P3_GO4(2);
        float gq[2];
#define P3_GG() do { const float* gp_ = (const float*)(ws + WS_G) + rt3 * 128 + p3 * 32; const unsigned go_ = (lane & 15) * 4; \
          asm volatile("global_load_dword %0, %2, %3 sc1\n\tglobal_load_dword %1, %2, %3 offset:64 sc1" : "=&v"(gq[0]), "=&v"(gq[1]) : "v"(go_), "s"(gp_) : "memory"); } while (0)
        const int g4 = lane >> 4, q4 = (lane & 15) >> 2;
        unsigned wa[4];
        { const unsigned a0 = (16 * (g4 >> 1) + 4 * (g4 & 1) + q4) * 128 + ((2 * (g4 & 1) + (q4 >> 1)) * 32) + (lane & 3) * 8;
#pragma unroll
          for (int n = 0; n < 4; ++n) wa[n] = lds0 + half * 8192 + (a0 ^ (n << 5)) + P3_SOFF(0); }
        f32x4 ac[2][2][4];
#pragma unroll
        for (int i = 0; i < 4; ++i)
#pragma unroll
            for (int j = 0; j < 4; ++j) ac[i >> 1][i & 1][j] = f32x4{};
        s16x4 wl[2][4], wh[2][4];
#define P3_RW(buf, k_, n_) do { wl[buf][n_] = ldtr(wa[n_] + (k_) * 4096); wh[buf][n_] = ldtr(wa[n_] + (k_) * 4096 + 1024); } while (0)
#define P3_MMA(st, k_, n_, wf_) do { ac[(st) >> 3][0][n_] = MFMA16(wf_, of[(st) % 7][2 * (k_)], ac[(st) >> 3][0][n_]); ac[(st) >> 3][1][n_] = MFMA16(wf_, of[(st) % 7][2 * (k_) + 1], ac[(st) >> 3][1][n_]); } while (0)
#define P3_K32(st, k_, X0, X1, X2, X3) do { \
            if ((k_) == 1 && (st) + 1 < 16) { _Pragma("unroll") for (int n_ = 0; n_ < 4; ++n_) { wa[n_] += (unsigned)(P3_SOFF((st) + 1) - P3_SOFF(st)); PIN(wa[n_]); } } \
            _Pragma("unroll") for (int n_ = 0; n_ < 4; ++n_) { \
                const bf16x8 wf_ = cat8(wl[k_][n_], wh[k_][n_]); \
                P3_MMA(st, k_, n_, wf_); \
                if ((k_) == 0) P3_RW(1, 1, n_); else if ((st) + 1 < 16) P3_RW(0, 0, n_); \
                if (n_ == 0) { X0; } if (n_ == 1) { X1; } if (n_ == 2) { X2; } if (n_ == 3) { X3; } \
                SBAR(); } } while (0)
#define P3_OW(st, NW) asm volatile("s_waitcnt vmcnt(" #NW ")" : "+v"(of[(st) % 7][0]), "+v"(of[(st) % 7][1]), "+v"(of[(st) % 7][2]), "+v"(of[(st) % 7][3])::"memory")
#define P3_STAGE(st, NW, BAR, nx) do { \
            P3_OW(st, NW); if (BAR) LBAR(); \
            P3_K32(st, 0, NOP_, NOP_, NOP_, NOP_); \
            if ((nx) < 16) { P3_K32(st, 1, P3_GO(nx, 0), P3_GO(nx, 1), P3_GO(nx, 2), P3_GO(nx, 3)); } \
            else { P3_K32(st, 1, NOP_, NOP_, NOP_, NOP_); } \
        } while (0)
#pragma unroll
        for (int n_ = 0; n_ < 4; ++n_) P3_RW(0, 0, n_);
        unsigned pv;
        const unsigned* fOAq = fO + (rt3 >> 1) * 32;
        P3_OW(0, 8);
        P3_K32(0, 0, P3_GO(3, 0), P3_GO(3, 1), P3_GO(3, 2), P3_GO(3, 3)); P3_K32(0, 1, P3_GO(4, 0), P3_GO(4, 1), P3_GO(4, 2), P3_GO(4, 3));
        P3_OW(1, 12);
        P3_K32(1, 0, P3_GO(5, 0), P3_GO(5, 1), P3_GO(5, 2), P3_GO(5, 3)); P3_K32(1, 1, P3_GO(6, 0), P3_GO(6, 1), P3_GO(6, 2), P3_GO(6, 3));
        P3_STAGE(2, 16, 0, 7);
        P3_OW(3, 16); LBAR();
        P3_K32(3, 0, POLL_ISSUE(pv, fOAq, 32), P3_GW(10, 0), P3_GW(10, 1), P3_GW(11, 0)); P3_K32(3, 1, P3_GW(11, 1), P3_GW(12, 0), P3_GW(12, 1), NOP_);
        asm volatile("s_waitcnt vmcnt(6)" : "+v"(pv), "+v"(of[4][0]), "+v"(of[4][1]), "+v"(of[4][2]), "+v"(of[4][3])::"memory");
        if (!__all(pv == token)) poll_spin(fOAq, 32, token, lane);
        P3_K32(4, 0, P3_GG(), P3_GO(8, 0); P3_GO(8, 1), P3_GO(8, 2); P3_GO(8, 3), P3_GO(9, 0); P3_GO(9, 1)); P3_K32(4, 1, P3_GO(9, 2); P3_GO(9, 3), P3_GO(10, 0); P3_GO(10, 1), P3_GO(10, 2), P3_GO(10, 3));
        P3_STAGE(5, 29, 0, 11);
        P3_OW(6, 29); LBAR();
        P3_K32(6, 0, P3_GW(13, 0), P3_GW(13, 1), P3_GW(14, 0), P3_GW(14, 1)); P3_K32(6, 1, P3_GW(15, 0); P3_GW(15, 1); P3_GO(12, 0), P3_GO(12, 1), P3_GO(12, 2), P3_GO(12, 3));
        P3_STAGE(7, 35, 0, 13); P3_STAGE(8, 26, 0, 14);
        P3_STAGE(9, 26, 1, 15); P3_STAGE(10, 26, 0, 16); P3_STAGE(11, 22, 0, 16);
        P3_STAGE(12, 12, 1, 16); P3_STAGE(13, 8, 0, 16); P3_STAGE(14, 4, 0, 16); P3_STAGE(15, 0, 0, 16);
        asm volatile("" : "+v"(gq[0]), "+v"(gq[1]));
        f32x4 r2[2][4];
#pragma unroll
        for (int n = 0; n < 4; ++n) { r2[0][n] = ac[1][0][n] * gq[0] + ac[0][0][n] * (1.0f - gq[0]); r2[1][n] = ac[1][1][n] * gq[1] + ac[0][1][n] * (1.0f - gq[1]); }
        LAS f32x4* mine = (LAS f32x4*)(uintptr_t)(lds0 + w * 4096 + l16);
        LAS f32x4* theirs = (LAS f32x4*)(uintptr_t)(lds0 + (w ^ 4) * 4096 + l16);
#pragma unroll
        for (int q = 0; q < 2; ++q) { mine[(q * 2) * 64] = half ? r2[q][0] : r2[q][2]; mine[(q * 2 + 1) * 64] = half ? r2[q][1] : r2[q][3]; }
        LBAR();
#pragma unroll
        for (int q = 0; q < 2; ++q)
#pragma unroll
            for (int e = 0; e < 2; ++e) {
                const f32x4 r = (half ? r2[q][2 + e] : r2[q][e]) + theirs[(q * 2 + e) * 64];
                *(LAS u32x2*)(uintptr_t)(lds0 + 32768 + (p3 * 32 + q * 16 + (lane & 15)) * 144 + half * 64 + e * 32 + g4 * 8) = u32x2{cvtpk(r[0], r[1]), cvtpk(r[2], r[3])};
            }
        LBAR();
#pragma unroll
        for (int i = 0; i < 2; ++i) {
            const int row = w * 16 + i * 8 + (lane >> 3);
            const u32x4 v = *(const LAS u32x4*)(uintptr_t)(lds0 + 32768 + row * 144 + (lane & 7) * 16);
            char* op = (char*)out + (size_t)(rt3 * 128) * 2048 + ct * 128;
            *(u32x4*)(op + row * 2048 + (lane & 7) * 16) = v;
        }
#undef P3_GO
#undef P3_GO4
#undef P3_MMA
#undef P3_GG
#undef P3_RW
#undef P3_K32
#undef P3_OW
#undef P3_STAGE
    }
#undef P3_GW
#undef P3_SOFF
    if (bid == 0 && w == 0) {
        const unsigned long long t0 = __builtin_amdgcn_s_memrealtime();
        for (;;) {
            bool ok = true;
#pragma unroll
            for (int i = 0; i < 4; ++i) ok = ok && (__hip_atomic_load((GAS unsigned*)(fO + i * 64 + lane), __ATOMIC_RELAXED, __HIP_MEMORY_SCOPE_AGENT) == token);
            if (__all(ok)) break;
            if (__builtin_amdgcn_s_memrealtime() - t0 > 400000ull) break;
            __builtin_amdgcn_s_sleep(1);
        }
        if (lane == 0) flag_set(flags, token);
    }
}

extern "C" void kernel_launch(void* const* d_in, const int* in_sizes, int n_in, void* d_out, int out_size, void* d_ws, size_t ws_size, hipStream_t stream) {
    (void)in_sizes; (void)n_in; (void)out_size; (void)ws_size;
    const u16* x = (const u16*)d_in[0]; const u16* wqkvA = (const u16*)d_in[1]; const u16* woA = (const u16*)d_in[2];
    const u16* wqkvB = (const u16*)d_in[4]; const u16* woB = (const u16*)d_in[5]; const u16* gw = (const u16*)d_in[6];
    (void)hipFuncSetAttribute((const void*)k_fused, hipFuncAttributeMaxDynamicSharedMemorySize, LDS_BYTES);
    k_fused<<<256, 512, LDS_BYTES, stream>>>(x, wqkvA, wqkvB, woA, woB, gw, (char*)d_ws, (u16*)d_out);
}
```
